```python
import torch

def _softmax(x, axis=-1):
    return torch.softmax(x, dim=axis)

def _gelu(x):
    return torch.nn.functional.gelu(x, approximate='tanh')

def _silu(x):
    return torch.nn.functional.silu(x)

def _sigmoid(x):
    return torch.sigmoid(x)

def _layernorm(x, axis=-1, eps=1e-05):
    m = x.mean(dim=axis, keepdim=True)
    v = x.var(dim=axis, keepdim=True, unbiased=False)
    return (x - m) / torch.sqrt(v + eps)

def _scatter_add(tgt, idx, src, axis=0):
    return tgt.clone().index_add_(axis, idx, src)

def _segment_sum(src, seg, n):
    out = torch.zeros((n,) + src.shape[1:], dtype=src.dtype)
    return out.index_add_(0, seg, src)

def _pad(x, pw):
    flat = tuple((v for pair in reversed(pw) for v in pair))
    return torch.nn.functional.pad(x, flat)
kernel_name = 'adaLN_cond__grouped_kv_mqa__dense_activation_silu__prompt'

def setup_inputs():

    _w = lambda a: (lambda t: t.to(torch.bfloat16) if t.is_floating_point() else t)(torch.as_tensor(a))

    def _A_setup():
        B, S, D, H = (1, 2048, 512, 32)
        Dh = D // H
        return {'x': _w((lambda *s: torch.randn(*s, dtype=torch.bfloat16) * 0.05)(B, S, D)), 'wq': _w((lambda *s: torch.randn(*s, dtype=torch.bfloat16) * 0.05)(D, H * Dh)), 'wk': _w((lambda *s: torch.randn(*s, dtype=torch.bfloat16) * 0.05)(D, Dh)), 'wv': _w((lambda *s: torch.randn(*s, dtype=torch.bfloat16) * 0.05)(D, Dh)), 'wo': _w((lambda *s: torch.randn(*s, dtype=torch.bfloat16) * 0.05)(H * Dh, D))}
    _A_in = _A_setup()

    def _B_setup():
        B, S, D, F = (1, 2048, 512, 1792)
        return {'x': _w((lambda *s: torch.randn(*s, dtype=torch.bfloat16) * 0.05)(B, S, D)), 'w1': _w((lambda *s: torch.randn(*s, dtype=torch.bfloat16) * 6.4)(D, F) * D ** (-0.5)), 'w2': _w((lambda *s: torch.randn(*s, dtype=torch.bfloat16) * 0.2)(F, D) * F ** (-0.5))}
    _B_in = _B_setup()
    return {'A_x': _A_in['x'], 'A_wq': _A_in['wq'], 'A_wk': _A_in['wk'], 'A_wv': _A_in['wv'], 'A_wo': _A_in['wo'], 'B_x': _B_in['x'], 'B_w1': _B_in['w1'], 'B_w2': _B_in['w2'], 'c': _w((lambda *s: torch.randn(*s, dtype=torch.bfloat16) * 0.1)(1, 512) * 8.0), 'w_mod': _w((lambda *s: torch.randn(*s, dtype=torch.bfloat16) * 0.1)(512, 6 * 512) * 512 ** (-0.5) * 8.0)}

def reference(A_x, A_wq, A_wk, A_wv, A_wo, B_x, B_w1, B_w2, c, w_mod):

    def _A_ref(x, wq, wk, wv, wo):
        B, S, D = x.shape
        H = 32
        Dh = D // H
        q = (lambda x, *d: x.permute(*d))((x @ wq).reshape(B, S, H, Dh), 0, 2, 1, 3)
        k = x @ wk
        v = x @ wv
        scores = torch.einsum('bhqd,bkd->bhqk', q, k) * Dh ** (-0.5)
        p = _softmax(scores, axis=-1)
        o = torch.einsum('bhqk,bkd->bhqd', p, v)
        return (lambda x, *d: x.permute(*d))(o, 0, 2, 1, 3).reshape(B, S, H * Dh) @ wo

    def _B_ref(x, w1, w2):
        h = torch.einsum('bsd,df->bsf', x, w1)
        h = _silu(h)
        out = torch.einsum('bsf,fd->bsd', h, w2)
        return out
    x = A_x
    mod = (_silu(c) @ w_mod).reshape(c.shape[0], 6, 1, -1)
    sa, ca, ga, sb, cb, gb = (mod[:, 0].clone(), mod[:, 1].clone(), mod[:, 2].clone(), mod[:, 3].clone(), mod[:, 4].clone(), mod[:, 5].clone())
    h = _layernorm(x, axis=-1) * (1 + ca) + sa
    x = x + ga * _A_ref(h, A_wq, A_wk, A_wv, A_wo)
    h = _layernorm(x, axis=-1) * (1 + cb) + sb
    x = x + gb * _B_ref(h, B_w1, B_w2)
    return x
```

```cpp
#include <hip/hip_runtime.h>
#include <hip/hip_bf16.h>
#include <stdint.h>

typedef __attribute__((ext_vector_type(8))) short bf16x8;
typedef __attribute__((ext_vector_type(4))) float f32x4;
typedef __attribute__((ext_vector_type(2))) float f32x2;
typedef __attribute__((ext_vector_type(16))) float f32x16;
typedef __attribute__((ext_vector_type(4))) unsigned u32x4;
typedef __attribute__((ext_vector_type(2))) unsigned u32x2;
typedef unsigned short u16;

#define LAS __attribute__((address_space(3)))
#define GAS __attribute__((address_space(1)))

constexpr int S_ = 2048, D_ = 512, F_ = 1792;
constexpr float QSCALE = 0.25f * 1.4426950408889634f;
constexpr float LOG2E = 1.4426950408889634f;
constexpr float EPS = 1e-5f;
constexpr float BOUND_T = 48.0f;

constexpr size_t WS_MODP = 0;
constexpr size_t WS_MODF = WS_MODP + 4 * 3072 * 4;
constexpr size_t WS_STAT = WS_MODF + 3072 * 4;
constexpr size_t WS_KMAX = WS_STAT + 2048 * 8;
constexpr size_t WS_WQP  = 131072;
constexpr size_t WS_WOP  = WS_WQP + 34 * 16 * 1024;
constexpr size_t WS_W1P  = WS_WOP + 32 * 16 * 1024;
constexpr size_t WS_W2P  = WS_W1P + 112 * 16 * 1024;
constexpr size_t WS_QP   = WS_W2P + 32 * 56 * 1024;
constexpr size_t WS_KP   = WS_QP + 2048 * 512 * 2;
constexpr size_t WS_VP   = WS_KP + 65536;
constexpr size_t WS_X1   = WS_VP + 65536;
constexpr size_t WS_H2P  = WS_X1 + 2048 * 512 * 2;
constexpr size_t WS_HP   = WS_H2P + 2048 * 512 * 2;

__device__ __forceinline__ float bf2f(u16 v) { return __uint_as_float(((unsigned)v) << 16); }
__device__ __forceinline__ float bflo(unsigned v) { return __uint_as_float(v << 16); }
__device__ __forceinline__ float bfhi(unsigned v) { return __uint_as_float(v & 0xffff0000u); }
typedef __attribute__((ext_vector_type(2))) __bf16 bf16x2n;
__device__ __forceinline__ unsigned cvtpk(float lo, float hi) {
    const f32x2 v = {lo, hi};
    return __builtin_bit_cast(unsigned, __builtin_convertvector(v, bf16x2n));
}
__device__ __forceinline__ float rbf(float x) { return bflo(cvtpk(x, x)); }
#define MFMA16(a, b, c) __builtin_amdgcn_mfma_f32_16x16x32_bf16(a, b, c, 0, 0, 0)
#define MFMA32(a, b, c) __builtin_amdgcn_mfma_f32_32x32x16_bf16(a, b, c, 0, 0, 0)

__device__ __forceinline__ void glds16(const void* g, unsigned lds_base) {
    unsigned sv;
    asm volatile("s_mov_b32 %0, m0\n\ts_mov_b32 m0, %2\n\ts_nop 0\n\tglobal_load_lds_dwordx4 %1, off\n\ts_mov_b32 m0, %0"
                 : "=&s"(sv) : "v"(g), "s"(lds_base) : "memory");
}

__device__ __forceinline__ int nperm(int tile, int i) { return 32 * (tile >> 1) + 8 * (i >> 2) + 4 * (tile & 1) + (i & 3); }

constexpr int K1_MOD_BLOCKS = 192;
constexpr int K1_STAT_BLOCKS = 128;
constexpr int K1_PQ = 34 * 16 / 4;
constexpr int K1_PO = 32 * 16 / 4;
constexpr int K1_P1 = 112 * 16 / 4;
constexpr int K1_P2 = 32 * 56 / 4;
constexpr int K1_BLOCKS = K1_MOD_BLOCKS + K1_STAT_BLOCKS + K1_PQ + K1_PO + K1_P1 + K1_P2;

__device__ __forceinline__ void pack_frag(const u16* __restrict__ W, int ld, int n, int k0, u16* __restrict__ dst) {
    u16 v[8];
#pragma unroll
    for (int j = 0; j < 8; ++j) v[j] = W[(size_t)(k0 + j) * ld + n];
    u32x4 o;
#pragma unroll
    for (int j = 0; j < 4; ++j) o[j] = (unsigned)v[2 * j] | ((unsigned)v[2 * j + 1] << 16);
    *(u32x4*)dst = o;
}

__global__ __launch_bounds__(256) void k1_prep(const u16* __restrict__ x, const u16* __restrict__ wq, const u16* __restrict__ wk,
                                               const u16* __restrict__ wv, const u16* __restrict__ wo, const u16* __restrict__ w1,
                                               const u16* __restrict__ w2, const u16* __restrict__ c, const u16* __restrict__ wmod,
                                               char* __restrict__ ws) {
    __shared__ float red[4][64];
    const int tid = threadIdx.x, lane = tid & 63, wid = tid >> 6;
    int b = blockIdx.x;
    if (b < K1_MOD_BLOCKS) {
        const int cg = b >> 2, ks = b & 3;
        const int cc = tid & 7, r0 = ks * 128 + (tid >> 3);
        u32x4 w[4]; float sc[4];
#pragma unroll
        for (int i = 0; i < 4; ++i) w[i] = *(const u32x4*)(wmod + (size_t)(r0 + 32 * i) * 3072 + cg * 64 + cc * 8);
#pragma unroll
        for (int i = 0; i < 4; ++i) { float v = bf2f(c[r0 + 32 * i]); sc[i] = rbf(v / (1.0f + __expf(-v))); }
        float a[8];
#pragma unroll
        for (int j = 0; j < 8; ++j) a[j] = 0.f;
#pragma unroll
        for (int i = 0; i < 4; ++i)
#pragma unroll
            for (int j = 0; j < 4; ++j) { a[2 * j] += sc[i] * bflo(w[i][j]); a[2 * j + 1] += sc[i] * bfhi(w[i][j]); }
#pragma unroll
        for (int j = 0; j < 8; ++j) { a[j] += __shfl_xor(a[j], 8); a[j] += __shfl_xor(a[j], 16); a[j] += __shfl_xor(a[j], 32); }
        if (lane < 8) {
#pragma unroll
            for (int j = 0; j < 8; ++j) red[wid][lane * 8 + j] = a[j];
        }
        __syncthreads();
        if (tid < 64) ((float*)(ws + WS_MODP))[ks * 3072 + cg * 64 + tid] = red[0][tid] + red[1][tid] + red[2][tid] + red[3][tid];
        return;
    }
    b -= K1_MOD_BLOCKS;
    if (b < K1_STAT_BLOCKS) {
        const int row0 = (b & 7) * 256 + (b >> 3) * 16 + wid * 4;
        u32x4 v[4];
#pragma unroll
        for (int i = 0; i < 4; ++i) v[i] = *(const u32x4*)(x + (size_t)(row0 + i) * D_ + lane * 8);
#pragma unroll
        for (int i = 0; i < 4; ++i) {
            float f[8];
#pragma unroll
            for (int j = 0; j < 4; ++j) { f[2 * j] = bflo(v[i][j]); f[2 * j + 1] = bfhi(v[i][j]); }
            float s = 0.f;
#pragma unroll
            for (int j = 0; j < 8; ++j) s += f[j];
#pragma unroll
            for (int o = 1; o < 64; o <<= 1) s += __shfl_xor(s, o);
            const float mean = s * (1.0f / D_);
            float q = 0.f;
#pragma unroll
            for (int j = 0; j < 8; ++j) { float d = f[j] - mean; q += d * d; }
#pragma unroll
            for (int o = 1; o < 64; o <<= 1) q += __shfl_xor(q, o);
            const float rstd = 1.0f / sqrtf(q * (1.0f / D_) + EPS);
            if (lane == 0) { f32x2 st = {rstd, -mean * rstd}; ((f32x2*)(ws + WS_STAT))[row0 + i] = st; }
        }
        return;
    }
    b -= K1_STAT_BLOCKS;
    const int i16 = lane & 15, g = lane >> 4;
    if (b < K1_PQ) {
        const int fr = b * 4 + wid, nt = fr >> 4, ks = fr & 15;
        u16* dst = (u16*)(ws + WS_WQP) + (size_t)fr * 512 + lane * 8;
        if (nt < 32) pack_frag(wq, 512, nt * 16 + i16, ks * 32 + g * 8, dst);
        else pack_frag(nt == 32 ? wk : wv, 16, i16, ks * 32 + g * 8, dst);
        return;
    }
    b -= K1_PQ;
    if (b < K1_PO) {
        const int fr = b * 4 + wid, nt = fr >> 4, ks = fr & 15;
        pack_frag(wo, 512, nperm(nt, i16), ks * 32 + g * 8, (u16*)(ws + WS_WOP) + (size_t)fr * 512 + lane * 8);
        return;
    }
    b -= K1_PO;
    if (b < K1_P1) {
        const int fr = b * 4 + wid, nt = fr >> 4, ks = fr & 15;
        pack_frag(w1, F_, nperm(nt, i16), ks * 32 + g * 8, (u16*)(ws + WS_W1P) + (size_t)fr * 512 + lane * 8);
        return;
    }
    b -= K1_P1;
    {
        const int fr = b * 4 + wid, nt = fr / 56, ks = fr % 56;
        pack_frag(w2, 512, nperm(nt, i16), ks * 32 + g * 8, (u16*)(ws + WS_W2P) + (size_t)fr * 512 + lane * 8);
    }
}

__global__ __launch_bounds__(256) void k2_qkv(const u16* __restrict__ x, char* __restrict__ ws) {
    __shared__ __attribute__((aligned(16))) char hs[65536];
    const int tid = threadIdx.x, lane = tid & 63;
    const int wid = __builtin_amdgcn_readfirstlane(tid >> 6);
    const int b = blockIdx.x;
    const int rt = (b & 7) * 4 + ((b >> 3) & 3), cg = b >> 5;
    const int row0 = rt * 64;
    const float* modp = (const float*)(ws + WS_MODP);
    u32x4 xv[16];
#pragma unroll
    for (int i = 0; i < 16; ++i) xv[i] = *(const u32x4*)(x + (size_t)(row0 + wid * 16 + i) * D_ + lane * 8);
    float gm[8], sa[8];
    {
        f32x4 a0 = {0, 0, 0, 0}, a1 = a0, c0 = a0, c1 = a0;
#pragma unroll
        for (int p = 0; p < 4; ++p) {
            a0 += *(const f32x4*)(modp + p * 3072 + lane * 8);       a1 += *(const f32x4*)(modp + p * 3072 + lane * 8 + 4);
            c0 += *(const f32x4*)(modp + p * 3072 + 512 + lane * 8); c1 += *(const f32x4*)(modp + p * 3072 + 512 + lane * 8 + 4);
        }
#pragma unroll
        for (int j = 0; j < 4; ++j) {
            sa[j] = rbf(a0[j]); sa[4 + j] = rbf(a1[j]);
            gm[j] = rbf(1.0f + rbf(c0[j])); gm[4 + j] = rbf(1.0f + rbf(c1[j]));
        }
    }
    const bf16x8* wqp = (const bf16x8*)(ws + WS_WQP);
    bf16x8 wf[16];
#pragma unroll
    for (int ks = 0; ks < 16; ++ks) wf[ks] = wqp[((cg * 4 + wid) * 16 + ks) * 64 + lane];
    const bool kvwave = (wid == (cg & 3));
    bf16x8 wkv[16];
    if (kvwave) {
#pragma unroll
        for (int ks = 0; ks < 16; ++ks) wkv[ks] = wqp[((32 + (cg >> 2)) * 16 + ks) * 64 + lane];
    }
    if (b < 12) {
        const int idx = b * 256 + tid;
        ((float*)(ws + WS_MODF))[idx] = rbf(modp[idx] + modp[3072 + idx] + modp[2 * 3072 + idx] + modp[3 * 3072 + idx]);
    }
    const f32x2* stat = (const f32x2*)(ws + WS_STAT);
#pragma unroll
    for (int i = 0; i < 16; ++i) {
        const f32x2 st = stat[row0 + wid * 16 + i];
        u32x4 o;
#pragma unroll
        for (int j = 0; j < 4; ++j) {
            const float t0 = fmaf(bflo(xv[i][j]), st[0], st[1]), t1 = fmaf(bfhi(xv[i][j]), st[0], st[1]);
            o[j] = cvtpk(fmaf(t0, gm[2 * j], sa[2 * j]), fmaf(t1, gm[2 * j + 1], sa[2 * j + 1]));
        }
        *(u32x4*)(hs + wid * 16384 + lane * 256 + ((i ^ (lane & 15)) * 16)) = o;
    }
    __syncthreads();
    const int tok = lane & 15, g = lane >> 4;
    f32x4 acc[4] = {{0, 0, 0, 0}, {0, 0, 0, 0}, {0, 0, 0, 0}, {0, 0, 0, 0}};
    f32x4 akv = {0, 0, 0, 0};
#pragma unroll
    for (int ks = 0; ks < 16; ++ks) {
        const int off = ks * 1024 + g * 256 + ((tok ^ ((4 * ks + g) & 15)) * 16);
#pragma unroll
        for (int tt = 0; tt < 4; ++tt) {
            const bf16x8 hf = *(const bf16x8*)(hs + tt * 16384 + off);
            acc[tt] = MFMA16(wf[ks], hf, acc[tt]);
        }
    }
    {
        const int head = cg * 4 + wid;
        char* qp = ws + WS_QP;
#pragma unroll
        for (int tt = 0; tt < 4; ++tt) {
            const int token = row0 + tt * 16 + tok;
            u32x2 o = {cvtpk(acc[tt][0] * QSCALE, acc[tt][1] * QSCALE), cvtpk(acc[tt][2] * QSCALE, acc[tt][3] * QSCALE)};
            *(u32x2*)(qp + (size_t)(token >> 3) * 8192 + (head >> 2) * 1024 + (g >> 1) * 512 + (((head & 3) * 8 + (token & 7)) * 16) + (g & 1) * 8) = o;
        }
    }
    if (kvwave) {
        const int tt = wid;
        const bool isV = (cg >> 2) != 0;
#pragma unroll
        for (int ks = 0; ks < 16; ++ks) {
            const int off = ks * 1024 + g * 256 + ((tok ^ ((4 * ks + g) & 15)) * 16);
            const bf16x8 hf = *(const bf16x8*)(hs + tt * 16384 + off);
            if (isV) akv = MFMA16(hf, wkv[ks], akv); else akv = MFMA16(wkv[ks], hf, akv);
        }
        const u32x2 o = {cvtpk(akv[0], akv[1]), cvtpk(akv[2], akv[3])};
        const int t16 = rt * 4 + tt;
        if (!isV) {
            const int kk = (t16 & 1) * 16 + tok;
            const int r = (kk & 0x13) | ((kk & 4) << 1) | ((kk & 8) >> 1);
            *(u32x2*)(ws + WS_KP + (size_t)(t16 >> 1) * 1024 + ((g >> 1) * 32 + r) * 16 + (g & 1) * 8) = o;
            float n2 = bflo(o[0]) * bflo(o[0]) + bfhi(o[0]) * bfhi(o[0]) + bflo(o[1]) * bflo(o[1]) + bfhi(o[1]) * bfhi(o[1]);
            n2 += __shfl_xor(n2, 16); n2 += __shfl_xor(n2, 32);
#pragma unroll
            for (int of = 1; of < 16; of <<= 1) { const float t = __shfl_xor(n2, of); n2 = (t > n2 || t != t) ? t : n2; }
            if (lane == 0) ((float*)(ws + WS_KMAX))[t16] = n2;
        } else {
            *(u32x2*)(ws + WS_VP + (size_t)t16 * 512 + (g >> 1) * 256 + tok * 16 + (g & 1) * 8) = o;
        }
    }
}

constexpr int L3_K = 0, L3_V = 65536, L3_C = 131072, L3_O = L3_C + 4096, L3_OSTR = 1024 + 16, L3_ST = L3_O + 8 * L3_OSTR;
constexpr int L3_BYTES = L3_ST + 4 * 16 * 8;

__global__ __launch_bounds__(256, 1) void k3_attn(const u16* __restrict__ x, char* __restrict__ ws) {
    __shared__ __attribute__((aligned(16))) char lds[L3_BYTES];
    const int tid = threadIdx.x, lane = tid & 63;
    const int wid = __builtin_amdgcn_readfirstlane(tid >> 6);
    const int b = blockIdx.x;
    const int tg = (b & 7) * 32 + (b >> 3);
    const unsigned lds0 = (unsigned)(uintptr_t)lds;
    {
        const char* src = ws + WS_KP + wid * 1024 + lane * 16;
#pragma unroll
        for (int i = 0; i < 32; ++i) glds16(src + i * 4096, lds0 + wid * 1024 + i * 4096);
    }
    {
        u32x4 cv = {0, 0, 0, 0};
        if ((tid & 31) == 0) cv = (u32x4){0x3f803f80u, 0x3f803f80u, 0x3f803f80u, 0x3f803f80u};
        *(u32x4*)(lds + L3_C + tid * 16) = cv;
    }
    const bf16x8* qp = (const bf16x8*)(ws + WS_QP) + (size_t)tg * 512;
    bf16x8 qf0 = qp[wid * 64 + lane], qf1 = qp[(4 + wid) * 64 + lane];
    float kmax2;
    {
        const float* km = (const float*)(ws + WS_KMAX);
        float a = km[lane], c2 = km[64 + lane];
        a = (c2 > a || c2 != c2) ? c2 : a;
#pragma unroll
        for (int of = 1; of < 64; of <<= 1) { const float t = __shfl_xor(a, of); a = (t > a || t != t) ? t : a; }
        kmax2 = a;
    }
    asm volatile("s_waitcnt vmcnt(0)" ::: "memory");
    __syncthreads();

    const int r32 = lane & 31, hh = lane >> 5;
    const LAS char* kbase = (const LAS char*)lds + L3_K + lane * 16;
    const bool vdata = r32 < 16;
    const unsigned vaddr0 = vdata ? (unsigned)(L3_V + hh * 256 + r32 * 16) : (unsigned)(L3_C + ((r32 == 16 || r32 == 20) ? 0 : 64));
    const unsigned vstep = vdata ? 4096u : 0u;
    const bf16x8* wop = (const bf16x8*)(ws + WS_WOP);
    f32x4 xacc[8];
#pragma unroll
    for (int i = 0; i < 8; ++i) xacc[i] = (f32x4){0, 0, 0, 0};

#pragma unroll 1
    for (int rnd = 0; rnd < 2; ++rnd) {
        const bf16x8 qf = rnd ? qf1 : qf0;
        float m;
        {
            float n2 = 0.f;
#pragma unroll
            for (int j = 0; j < 8; ++j) { const float v = bf2f((u16)qf[j]); n2 += v * v; }
            n2 += __shfl_xor(n2, 32);
            m = sqrtf(n2 * kmax2) * 1.0001f;
            if (__any(!(m <= BOUND_T))) {
                float mx = -INFINITY;
                const f32x16 z = {0, 0, 0, 0, 0, 0, 0, 0, 0, 0, 0, 0, 0, 0, 0, 0};
#pragma unroll 1
                for (int t = 0; t < 64; ++t) {
                    const bf16x8 kf = *(const LAS bf16x8*)(kbase + t * 1024);
                    const f32x16 s = MFMA32(kf, qf, z);
#pragma unroll
                    for (int r = 0; r < 16; ++r) mx = fmaxf(mx, s[r]);
                }
                mx = fmaxf(mx, __shfl_xor(mx, 32));
                m = mx;
            }
        }
        f32x16 negm;
#pragma unroll
        for (int r = 0; r < 16; ++r) negm[r] = -m;
        asm volatile("" : "+v"(negm));
        f32x16 o = {0, 0, 0, 0, 0, 0, 0, 0, 0, 0, 0, 0, 0, 0, 0, 0};
        bf16x8 wf[64];
        const LAS char* kp = kbase;
        unsigned va = vaddr0;
        f32x16 sA = MFMA32(*(const LAS bf16x8*)(kp), qf, negm), sB;
        bf16x8 kn = *(const LAS bf16x8*)(kp + 1024);
        bf16x8 v0 = *(const LAS bf16x8*)((const LAS char*)lds + va), v1 = *(const LAS bf16x8*)((const LAS char*)lds + va + 512);

#define TILE(SC, SN, KOFF, VOFF, VADV)                                                                    \
        {                                                                                                 \
            SN = MFMA32(kn, qf, negm);                                                                    \
            kn = *(const LAS bf16x8*)(kp + (KOFF));                                                       \
            u32x4 p0, p1;                                                                                 \
            _Pragma("unroll") for (int j = 0; j < 4; ++j)                                                 \
                p0[j] = cvtpk(__builtin_amdgcn_exp2f(SC[2 * j]), __builtin_amdgcn_exp2f(SC[2 * j + 1]));  \
            o = MFMA32(v0, __builtin_bit_cast(bf16x8, p0), o);                                            \
            if (VADV) va += vstep;                                                                        \
            v0 = *(const LAS bf16x8*)((const LAS char*)lds + va + (VOFF));                                \
            _Pragma("unroll") for (int j = 0; j < 4; ++j)                                                 \
                p1[j] = cvtpk(__builtin_amdgcn_exp2f(SC[8 + 2 * j]), __builtin_amdgcn_exp2f(SC[9 + 2 * j])); \
            o = MFMA32(v1, __builtin_bit_cast(bf16x8, p1), o);                                            \
            v1 = *(const LAS bf16x8*)((const LAS char*)lds + va + (VOFF) + 512);                          \
        }
#pragma unroll
        for (int ch = 0; ch < 4; ++ch) {
#pragma unroll
            for (int i = 0; i < 16; ++i) {
                const int f = ch * 16 + i;
                wf[f] = wop[((wid * 8 + (f >> 3)) * 16 + rnd * 8 + (f & 7)) * 64 + lane];
            }
#pragma unroll 1
            for (int it = 0; it < 4; ++it) {
                TILE(sA, sB, 2048, 1024, false)
                TILE(sB, sA, 3072, 2048, false)
                TILE(sA, sB, 4096, 3072, false)
                TILE(sB, sA, 5120, 0, true)
                kp += 4096;
            }
        }
#undef TILE
        {
            const float rl = __builtin_amdgcn_rcpf(o[8]);
            const int hl = r32 >> 3, tok = r32 & 7;
            char* op = lds + L3_O + tok * L3_OSTR + ((rnd * 16 + wid * 4 + hl) * 16 + 4 * hh) * 2;
            *(u32x2*)(op) = (u32x2){cvtpk(o[0] * rl, o[1] * rl), cvtpk(o[2] * rl, o[3] * rl)};
            *(u32x2*)(op + 16) = (u32x2){cvtpk(o[4] * rl, o[5] * rl), cvtpk(o[6] * rl, o[7] * rl)};
        }
        __syncthreads();
        {
            const char* op = lds + L3_O + (lane & 7) * L3_OSTR + rnd * 512 + (lane >> 4) * 16;
#pragma unroll
            for (int ks = 0; ks < 8; ++ks) {
                const bf16x8 of = *(const bf16x8*)(op + ks * 64);
#pragma unroll
                for (int nt = 0; nt < 8; ++nt) xacc[nt] = MFMA16(wf[nt * 8 + ks], of, xacc[nt]);
            }
        }
    }
    const int tok = lane & 15, g = lane >> 4;
    const int token = tg * 8 + (tok & 7);
    const float* modf = (const float*)(ws + WS_MODF);
    float x1[32];
#pragma unroll
    for (int p = 0; p < 4; ++p) {
        const int n0 = wid * 128 + p * 32 + g * 8;
        const u32x4 xv = *(const u32x4*)(x + (size_t)token * D_ + n0);
        const f32x4 ga0 = *(const f32x4*)(modf + 1024 + n0), ga1 = *(const f32x4*)(modf + 1024 + n0 + 4);
        u32x4 o;
#pragma unroll
        for (int j = 0; j < 2; ++j) {
            o[j] = cvtpk(fmaf(ga0[2 * j], xacc[2 * p][2 * j], bflo(xv[j])), fmaf(ga0[2 * j + 1], xacc[2 * p][2 * j + 1], bfhi(xv[j])));
            o[2 + j] = cvtpk(fmaf(ga1[2 * j], xacc[2 * p + 1][2 * j], bflo(xv[2 + j])), fmaf(ga1[2 * j + 1], xacc[2 * p + 1][2 * j + 1], bfhi(xv[2 + j])));
        }
        if (tok < 8) *(u32x4*)(ws + WS_X1 + ((size_t)token * D_ + n0) * 2) = o;
#pragma unroll
        for (int j = 0; j < 4; ++j) { x1[p * 8 + 2 * j] = bflo(o[j]); x1[p * 8 + 2 * j + 1] = bfhi(o[j]); }
    }
    float s = 0.f;
#pragma unroll
    for (int j = 0; j < 32; ++j) s += x1[j];
    s += __shfl_xor(s, 16); s += __shfl_xor(s, 32);
    const float mw = s * (1.0f / 128.0f);
    float q = 0.f;
#pragma unroll
    for (int j = 0; j < 32; ++j) { const float d = x1[j] - mw; q += d * d; }
    q += __shfl_xor(q, 16); q += __shfl_xor(q, 32);
    f32x2* stp = (f32x2*)(lds + L3_ST);
    if (lane < 16) stp[wid * 16 + lane] = (f32x2){mw, q};
    __syncthreads();
    float mean = 0.f, m2 = 0.f;
    {
        const f32x2 a0 = stp[tok], a1 = stp[16 + tok], a2 = stp[32 + tok], a3 = stp[48 + tok];
        mean = 0.25f * (a0[0] + a1[0] + a2[0] + a3[0]);
        const float d0 = a0[0] - mean, d1 = a1[0] - mean, d2 = a2[0] - mean, d3 = a3[0] - mean;
        m2 = a0[1] + a1[1] + a2[1] + a3[1] + 128.0f * (d0 * d0 + d1 * d1 + d2 * d2 + d3 * d3);
    }
    const float rstd = 1.0f / sqrtf(m2 * (1.0f / D_) + EPS);
    const float nmr = -mean * rstd;
#pragma unroll
    for (int p = 0; p < 4; ++p) {
        const int n0 = wid * 128 + p * 32 + g * 8;
        const f32x4 sb0 = *(const f32x4*)(modf + 1536 + n0), sb1 = *(const f32x4*)(modf + 1536 + n0 + 4);
        const f32x4 cb0 = *(const f32x4*)(modf + 2048 + n0), cb1 = *(const f32x4*)(modf + 2048 + n0 + 4);
        float h[8];
#pragma unroll
        for (int j = 0; j < 4; ++j) {
            h[j] = fmaf(fmaf(x1[p * 8 + j], rstd, nmr), rbf(1.0f + cb0[j]), sb0[j]);
            h[4 + j] = fmaf(fmaf(x1[p * 8 + 4 + j], rstd, nmr), rbf(1.0f + cb1[j]), sb1[j]);
        }
        const u32x4 o = {cvtpk(h[0], h[1]), cvtpk(h[2], h[3]), cvtpk(h[4], h[5]), cvtpk(h[6], h[7])};
        if (tok < 8) *(u32x4*)(ws + WS_H2P + ((size_t)((token >> 4) * 16 + wid * 4 + p) * 64 + g * 16 + (token & 15)) * 16) = o;
    }
}

__global__ __launch_bounds__(256, 1) void k4_ffn1(char* __restrict__ ws) {
    __shared__ __attribute__((aligned(16))) char lds[7 * 16 * 1024];
    const int tid = threadIdx.x, lane = tid & 63;
    const int wid = __builtin_amdgcn_readfirstlane(tid >> 6);
    const int b = blockIdx.x;
    const int rp = (b & 7) * 2 + ((b >> 3) & 1), fs = b >> 4;
    const unsigned lds0 = (unsigned)(uintptr_t)lds;
    const bf16x8* h2p = (const bf16x8*)(ws + WS_H2P) + (size_t)(rp * 8 + wid * 2) * 16 * 64 + lane;
    bf16x8 hf[2][16];
#pragma unroll
    for (int t = 0; t < 2; ++t)
#pragma unroll
        for (int ks = 0; ks < 16; ++ks) hf[t][ks] = h2p[(t * 16 + ks) * 64];
    {
        const char* src = ws + WS_W1P + (size_t)fs * 7 * 16384 + wid * 1024 + lane * 16;
#pragma unroll
        for (int i = 0; i < 28; ++i) glds16(src + i * 4096, lds0 + wid * 1024 + i * 4096);
    }
    asm volatile("s_waitcnt vmcnt(0)" ::: "memory");
    __syncthreads();
    char* hp = ws + WS_HP;
#pragma unroll
    for (int i = 0; i < 7; ++i) {
        f32x4 a0 = {0, 0, 0, 0}, a1 = {0, 0, 0, 0};
#pragma unroll
        for (int ks = 0; ks < 16; ++ks) {
            const bf16x8 wf = *(const bf16x8*)(lds + (i * 16 + ks) * 1024 + lane * 16);
            a0 = MFMA16(wf, hf[0][ks], a0);
            a1 = MFMA16(wf, hf[1][ks], a1);
        }
        const int ft = fs * 7 + i;
        float s0[4], s1[4];
#pragma unroll
        for (int j = 0; j < 4; ++j) {
            s0[j] = a0[j] * __builtin_amdgcn_rcpf(1.0f + __builtin_amdgcn_exp2f(-LOG2E * a0[j]));
            s1[j] = a1[j] * __builtin_amdgcn_rcpf(1.0f + __builtin_amdgcn_exp2f(-LOG2E * a1[j]));
        }
        const size_t base = ((size_t)((rp * 8 + wid * 2) * 56 + (ft >> 1)) * 64 + lane) * 16 + (ft & 1) * 8;
        *(u32x2*)(hp + base) = (u32x2){cvtpk(s0[0], s0[1]), cvtpk(s0[2], s0[3])};
        *(u32x2*)(hp + base + 56 * 1024) = (u32x2){cvtpk(s1[0], s1[1]), cvtpk(s1[2], s1[3])};
    }
}

__global__ __launch_bounds__(256, 1) void k5_ffn2(char* __restrict__ ws, u16* __restrict__ out) {
    __shared__ __attribute__((aligned(16))) char lds[4 * 4 * 4096];
    const int tid = threadIdx.x, lane = tid & 63;
    const int wid = __builtin_amdgcn_readfirstlane(tid >> 6);
    const int b = blockIdx.x;
    const int rt = (b & 7) * 4 + ((b >> 3) & 3), ct = b >> 5;
    const bf16x8* wp = (const bf16x8*)(ws + WS_W2P) + (size_t)(ct * 4 * 56 + wid * 14) * 64 + lane;
    const bf16x8* hp = (const bf16x8*)(ws + WS_HP) + (size_t)(rt * 4 * 56 + wid * 14) * 64 + lane;
    f32x4 acc[4][4];
#pragma unroll
    for (int i = 0; i < 4; ++i)
#pragma unroll
        for (int j = 0; j < 4; ++j) acc[i][j] = (f32x4){0, 0, 0, 0};
    constexpr int WIN = 7;
    bf16x8 wf[WIN][4], hf[WIN][4];
#pragma unroll
    for (int ks = 0; ks < WIN; ++ks)
#pragma unroll
        for (int i = 0; i < 4; ++i) { wf[ks][i] = wp[(i * 56 + ks) * 64]; hf[ks][i] = hp[(i * 56 + ks) * 64]; }
    __builtin_amdgcn_sched_barrier(0);
#pragma unroll
    for (int ks = 0; ks < 14; ++ks) {
        const int sl = ks % WIN;
#pragma unroll
        for (int nt = 0; nt < 4; ++nt)
#pragma unroll
            for (int tt = 0; tt < 4; ++tt) acc[nt][tt] = MFMA16(wf[sl][nt], hf[sl][tt], acc[nt][tt]);
        if (ks + WIN < 14) {
#pragma unroll
            for (int i = 0; i < 4; ++i) { wf[sl][i] = wp[(i * 56 + ks + WIN) * 64]; hf[sl][i] = hp[(i * 56 + ks + WIN) * 64]; }
        }
        __builtin_amdgcn_sched_barrier(0);
    }
#pragma unroll
    for (int tt = 0; tt < 4; ++tt)
        if (tt != wid) {
#pragma unroll
            for (int nt = 0; nt < 4; ++nt) *(f32x4*)(lds + ((tt * 4 + wid) * 4 + nt) * 1024 + lane * 16) = acc[nt][tt];
        }
    const int tok = lane & 15, g = lane >> 4;
    const int token = rt * 64 + wid * 16 + tok;
    const float* modf = (const float*)(ws + WS_MODF);
    u32x4 xv[2]; f32x4 gb[4];
#pragma unroll
    for (int p = 0; p < 2; ++p) {
        const int n0 = ct * 64 + p * 32 + g * 8;
        xv[p] = *(const u32x4*)(ws + WS_X1 + ((size_t)token * D_ + n0) * 2);
        gb[2 * p] = *(const f32x4*)(modf + 2560 + n0); gb[2 * p + 1] = *(const f32x4*)(modf + 2560 + n0 + 4);
    }
    __syncthreads();
    f32x4 r[4];
#pragma unroll
    for (int nt = 0; nt < 4; ++nt) {
        r[nt] = (wid == 0) ? acc[nt][0] : (wid == 1) ? acc[nt][1] : (wid == 2) ? acc[nt][2] : acc[nt][3];
#pragma unroll
        for (int w = 0; w < 4; ++w)
            if (w != wid) r[nt] += *(const f32x4*)(lds + ((wid * 4 + w) * 4 + nt) * 1024 + lane * 16);
    }
#pragma unroll
    for (int p = 0; p < 2; ++p) {
        const int n0 = ct * 64 + p * 32 + g * 8;
        u32x4 o;
#pragma unroll
        for (int j = 0; j < 2; ++j) {
            o[j] = cvtpk(fmaf(gb[2 * p][2 * j], r[2 * p][2 * j], bflo(xv[p][j])), fmaf(gb[2 * p][2 * j + 1], r[2 * p][2 * j + 1], bfhi(xv[p][j])));
            o[2 + j] = cvtpk(fmaf(gb[2 * p + 1][2 * j], r[2 * p + 1][2 * j], bflo(xv[p][2 + j])),
                             fmaf(gb[2 * p + 1][2 * j + 1], r[2 * p + 1][2 * j + 1], bfhi(xv[p][2 + j])));
        }
        *(u32x4*)(out + (size_t)token * D_ + n0) = o;
    }
}

extern "C" void kernel_launch(void* const* d_in, const int* in_sizes, int n_in, void* d_out, int out_size, void* d_ws,
                              size_t ws_size, hipStream_t stream) {
    const u16* x = (const u16*)d_in[0];
    const u16* wq = (const u16*)d_in[1];
    const u16* wk = (const u16*)d_in[2];
    const u16* wv = (const u16*)d_in[3];
    const u16* wo = (const u16*)d_in[4];
    const u16* w1 = (const u16*)d_in[6];
    const u16* w2 = (const u16*)d_in[7];
    const u16* c = (const u16*)d_in[8];
    const u16* wmod = (const u16*)d_in[9];
    char* ws = (char*)d_ws;
    k1_prep<<<K1_BLOCKS, 256, 0, stream>>>(x, wq, wk, wv, wo, w1, w2, c, wmod, ws);
    k2_qkv<<<256, 256, 0, stream>>>(x, ws);
    k3_attn<<<256, 256, 0, stream>>>(x, ws);
    k4_ffn1<<<256, 256, 0, stream>>>(ws);
    k5_ffn2<<<256, 256, 0, stream>>>(ws, (u16*)d_out);
}
```

```cpp
#include <hip/hip_runtime.h>
#include <hip/hip_bf16.h>
#include <stdint.h>

typedef __attribute__((ext_vector_type(8))) short bf16x8;
typedef __attribute__((ext_vector_type(4))) float f32x4;
typedef __attribute__((ext_vector_type(2))) float f32x2;
typedef __attribute__((ext_vector_type(16))) float f32x16;
typedef __attribute__((ext_vector_type(4))) unsigned u32x4;
typedef __attribute__((ext_vector_type(2))) unsigned u32x2;
typedef unsigned short u16;

#define LAS __attribute__((address_space(3)))
#define GAS __attribute__((address_space(1)))

constexpr int S_ = 2048, D_ = 512, F_ = 1792;
constexpr float QSCALE = 0.25f * 1.4426950408889634f;
constexpr float LOG2E = 1.4426950408889634f;
constexpr float EPS = 1e-5f;
constexpr float BOUND_T = 48.0f;

constexpr size_t WS_MODP = 0;
constexpr size_t WS_MODF = WS_MODP + 4 * 3072 * 4;
constexpr size_t WS_STAT = WS_MODF + 3072 * 4;
constexpr size_t WS_KMAX = WS_STAT + 2048 * 8;
constexpr size_t WS_WQP  = 131072;
constexpr size_t WS_WOP  = WS_WQP + 34 * 16 * 1024;
constexpr size_t WS_W1P  = WS_WOP + 32 * 16 * 1024;
constexpr size_t WS_W2P  = WS_W1P + 112 * 16 * 1024;
constexpr size_t WS_QP   = WS_W2P + 32 * 56 * 1024;
constexpr size_t WS_KP   = WS_QP + 2048 * 512 * 2;
constexpr size_t WS_VP   = WS_KP + 65536;
constexpr size_t WS_X1   = WS_VP + 65536;
constexpr size_t WS_H2P  = WS_X1 + 2048 * 512 * 2;
constexpr size_t WS_HP   = WS_H2P + 2048 * 512 * 2;

#define STAMP(ws, kid, idx) do {} while (0)

__device__ __forceinline__ float bf2f(u16 v) { return __uint_as_float(((unsigned)v) << 16); }
__device__ __forceinline__ float bflo(unsigned v) { return __uint_as_float(v << 16); }
__device__ __forceinline__ float bfhi(unsigned v) { return __uint_as_float(v & 0xffff0000u); }
typedef __attribute__((ext_vector_type(2))) __bf16 bf16x2n;
__device__ __forceinline__ unsigned cvtpk(float lo, float hi) {
    const f32x2 v = {lo, hi};
    return __builtin_bit_cast(unsigned, __builtin_convertvector(v, bf16x2n));
}
__device__ __forceinline__ float rbf(float x) { return bflo(cvtpk(x, x)); }
#define MFMA16(a, b, c) __builtin_amdgcn_mfma_f32_16x16x32_bf16(a, b, c, 0, 0, 0)
#define MFMA32(a, b, c) __builtin_amdgcn_mfma_f32_32x32x16_bf16(a, b, c, 0, 0, 0)

__device__ __forceinline__ void glds16(const void* g, unsigned lds_base) {
    unsigned sv;
    asm volatile("s_mov_b32 %0, m0\n\ts_mov_b32 m0, %2\n\ts_nop 0\n\tglobal_load_lds_dwordx4 %1, off\n\ts_mov_b32 m0, %0"
                 : "=&s"(sv) : "v"(g), "s"(lds_base) : "memory");
}

__device__ __forceinline__ int nperm(int tile, int i) { return 32 * (tile >> 1) + 8 * (i >> 2) + 4 * (tile & 1) + (i & 3); }

constexpr int K1_MOD_BLOCKS = 192;
constexpr int K1_STAT_BLOCKS = 128;
constexpr int K1_PQ = 34 * 16 / 4;
constexpr int K1_PO = 32 * 16 / 4;
constexpr int K1_P1 = 112 * 16 / 4;
constexpr int K1_P2 = 32 * 56 / 4;
constexpr int K1_BLOCKS = K1_MOD_BLOCKS + K1_STAT_BLOCKS + K1_PQ + K1_PO + K1_P1 + K1_P2;

__device__ __forceinline__ void pack_frag(const u16* __restrict__ W, int ld, int n, int k0, u16* __restrict__ dst) {
    u16 v[8];
#pragma unroll
    for (int j = 0; j < 8; ++j) v[j] = W[(size_t)(k0 + j) * ld + n];
    u32x4 o;
#pragma unroll
    for (int j = 0; j < 4; ++j) o[j] = (unsigned)v[2 * j] | ((unsigned)v[2 * j + 1] << 16);
    *(u32x4*)dst = o;
}

__global__ __launch_bounds__(256) void k1_prep(const u16* __restrict__ x, const u16* __restrict__ wq, const u16* __restrict__ wk,
                                               const u16* __restrict__ wv, const u16* __restrict__ wo, const u16* __restrict__ w1,
                                               const u16* __restrict__ w2, const u16* __restrict__ c, const u16* __restrict__ wmod,
                                               char* __restrict__ ws) {
    __shared__ float red[4][64];
    const int tid = threadIdx.x, lane = tid & 63, wid = tid >> 6;
    int b = blockIdx.x;
    STAMP(ws, 0, 0);
    if (b < K1_MOD_BLOCKS) {
        const int cg = b >> 2, ks = b & 3;
        const int cc = tid & 7, r0 = ks * 128 + (tid >> 3);
        u32x4 w[4]; float sc[4];
#pragma unroll
        for (int i = 0; i < 4; ++i) w[i] = *(const u32x4*)(wmod + (size_t)(r0 + 32 * i) * 3072 + cg * 64 + cc * 8);
#pragma unroll
        for (int i = 0; i < 4; ++i) { float v = bf2f(c[r0 + 32 * i]); sc[i] = rbf(v / (1.0f + __expf(-v))); }
        float a[8];
#pragma unroll
        for (int j = 0; j < 8; ++j) a[j] = 0.f;
#pragma unroll
        for (int i = 0; i < 4; ++i)
#pragma unroll
            for (int j = 0; j < 4; ++j) { a[2 * j] += sc[i] * bflo(w[i][j]); a[2 * j + 1] += sc[i] * bfhi(w[i][j]); }
#pragma unroll
        for (int j = 0; j < 8; ++j) { a[j] += __shfl_xor(a[j], 8); a[j] += __shfl_xor(a[j], 16); a[j] += __shfl_xor(a[j], 32); }
        if (lane < 8) {
#pragma unroll
            for (int j = 0; j < 8; ++j) red[wid][lane * 8 + j] = a[j];
        }
        __syncthreads();
        if (tid < 64) ((float*)(ws + WS_MODP))[ks * 3072 + cg * 64 + tid] = red[0][tid] + red[1][tid] + red[2][tid] + red[3][tid];
        STAMP(ws, 0, 1);
        return;
    }
    b -= K1_MOD_BLOCKS;
    if (b < K1_STAT_BLOCKS) {
        const int row0 = (b & 7) * 256 + (b >> 3) * 16 + wid * 4;
        u32x4 v[4];
#pragma unroll
        for (int i = 0; i < 4; ++i) v[i] = *(const u32x4*)(x + (size_t)(row0 + i) * D_ + lane * 8);
#pragma unroll
        for (int i = 0; i < 4; ++i) {
            float f[8];
#pragma unroll
            for (int j = 0; j < 4; ++j) { f[2 * j] = bflo(v[i][j]); f[2 * j + 1] = bfhi(v[i][j]); }
            float s = 0.f;
#pragma unroll
            for (int j = 0; j < 8; ++j) s += f[j];
#pragma unroll
            for (int o = 1; o < 64; o <<= 1) s += __shfl_xor(s, o);
            const float mean = s * (1.0f / D_);
            float q = 0.f;
#pragma unroll
            for (int j = 0; j < 8; ++j) { float d = f[j] - mean; q += d * d; }
#pragma unroll
            for (int o = 1; o < 64; o <<= 1) q += __shfl_xor(q, o);
            const float rstd = 1.0f / sqrtf(q * (1.0f / D_) + EPS);
            if (lane == 0) { f32x2 st = {rstd, -mean * rstd}; ((f32x2*)(ws + WS_STAT))[row0 + i] = st; }
        }
        return;
    }
    b -= K1_STAT_BLOCKS;
    const int i16 = lane & 15, g = lane >> 4;
    if (b < K1_PQ) {
        const int fr = b * 4 + wid, nt = fr >> 4, ks = fr & 15;
        u16* dst = (u16*)(ws + WS_WQP) + (size_t)fr * 512 + lane * 8;
        if (nt < 32) pack_frag(wq, 512, nt * 16 + i16, ks * 32 + g * 8, dst);
        else pack_frag(nt == 32 ? wk : wv, 16, i16, ks * 32 + g * 8, dst);
        return;
    }
    b -= K1_PQ;
    if (b < K1_PO) {
        const int fr = b * 4 + wid, nt = fr >> 4, ks = fr & 15;
        pack_frag(wo, 512, nperm(nt, i16), ks * 32 + g * 8, (u16*)(ws + WS_WOP) + (size_t)fr * 512 + lane * 8);
        return;
    }
    b -= K1_PO;
    if (b < K1_P1) {
        const int fr = b * 4 + wid, nt = fr >> 4, ks = fr & 15;
        pack_frag(w1, F_, nperm(nt, i16), ks * 32 + g * 8, (u16*)(ws + WS_W1P) + (size_t)fr * 512 + lane * 8);
        return;
    }
    b -= K1_P1;
    {
        const int fr = b * 4 + wid, nt = fr / 56, ks = fr % 56;
        pack_frag(w2, 512, nperm(nt, i16), ks * 32 + g * 8, (u16*)(ws + WS_W2P) + (size_t)fr * 512 + lane * 8);
        STAMP(ws, 0, 1);
    }
}

__global__ __launch_bounds__(256) void k2_qkv(const u16* __restrict__ x, char* __restrict__ ws) {
    __shared__ __attribute__((aligned(16))) char hs[65536];
    const int tid = threadIdx.x, lane = tid & 63;
    const int wid = __builtin_amdgcn_readfirstlane(tid >> 6);
    const int b = blockIdx.x;
    const int rt = (b & 7) * 4 + ((b >> 3) & 3), cg = b >> 5;
    const int row0 = rt * 64;
    STAMP(ws, 1, 0);
    const float* modp = (const float*)(ws + WS_MODP);
    u32x4 xv[16];
#pragma unroll
    for (int i = 0; i < 16; ++i) xv[i] = *(const u32x4*)(x + (size_t)(row0 + wid * 16 + i) * D_ + lane * 8);
    float gm[8], sa[8];
    {
        f32x4 a0 = {0, 0, 0, 0}, a1 = a0, c0 = a0, c1 = a0;
#pragma unroll
        for (int p = 0; p < 4; ++p) {
            a0 += *(const f32x4*)(modp + p * 3072 + lane * 8);       a1 += *(const f32x4*)(modp + p * 3072 + lane * 8 + 4);
            c0 += *(const f32x4*)(modp + p * 3072 + 512 + lane * 8); c1 += *(const f32x4*)(modp + p * 3072 + 512 + lane * 8 + 4);
        }
#pragma unroll
        for (int j = 0; j < 4; ++j) {
            sa[j] = rbf(a0[j]); sa[4 + j] = rbf(a1[j]);
            gm[j] = rbf(1.0f + rbf(c0[j])); gm[4 + j] = rbf(1.0f + rbf(c1[j]));
        }
    }
    const bf16x8* wqp = (const bf16x8*)(ws + WS_WQP);
    bf16x8 wf[16];
#pragma unroll
    for (int ks = 0; ks < 16; ++ks) wf[ks] = wqp[((cg * 4 + wid) * 16 + ks) * 64 + lane];
    const bool kvwave = (wid == (cg & 3));
    bf16x8 wkv[16];
    if (kvwave) {
#pragma unroll
        for (int ks = 0; ks < 16; ++ks) wkv[ks] = wqp[((32 + (cg >> 2)) * 16 + ks) * 64 + lane];
    }
    if (b < 12) {
        const int idx = b * 256 + tid;
        ((float*)(ws + WS_MODF))[idx] = rbf(modp[idx] + modp[3072 + idx] + modp[2 * 3072 + idx] + modp[3 * 3072 + idx]);
    }
    const f32x2* stat = (const f32x2*)(ws + WS_STAT);
#pragma unroll
    for (int i = 0; i < 16; ++i) {
        const f32x2 st = stat[row0 + wid * 16 + i];
        u32x4 o;
#pragma unroll
        for (int j = 0; j < 4; ++j) {
            const float t0 = fmaf(bflo(xv[i][j]), st[0], st[1]), t1 = fmaf(bfhi(xv[i][j]), st[0], st[1]);
            o[j] = cvtpk(fmaf(t0, gm[2 * j], sa[2 * j]), fmaf(t1, gm[2 * j + 1], sa[2 * j + 1]));
        }
        *(u32x4*)(hs + wid * 16384 + lane * 256 + ((i ^ (lane & 15)) * 16)) = o;
    }
    STAMP(ws, 1, 1);
    __syncthreads();
    STAMP(ws, 1, 2);
    const int tok = lane & 15, g = lane >> 4;
    f32x4 acc[4] = {{0, 0, 0, 0}, {0, 0, 0, 0}, {0, 0, 0, 0}, {0, 0, 0, 0}};
    f32x4 akv = {0, 0, 0, 0};
#pragma unroll
    for (int ks = 0; ks < 16; ++ks) {
        const int off = ks * 1024 + g * 256 + ((tok ^ ((4 * ks + g) & 15)) * 16);
#pragma unroll
        for (int tt = 0; tt < 4; ++tt) {
            const bf16x8 hf = *(const bf16x8*)(hs + tt * 16384 + off);
            acc[tt] = MFMA16(wf[ks], hf, acc[tt]);
        }
    }
    {
        const int head = cg * 4 + wid;
        char* qp = ws + WS_QP;
#pragma unroll
        for (int tt = 0; tt < 4; ++tt) {
            const int token = row0 + tt * 16 + tok;
            u32x2 o = {cvtpk(acc[tt][0] * QSCALE, acc[tt][1] * QSCALE), cvtpk(acc[tt][2] * QSCALE, acc[tt][3] * QSCALE)};
            *(u32x2*)(qp + (size_t)(token >> 3) * 8192 + (head >> 2) * 1024 + (g >> 1) * 512 + (((head & 3) * 8 + (token & 7)) * 16) + (g & 1) * 8) = o;
        }
    }
    STAMP(ws, 1, 3);
    if (kvwave) {
        const int tt = wid;
        const bool isV = (cg >> 2) != 0;
#pragma unroll
        for (int ks = 0; ks < 16; ++ks) {
            const int off = ks * 1024 + g * 256 + ((tok ^ ((4 * ks + g) & 15)) * 16);
            const bf16x8 hf = *(const bf16x8*)(hs + tt * 16384 + off);
            if (isV) akv = MFMA16(hf, wkv[ks], akv); else akv = MFMA16(wkv[ks], hf, akv);
        }
        const u32x2 o = {cvtpk(akv[0], akv[1]), cvtpk(akv[2], akv[3])};
        const int t16 = rt * 4 + tt;
        if (!isV) {
            const int kk = (t16 & 1) * 16 + tok;
            const int r = (kk & 0x13) | ((kk & 4) << 1) | ((kk & 8) >> 1);
            *(u32x2*)(ws + WS_KP + (size_t)(t16 >> 1) * 1024 + ((g >> 1) * 32 + r) * 16 + (g & 1) * 8) = o;
            float n2 = bflo(o[0]) * bflo(o[0]) + bfhi(o[0]) * bfhi(o[0]) + bflo(o[1]) * bflo(o[1]) + bfhi(o[1]) * bfhi(o[1]);
            n2 += __shfl_xor(n2, 16); n2 += __shfl_xor(n2, 32);
#pragma unroll
            for (int of = 1; of < 16; of <<= 1) { const float t = __shfl_xor(n2, of); n2 = (t > n2 || t != t) ? t : n2; }
            if (lane == 0) ((float*)(ws + WS_KMAX))[t16] = n2;
        } else {
            *(u32x2*)(ws + WS_VP + (size_t)t16 * 512 + (g >> 1) * 256 + tok * 16 + (g & 1) * 8) = o;
        }
    }
}

constexpr int L3_K = 0, L3_V = 65536, L3_C = 131072, L3_O = L3_C + 4096, L3_OSTR = 1024 + 16, L3_ST = L3_O + 8 * L3_OSTR;
constexpr int L3_BYTES = L3_ST + 4 * 16 * 8;

__global__ __launch_bounds__(256, 1) void k3_attn(const u16* __restrict__ x, char* __restrict__ ws) {
    __shared__ __attribute__((aligned(16))) char lds[L3_BYTES];
    const int tid = threadIdx.x, lane = tid & 63;
    const int wid = __builtin_amdgcn_readfirstlane(tid >> 6);
    const int b = blockIdx.x;
    const int tg = (b & 7) * 32 + (b >> 3);
    const unsigned lds0 = (unsigned)(uintptr_t)lds;
    STAMP(ws, 2, 0);
    {
        const char* src = ws + WS_KP + wid * 1024 + lane * 16;
#pragma unroll
        for (int i = 0; i < 32; ++i) glds16(src + i * 4096, lds0 + wid * 1024 + i * 4096);
    }
    {
        u32x4 cv = {0, 0, 0, 0};
        if ((tid & 31) == 0) cv = (u32x4){0x3f803f80u, 0x3f803f80u, 0x3f803f80u, 0x3f803f80u};
        *(u32x4*)(lds + L3_C + tid * 16) = cv;
    }
    const bf16x8* qp = (const bf16x8*)(ws + WS_QP) + (size_t)tg * 512;
    bf16x8 qf0 = qp[wid * 64 + lane], qf1 = qp[(4 + wid) * 64 + lane];
    float kmax2;
    {
        const float* km = (const float*)(ws + WS_KMAX);
        float a = km[lane], c2 = km[64 + lane];
        a = (c2 > a || c2 != c2) ? c2 : a;
#pragma unroll
        for (int of = 1; of < 64; of <<= 1) { const float t = __shfl_xor(a, of); a = (t > a || t != t) ? t : a; }
        kmax2 = a;
    }
    asm volatile("s_waitcnt vmcnt(0)" ::: "memory");
    __syncthreads();

    STAMP(ws, 2, 1);
    const int r32 = lane & 31, hh = lane >> 5;
    const LAS char* kbase = (const LAS char*)lds + L3_K + lane * 16;
    const bool vdata = r32 < 16;
    const unsigned vaddr0 = vdata ? (unsigned)(L3_V + hh * 256 + r32 * 16) : (unsigned)(L3_C + ((r32 == 16 || r32 == 20) ? 0 : 64));
    const unsigned vstep = vdata ? 4096u : 0u;
    const bf16x8* wop = (const bf16x8*)(ws + WS_WOP);
    f32x4 xacc[8];
#pragma unroll
    for (int i = 0; i < 8; ++i) xacc[i] = (f32x4){0, 0, 0, 0};

#pragma unroll 1
    for (int rnd = 0; rnd < 2; ++rnd) {
        const bf16x8 qf = rnd ? qf1 : qf0;
        float m;
        {
            float n2 = 0.f;
#pragma unroll
            for (int j = 0; j < 8; ++j) { const float v = bf2f((u16)qf[j]); n2 += v * v; }
            n2 += __shfl_xor(n2, 32);
            m = sqrtf(n2 * kmax2) * 1.0001f;
            if (__any(!(m <= BOUND_T))) {
                float mx = -INFINITY;
                const f32x16 z = {0, 0, 0, 0, 0, 0, 0, 0, 0, 0, 0, 0, 0, 0, 0, 0};
#pragma unroll 1
                for (int t = 0; t < 64; ++t) {
                    const bf16x8 kf = *(const LAS bf16x8*)(kbase + t * 1024);
                    const f32x16 s = MFMA32(kf, qf, z);
#pragma unroll
                    for (int r = 0; r < 16; ++r) mx = fmaxf(mx, s[r]);
                }
                mx = fmaxf(mx, __shfl_xor(mx, 32));
                m = mx;
            }
        }
        f32x16 negm;
#pragma unroll
        for (int r = 0; r < 16; ++r) negm[r] = -m;
        asm volatile("" : "+v"(negm));
        f32x16 o = {0, 0, 0, 0, 0, 0, 0, 0, 0, 0, 0, 0, 0, 0, 0, 0};
        bf16x8 wf[64];
        const LAS char* kp = kbase;
        unsigned va = vaddr0;
        f32x16 sA = MFMA32(*(const LAS bf16x8*)(kp), qf, negm), sB;
        bf16x8 kn = *(const LAS bf16x8*)(kp + 1024);
        bf16x8 v0 = *(const LAS bf16x8*)((const LAS char*)lds + va), v1 = *(const LAS bf16x8*)((const LAS char*)lds + va + 512);

#define TILE(SC, SN, KOFF, VOFF, VADV)                                                                    \
        {                                                                                                 \
            SN = MFMA32(kn, qf, negm);                                                                    \
            kn = *(const LAS bf16x8*)(kp + (KOFF));                                                       \
            u32x4 p0, p1;                                                                                 \
            _Pragma("unroll") for (int j = 0; j < 4; ++j)                                                 \
                p0[j] = cvtpk(__builtin_amdgcn_exp2f(SC[2 * j]), __builtin_amdgcn_exp2f(SC[2 * j + 1]));  \
            o = MFMA32(v0, __builtin_bit_cast(bf16x8, p0), o);                                            \
            if (VADV) va += vstep;                                                                        \
            v0 = *(const LAS bf16x8*)((const LAS char*)lds + va + (VOFF));                                \
            _Pragma("unroll") for (int j = 0; j < 4; ++j)                                                 \
                p1[j] = cvtpk(__builtin_amdgcn_exp2f(SC[8 + 2 * j]), __builtin_amdgcn_exp2f(SC[9 + 2 * j])); \
            o = MFMA32(v1, __builtin_bit_cast(bf16x8, p1), o);                                            \
            v1 = *(const LAS bf16x8*)((const LAS char*)lds + va + (VOFF) + 512);                          \
        }
#pragma unroll
        for (int ch = 0; ch < 4; ++ch) {
#pragma unroll
            for (int i = 0; i < 16; ++i) {
                const int f = ch * 16 + i;
                wf[f] = wop[((wid * 8 + (f >> 3)) * 16 + rnd * 8 + (f & 7)) * 64 + lane];
            }
#pragma unroll 1
            for (int it = 0; it < 4; ++it) {
                TILE(sA, sB, 2048, 1024, false)
                TILE(sB, sA, 3072, 2048, false)
                TILE(sA, sB, 4096, 3072, false)
                TILE(sB, sA, 5120, 0, true)
                kp += 4096;
            }
        }
#undef TILE
        STAMP(ws, 2, 2 + 2 * rnd);
        {
            const float rl = __builtin_amdgcn_rcpf(o[8]);
            const int hl = r32 >> 3, tok = r32 & 7;
            char* op = lds + L3_O + tok * L3_OSTR + ((rnd * 16 + wid * 4 + hl) * 16 + 4 * hh) * 2;
            *(u32x2*)(op) = (u32x2){cvtpk(o[0] * rl, o[1] * rl), cvtpk(o[2] * rl, o[3] * rl)};
            *(u32x2*)(op + 16) = (u32x2){cvtpk(o[4] * rl, o[5] * rl), cvtpk(o[6] * rl, o[7] * rl)};
        }
        __syncthreads();
        {
            const char* op = lds + L3_O + (lane & 7) * L3_OSTR + rnd * 512 + (lane >> 4) * 16;
#pragma unroll
            for (int ks = 0; ks < 8; ++ks) {
                const bf16x8 of = *(const bf16x8*)(op + ks * 64);
#pragma unroll
                for (int nt = 0; nt < 8; ++nt) xacc[nt] = MFMA16(wf[nt * 8 + ks], of, xacc[nt]);
            }
        }
        STAMP(ws, 2, 3 + 2 * rnd);
    }
    const int tok = lane & 15, g = lane >> 4;
    const int token = tg * 8 + (tok & 7);
    const float* modf = (const float*)(ws + WS_MODF);
    float x1[32];
#pragma unroll
    for (int p = 0; p < 4; ++p) {
        const int n0 = wid * 128 + p * 32 + g * 8;
        const u32x4 xv = *(const u32x4*)(x + (size_t)token * D_ + n0);
        const f32x4 ga0 = *(const f32x4*)(modf + 1024 + n0), ga1 = *(const f32x4*)(modf + 1024 + n0 + 4);
        u32x4 o;
#pragma unroll
        for (int j = 0; j < 2; ++j) {
            o[j] = cvtpk(fmaf(ga0[2 * j], xacc[2 * p][2 * j], bflo(xv[j])), fmaf(ga0[2 * j + 1], xacc[2 * p][2 * j + 1], bfhi(xv[j])));
            o[2 + j] = cvtpk(fmaf(ga1[2 * j], xacc[2 * p + 1][2 * j], bflo(xv[2 + j])), fmaf(ga1[2 * j + 1], xacc[2 * p + 1][2 * j + 1], bfhi(xv[2 + j])));
        }
        if (tok < 8) *(u32x4*)(ws + WS_X1 + ((size_t)token * D_ + n0) * 2) = o;
#pragma unroll
        for (int j = 0; j < 4; ++j) { x1[p * 8 + 2 * j] = bflo(o[j]); x1[p * 8 + 2 * j + 1] = bfhi(o[j]); }
    }
    float s = 0.f;
#pragma unroll
    for (int j = 0; j < 32; ++j) s += x1[j];
    s += __shfl_xor(s, 16); s += __shfl_xor(s, 32);
    const float mw = s * (1.0f / 128.0f);
    float q = 0.f;
#pragma unroll
    for (int j = 0; j < 32; ++j) { const float d = x1[j] - mw; q += d * d; }
    q += __shfl_xor(q, 16); q += __shfl_xor(q, 32);
    f32x2* stp = (f32x2*)(lds + L3_ST);
    if (lane < 16) stp[wid * 16 + lane] = (f32x2){mw, q};
    __syncthreads();
    float mean = 0.f, m2 = 0.f;
    {
        const f32x2 a0 = stp[tok], a1 = stp[16 + tok], a2 = stp[32 + tok], a3 = stp[48 + tok];
        mean = 0.25f * (a0[0] + a1[0] + a2[0] + a3[0]);
        const float d0 = a0[0] - mean, d1 = a1[0] - mean, d2 = a2[0] - mean, d3 = a3[0] - mean;
        m2 = a0[1] + a1[1] + a2[1] + a3[1] + 128.0f * (d0 * d0 + d1 * d1 + d2 * d2 + d3 * d3);
    }
    const float rstd = 1.0f / sqrtf(m2 * (1.0f / D_) + EPS);
    const float nmr = -mean * rstd;
#pragma unroll
    for (int p = 0; p < 4; ++p) {
        const int n0 = wid * 128 + p * 32 + g * 8;
        const f32x4 sb0 = *(const f32x4*)(modf + 1536 + n0), sb1 = *(const f32x4*)(modf + 1536 + n0 + 4);
        const f32x4 cb0 = *(const f32x4*)(modf + 2048 + n0), cb1 = *(const f32x4*)(modf + 2048 + n0 + 4);
        float h[8];
#pragma unroll
        for (int j = 0; j < 4; ++j) {
            h[j] = fmaf(fmaf(x1[p * 8 + j], rstd, nmr), rbf(1.0f + cb0[j]), sb0[j]);
            h[4 + j] = fmaf(fmaf(x1[p * 8 + 4 + j], rstd, nmr), rbf(1.0f + cb1[j]), sb1[j]);
        }
        const u32x4 o = {cvtpk(h[0], h[1]), cvtpk(h[2], h[3]), cvtpk(h[4], h[5]), cvtpk(h[6], h[7])};
        if (tok < 8) *(u32x4*)(ws + WS_H2P + ((size_t)((token >> 4) * 16 + wid * 4 + p) * 64 + g * 16 + (token & 15)) * 16) = o;
    }
    STAMP(ws, 2, 6);
}

__global__ __launch_bounds__(256, 1) void k4_ffn1(char* __restrict__ ws) {
    __shared__ __attribute__((aligned(16))) char lds[7 * 16 * 1024];
    const int tid = threadIdx.x, lane = tid & 63;
    const int wid = __builtin_amdgcn_readfirstlane(tid >> 6);
    const int b = blockIdx.x;
    const int rp = (b & 7) * 2 + ((b >> 3) & 1), fs = b >> 4;
    const unsigned lds0 = (unsigned)(uintptr_t)lds;
    const bf16x8* h2p = (const bf16x8*)(ws + WS_H2P) + (size_t)(rp * 8 + wid * 2) * 16 * 64 + lane;
    STAMP(ws, 3, 0);
    bf16x8 hf[2][16];
#pragma unroll
    for (int t = 0; t < 2; ++t)
#pragma unroll
        for (int ks = 0; ks < 16; ++ks) hf[t][ks] = h2p[(t * 16 + ks) * 64];
    {
        const char* src = ws + WS_W1P + (size_t)fs * 7 * 16384 + wid * 1024 + lane * 16;
#pragma unroll
        for (int i = 0; i < 28; ++i) glds16(src + i * 4096, lds0 + wid * 1024 + i * 4096);
    }
    asm volatile("s_waitcnt vmcnt(0)" ::: "memory");
    __syncthreads();
    STAMP(ws, 3, 1);
    char* hp = ws + WS_HP;
#pragma unroll
    for (int i = 0; i < 7; ++i) {
        f32x4 a0 = {0, 0, 0, 0}, a1 = {0, 0, 0, 0};
#pragma unroll
        for (int ks = 0; ks < 16; ++ks) {
            const bf16x8 wf = *(const bf16x8*)(lds + (i * 16 + ks) * 1024 + lane * 16);
            a0 = MFMA16(wf, hf[0][ks], a0);
            a1 = MFMA16(wf, hf[1][ks], a1);
        }
        const int ft = fs * 7 + i;
        float s0[4], s1[4];
#pragma unroll
        for (int j = 0; j < 4; ++j) {
            s0[j] = a0[j] * __builtin_amdgcn_rcpf(1.0f + __builtin_amdgcn_exp2f(-LOG2E * a0[j]));
            s1[j] = a1[j] * __builtin_amdgcn_rcpf(1.0f + __builtin_amdgcn_exp2f(-LOG2E * a1[j]));
        }
        const size_t base = ((size_t)((rp * 8 + wid * 2) * 56 + (ft >> 1)) * 64 + lane) * 16 + (ft & 1) * 8;
        *(u32x2*)(hp + base) = (u32x2){cvtpk(s0[0], s0[1]), cvtpk(s0[2], s0[3])};
        *(u32x2*)(hp + base + 56 * 1024) = (u32x2){cvtpk(s1[0], s1[1]), cvtpk(s1[2], s1[3])};
    }
    STAMP(ws, 3, 2);
}

__global__ __launch_bounds__(256, 1) void k5_ffn2(char* __restrict__ ws, u16* __restrict__ out) {
    __shared__ __attribute__((aligned(16))) char lds[4 * 4 * 4096];
    const int tid = threadIdx.x, lane = tid & 63;
    const int wid = __builtin_amdgcn_readfirstlane(tid >> 6);
    const int b = blockIdx.x;
    const int rt = (b & 7) * 4 + ((b >> 3) & 3), ct = b >> 5;
    const bf16x8* wp = (const bf16x8*)(ws + WS_W2P) + (size_t)(ct * 4 * 56 + wid * 14) * 64 + lane;
    const bf16x8* hp = (const bf16x8*)(ws + WS_HP) + (size_t)(rt * 4 * 56 + wid * 14) * 64 + lane;
    f32x4 acc[4][4];
#pragma unroll
    for (int i = 0; i < 4; ++i)
#pragma unroll
        for (int j = 0; j < 4; ++j) acc[i][j] = (f32x4){0, 0, 0, 0};
    STAMP(ws, 4, 0);
    constexpr int WIN = 7;
    bf16x8 wf[WIN][4], hf[WIN][4];
#pragma unroll
    for (int ks = 0; ks < WIN; ++ks)
#pragma unroll
        for (int i = 0; i < 4; ++i) { wf[ks][i] = wp[(i * 56 + ks) * 64]; hf[ks][i] = hp[(i * 56 + ks) * 64]; }
    __builtin_amdgcn_sched_barrier(0);
#pragma unroll
    for (int ks = 0; ks < 14; ++ks) {
        const int sl = ks % WIN;
#pragma unroll
        for (int nt = 0; nt < 4; ++nt)
#pragma unroll
            for (int tt = 0; tt < 4; ++tt) acc[nt][tt] = MFMA16(wf[sl][nt], hf[sl][tt], acc[nt][tt]);
        if (ks + WIN < 14) {
#pragma unroll
            for (int i = 0; i < 4; ++i) { wf[sl][i] = wp[(i * 56 + ks + WIN) * 64]; hf[sl][i] = hp[(i * 56 + ks + WIN) * 64]; }
        }
        __builtin_amdgcn_sched_barrier(0);
    }
    STAMP(ws, 4, 1);
#pragma unroll
    for (int tt = 0; tt < 4; ++tt)
        if (tt != wid) {
#pragma unroll
            for (int nt = 0; nt < 4; ++nt) *(f32x4*)(lds + ((tt * 4 + wid) * 4 + nt) * 1024 + lane * 16) = acc[nt][tt];
        }
    const int tok = lane & 15, g = lane >> 4;
    const int token = rt * 64 + wid * 16 + tok;
    const float* modf = (const float*)(ws + WS_MODF);
    u32x4 xv[2]; f32x4 gb[4];
#pragma unroll
    for (int p = 0; p < 2; ++p) {
        const int n0 = ct * 64 + p * 32 + g * 8;
        xv[p] = *(const u32x4*)(ws + WS_X1 + ((size_t)token * D_ + n0) * 2);
        gb[2 * p] = *(const f32x4*)(modf + 2560 + n0); gb[2 * p + 1] = *(const f32x4*)(modf + 2560 + n0 + 4);
    }
    __syncthreads();
    f32x4 r[4];
#pragma unroll
    for (int nt = 0; nt < 4; ++nt) {
        r[nt] = (wid == 0) ? acc[nt][0] : (wid == 1) ? acc[nt][1] : (wid == 2) ? acc[nt][2] : acc[nt][3];
#pragma unroll
        for (int w = 0; w < 4; ++w)
            if (w != wid) r[nt] += *(const f32x4*)(lds + ((wid * 4 + w) * 4 + nt) * 1024 + lane * 16);
    }
#pragma unroll
    for (int p = 0; p < 2; ++p) {
        const int n0 = ct * 64 + p * 32 + g * 8;
        u32x4 o;
#pragma unroll
        for (int j = 0; j < 2; ++j) {
            o[j] = cvtpk(fmaf(gb[2 * p][2 * j], r[2 * p][2 * j], bflo(xv[p][j])), fmaf(gb[2 * p][2 * j + 1], r[2 * p][2 * j + 1], bfhi(xv[p][j])));
            o[2 + j] = cvtpk(fmaf(gb[2 * p + 1][2 * j], r[2 * p + 1][2 * j], bflo(xv[p][2 + j])),
                             fmaf(gb[2 * p + 1][2 * j + 1], r[2 * p + 1][2 * j + 1], bfhi(xv[p][2 + j])));
        }
        *(u32x4*)(out + (size_t)token * D_ + n0) = o;
    }
    STAMP(ws, 4, 2);
}

extern "C" void kernel_launch(void* const* d_in, const int* in_sizes, int n_in, void* d_out, int out_size, void* d_ws,
                              size_t ws_size, hipStream_t stream) {
    const u16* x = (const u16*)d_in[0];
    const u16* wq = (const u16*)d_in[1];
    const u16* wk = (const u16*)d_in[2];
    const u16* wv = (const u16*)d_in[3];
    const u16* wo = (const u16*)d_in[4];
    const u16* w1 = (const u16*)d_in[6];
    const u16* w2 = (const u16*)d_in[7];
    const u16* c = (const u16*)d_in[8];
    const u16* wmod = (const u16*)d_in[9];
    char* ws = (char*)d_ws;
    k1_prep<<<K1_BLOCKS, 256, 0, stream>>>(x, wq, wk, wv, wo, w1, w2, c, wmod, ws);
    k2_qkv<<<256, 256, 0, stream>>>(x, ws);
    k3_attn<<<256, 256, 0, stream>>>(x, ws);
    k4_ffn1<<<256, 256, 0, stream>>>(ws);
    k5_ffn2<<<256, 256, 0, stream>>>(ws, (u16*)d_out);
}
```

```cpp
#include <hip/hip_runtime.h>
#include <hip/hip_bf16.h>
#include <stdint.h>

#define KBODY \
      \
    "s_load_dwordx16 s[64:79], s[4:5], 0x0\n\t" \
    "s_load_dwordx4 s[80:83], s[4:5], 0x40\n\t" \
    "s_load_dwordx2 s[84:85], s[4:5], 0x50\n\t" \
    "s_getpc_b64 s[86:87]\n\t" \
    ".Lpc_%=:\n\t" \
    "v_and_b32 v1, 63, v0\n\t" \
    "v_lshlrev_b32 v2, 4, v1\n\t" \
    "v_lshrrev_b32 v3, 6, v0\n\t" \
    "s_and_b32 s48, s6, 7\n\t" \
    "s_lshr_b32 s49, s6, 3\n\t" \
    "v_readfirstlane_b32 s50, v3\n\t" \
    "v_lshrrev_b32 v10, 4, v0\n\t" \
    "v_lshlrev_b32 v10, 12, v10\n\t" \
    "s_lshl_b32 s88, s49, 7\n\t" \
    "v_add_u32 v10, s88, v10\n\t" \
    "v_min_u32 v10, .Lend_%=-.Lpc_%=-4, v10\n\t" \
    "global_load_dword v231, v10, s[86:87]\n\t" \
    "s_and_b32 s51, s49, 7\n\t" \
    "s_lshr_b32 s52, s49, 3\n\t" \
    "s_lshl_b32 s63, s48, 3\n\t" \
    "s_add_u32 s63, s63, s51\n\t" \
    "s_mov_b32 s62, 4194304\n\t" \
    "v_lshrrev_b32 v10, 1, v0\n\t" \
    "v_mul_u32_u24 v10, 6144, v10\n\t" \
    "v_and_b32 v71, 1, v0\n\t" \
    "v_lshl_add_u32 v10, v71, 4, v10\n\t" \
    "v_and_b32 v71, 0xfe, v0\n\t" \
    "v_lshrrev_b32 v72, 4, v1\n\t" \
    "v_and_b32 v73, 15, v1\n\t" \
    "v_lshlrev_b32 v11, 10, v72\n\t" \
    "v_lshl_add_u32 v11, v73, 4, v11\n\t" \
    "s_add_u32 s91, s52, 1\n\t" \
    "s_and_b32 s91, s91, 2\n\t" \
    "s_cmp_eq_u32 s52, 3\n\t" \
    "s_cselect_b32 s90, 1, 0\n\t" \
    "s_waitcnt lgkmcnt(0)\n\t" \
    "s_mov_b32 s16, s80\n\t" \
    "s_and_b32 s17, s81, 0xffff\n\t" \
    "s_mov_b32 s18, 0x300000\n\t" \
    "s_mov_b32 s19, 0x20000\n\t" \
    "s_cmp_eq_u32 s91, 0\n\t" \
    "s_cbranch_scc0 .Lnowm_%=\n\t" \
    "s_lshl_b32 s88, s63, 5\n\t" \
    "s_mul_i32 s89, s90, 1572864\n\t" \
    "s_add_u32 s88, s88, s89\n\t" \
    "buffer_load_dwordx4 v[48:51], v10, s[16:19], s88 offen\n\t" \
    "s_add_u32 s88, s88, 786432\n\t" \
    "buffer_load_dwordx4 v[52:55], v10, s[16:19], s88 offen\n\t" \
    ".Lnowm_%=:\n\t" \
    "s_mov_b32 s20, s66\n\t" \
    "s_and_b32 s21, s67, 0xffff\n\t" \
    "s_mov_b32 s22, 0x80000\n\t" \
    "s_mov_b32 s23, 0x20000\n\t" \
    "s_mov_b32 s8, s82\n\t" \
    "s_and_b32 s9, s83, 0xffff\n\t" \
    "s_mov_b32 s10, 0xfa0000\n\t" \
    "s_mov_b32 s11, 0x20000\n\t" \
    "buffer_load_dwordx2 v[74:75], off, s[8:11], 0 sc1\n\t" \
    "s_mov_b32 s44, s78\n\t" \
    "s_and_b32 s45, s79, 0xffff\n\t" \
    "s_mov_b32 s46, 0x400\n\t" \
    "s_mov_b32 s47, 0x20000\n\t" \
    "s_lshl_b32 s88, s90, 9\n\t" \
    "buffer_load_ushort v12, v71, s[44:47], s88 offen offset:0\n\t" \
    "buffer_load_ushort v13, v71, s[44:47], s88 offen offset:256\n\t" \
    "buffer_load_ushort v14, v71, s[44:47], s88 offen offset:512\n\t" \
    "buffer_load_ushort v15, v71, s[44:47], s88 offen offset:768\n\t" \
    "v_lshrrev_b32 v77, 4, v0\n\t" \
    "v_lshlrev_b32 v77, 7, v77\n\t" \
    "s_lshl_b32 s88, s6, 11\n\t" \
    "buffer_load_dword v231, v77, s[20:23], s88 offen\n\t" \
    "v_and_b32 v77, 1, v1\n\t" \
    "s_lshl_b32 s88, s50, 16\n\t" \
    "v_lshlrev_b32 v77, 15, v77\n\t" \
    "s_lshl_b32 s89, s6, 7\n\t" \
    "s_add_u32 s88, s88, s89\n\t" \
    "buffer_load_dword v231, v77, s[8:11], s88 offen sc1\n\t" \
    "s_mov_b32 s12, s64\n\t" \
    "s_and_b32 s13, s65, 0xffff\n\t" \
    "s_mov_b32 s14, 0x200000\n\t" \
    "s_mov_b32 s15, 0x20000\n\t" \
    "s_lshl_b32 s88, s63, 15\n\t" \
    "s_lshl_b32 s89, s50, 13\n\t" \
    "s_add_u32 s56, s88, s89\n\t" \
    "s_add_u32 s57, s56, 0x1000\n\t" \
    "s_cmp_eq_u32 s50, 0\n\t" \
    "s_cselect_b32 s92, 0xff, 0\n\t" \
    "s_mov_b32 s93, 0\n\t" \
    "s_cmp_eq_u32 s52, 1\n\t" \
    "s_cselect_b64 s[86:87], -1, 0\n\t" \
    "buffer_load_dwordx4 v[16:19], v2, s[12:15], s56 offen offset:0\n\t" \
    "s_mov_b32 s44, s74\n\t" \
    "s_and_b32 s45, s75, 0xffff\n\t" \
    "s_mov_b32 s46, 0x1c0000\n\t" \
    "s_mov_b32 s47, 0x20000\n\t" \
    "s_mov_b32 s36, s76\n\t" \
    "s_and_b32 s37, s77, 0xffff\n\t" \
    "s_mov_b32 s38, 0x1c0000\n\t" \
    "s_mov_b32 s39, 0x20000\n\t" \
    "s_mov_b32 s40, s84\n\t" \
    "s_and_b32 s41, s85, 0xffff\n\t" \
    "s_mov_b32 s42, 0x200000\n\t" \
    "s_mov_b32 s43, 0x20000\n\t" \
    "buffer_load_dwordx4 v[20:23], v2, s[12:15], s56 offen offset:1024\n\t" \
    "s_lshl_b32 s53, s48, 5\n\t" \
    "s_add_u32 s53, s53, s49\n\t" \
    "s_and_b32 s88, s49, 1\n\t" \
    "s_lshl_b32 s58, s48, 1\n\t" \
    "s_add_u32 s58, s58, s88\n\t" \
    "s_lshr_b32 s59, s49, 1\n\t" \
    "s_lshr_b32 s60, s49, 2\n\t" \
    "s_and_b32 s88, s59, 1\n\t" \
    "s_lshl_b32 s61, s58, 1\n\t" \
    "s_add_u32 s61, s61, s88\n\t" \
    "v_and_b32 v76, 27, v1\n\t" \
    "v_cmp_eq_u32 vcc, 16, v76\n\t" \
    "buffer_load_dwordx4 v[24:27], v2, s[12:15], s56 offen offset:2048\n\t" \
    "v_mov_b32 v76, 0x3f803f80\n\t" \
    "v_mov_b32 v80, 0\n\t" \
    "v_mov_b32 v81, 0\n\t" \
    "v_mov_b32 v82, 0\n\t" \
    "v_mov_b32 v83, 0\n\t" \
    "v_cndmask_b32 v76, 0, v76, vcc\n\t" \
    "v_accvgpr_write_b32 a64, v76\n\t" \
    "v_accvgpr_write_b32 a65, v76\n\t" \
    "v_accvgpr_write_b32 a66, v76\n\t" \
    "v_accvgpr_write_b32 a67, v76\n\t" \
    "v_accvgpr_write_b32 a68, v76\n\t" \
    "v_accvgpr_write_b32 a69, v76\n\t" \
    "buffer_load_dwordx4 v[28:31], v2, s[12:15], s56 offen offset:3072\n\t" \
    "v_accvgpr_write_b32 a70, v76\n\t" \
    "v_accvgpr_write_b32 a71, v76\n\t" \
    "v_accvgpr_write_b32 a72, v76\n\t" \
    "v_accvgpr_write_b32 a73, v76\n\t" \
    "v_accvgpr_write_b32 a74, v76\n\t" \
    "v_accvgpr_write_b32 a75, v76\n\t" \
    "v_accvgpr_write_b32 a76, v76\n\t" \
    "v_accvgpr_write_b32 a77, v76\n\t" \
    "v_accvgpr_write_b32 a78, v76\n\t" \
    "v_accvgpr_write_b32 a79, v76\n\t" \
    "v_bfe_u32 v225, v1, 2, 2\n\t" \
    "v_and_b32 v226, 3, v1\n\t" \
    "buffer_load_dwordx4 v[32:35], v2, s[12:15], s57 offen offset:0\n\t" \
    "v_lshl_add_u32 v227, v72, 3, v225\n\t" \
    "v_and_b32 v230, 1, v72\n\t" \
    "v_xor_b32 v228, s50, v225\n\t" \
    "v_lshlrev_b32 v228, 6, v228\n\t" \
    "v_lshl_add_u32 v220, v227, 8, v228\n\t" \
    "v_lshl_add_u32 v220, v226, 3, v220\n\t" \
    "v_mfma_f32_32x32x16_bf16 a[80:95], v[80:83], v[80:83], a[64:79]\n\t" \
    "v_lshl_add_u32 v220, v72, 5, v220\n\t" \
    "v_add_u32 v221, 66560, v220\n\t" \
    "v_lshlrev_b32 v224, 5, v227\n\t" \
    "v_lshl_add_u32 v224, v226, 3, v224\n\t" \
    "v_add_u32 v224, 133120, v224\n\t" \
    "buffer_load_dwordx4 v[36:39], v2, s[12:15], s57 offen offset:1024\n\t" \
    "s_lshl_b32 s88, s50, 6\n\t" \
    "s_add_u32 s88, s88, 133120\n\t" \
    "v_lshlrev_b32 v219, 4, v226\n\t" \
    "v_mfma_f32_32x32x16_bf16 a[96:111], v[80:83], v[80:83], a[64:79]\n\t" \
    "v_add_u32 v219, s88, v219\n\t" \
    "v_lshl_add_u32 v219, v227, 8, v219\n\t" \
    "v_lshlrev_b32 v191, 5, v0\n\t" \
    "s_and_b32 s88, s50, 1\n\t" \
    "s_lshl_b32 s88, s88, 3\n\t" \
    "v_xor_b32 v229, s88, v73\n\t" \
    "v_lshlrev_b32 v229, 4, v229\n\t" \
    "v_lshl_add_u32 v176, v1, 8, v229\n\t" \
    "buffer_load_dwordx4 v[40:43], v2, s[12:15], s57 offen offset:2048\n\t" \
    "v_mfma_f32_32x32x16_bf16 a[112:127], v[80:83], v[80:83], a[64:79]\n\t" \
    "s_lshr_b32 s88, s50, 1\n\t" \
    "s_lshl_b32 s88, s88, 14\n\t" \
    "v_add_u32 v176, s88, v176\n\t" \
    "v_xor_b32 v177, 16, v176\n\t" \
    "v_xor_b32 v178, 32, v176\n\t" \
    "v_xor_b32 v179, 48, v176\n\t" \
    "v_xor_b32 v180, 64, v176\n\t" \
    "v_xor_b32 v181, 80, v176\n\t" \
    "v_mfma_f32_32x32x16_bf16 a[128:143], v[80:83], v[80:83], a[64:79]\n\t" \
    "v_xor_b32 v182, 96, v176\n\t" \
    "v_xor_b32 v183, 112, v176\n\t" \
    "buffer_load_dwordx4 v[44:47], v2, s[12:15], s57 offen offset:3072\n\t" \
    "v_lshrrev_b32 v77, 7, v0\n\t" \
    "v_mul_u32_u24 v77, 6144, v77\n\t" \
    "v_bfe_u32 v78, v0, 2, 5\n\t" \
    "v_lshl_add_u32 v77, v78, 7, v77\n\t" \
    "s_mul_i32 s54, s6, 12288\n\t" \
    "buffer_load_dword v231, v77, s[16:19], s54 offen offset:2048\n\t" \
    "s_cmp_eq_u32 s91, 0\n\t" \
    "s_cbranch_scc1 .Lnokvl_%=\n\t" \
    "s_cmp_eq_u32 s52, 1\n\t" \
    "s_cselect_b32 s54, s68, s70\n\t" \
    "s_cselect_b32 s55, s69, s71\n\t" \
    "s_mov_b32 s24, s54\n\t" \
    "s_and_b32 s25, s55, 0xffff\n\t" \
    "s_mov_b32 s26, 0x4000\n\t" \
    "s_mov_b32 s27, 0x20000\n\t" \
    "s_lshl_b32 s54, s50, 12\n\t" \
    "s_add_u32 m0, s54, 133120\n\t" \
    "s_nop 0\n\t" \
    "buffer_load_dwordx4 v2, s[24:27], s54 offen offset:0 lds\n\t" \
    "buffer_load_dwordx4 v2, s[24:27], s54 offen offset:1024 lds\n\t" \
    "buffer_load_dwordx4 v2, s[24:27], s54 offen offset:2048 lds\n\t" \
    "buffer_load_dwordx4 v2, s[24:27], s54 offen offset:3072 lds\n\t" \
    ".Lnokvl_%=:\n\t" \
    "s_cmp_eq_u32 s90, 0\n\t" \
    "s_cbranch_scc1 .Lnowo_%=\n\t" \
    "s_mov_b32 s28, s72\n\t" \
    "s_and_b32 s29, s73, 0xffff\n\t" \
    "s_mov_b32 s30, 0x80000\n\t" \
    "s_mov_b32 s31, 0x20000\n\t" \
    "s_lshr_b32 s54, s63, 2\n\t" \
    "s_lshl_b32 s54, s54, 15\n\t" \
    "s_lshl_b32 s55, s50, 13\n\t" \
    "s_add_u32 s54, s54, s55\n\t" \
    "s_and_b32 s55, s63, 3\n\t" \
    "s_lshl_b32 s55, s55, 8\n\t" \
    "s_add_u32 s54, s54, s55\n\t" \
    "s_lshl_b32 s55, s50, 11\n\t" \
    "s_add_u32 m0, s55, 133120\n\t" \
    "s_nop 0\n\t" \
    "buffer_load_dwordx4 v11, s[28:31], s54 offen lds\n\t" \
    "s_add_u32 s54, s54, 0xc00\n\t" \
    "buffer_load_dwordx4 v11, s[28:31], s54 offen offset:1024 lds\n\t" \
    ".Lnowo_%=:\n\t" \
    "s_lshl_b32 s7, s50, 17\n\t" \
    "s_lshl_b32 s55, s52, 8\n\t" \
    "s_add_u32 s7, s7, s55\n\t" \
    "v_lshlrev_b32 v3, 6, v72\n\t" \
    "v_xor_b32 v3, v3, v11\n\t" \
    "s_mul_i32 m0, s50, 16640\n\t" \
    "v_xor_b32 v229, v73, v72\n\t" \
    "v_lshlrev_b32 v229, 4, v229\n\t" \
    "v_lshl_add_u32 v184, v72, 8, v229\n\t" \
    "s_and_b32 s88, s50, 1\n\t" \
    "s_lshl_b32 s88, s88, 14\n\t" \
    "v_add_u32 v184, s88, v184\n\t" \
    "v_mfma_f32_32x32x16_bf16 a[144:159], v[80:83], v[80:83], a[64:79]\n\t" \
    "v_xor_b32 v185, 64, v184\n\t" \
    "v_xor_b32 v186, 128, v184\n\t" \
    "v_xor_b32 v187, 192, v184\n\t" \
    "v_xor_b32 v196, 0x4000, v184\n\t" \
    "v_xor_b32 v197, 0x4000, v185\n\t" \
    "v_xor_b32 v198, 0x4000, v186\n\t" \
    "v_xor_b32 v199, 0x4000, v187\n\t" \
    "v_lshlrev_b32 v188, 5, v1\n\t" \
    "v_mfma_f32_32x32x16_bf16 a[160:175], v[80:83], v[80:83], a[64:79]\n\t" \
    "v_add_u32 v188, 155648, v188\n\t" \
    "v_lshlrev_b32 v190, 4, v0\n\t" \
    "v_add_u32 v190, 155648, v190\n\t" \
    "v_and_b32 v216, 7, v1\n\t" \
    "v_lshlrev_b32 v216, 4, v216\n\t" \
    "v_bfe_u32 v229, v1, 3, 1\n\t" \
    "v_lshl_add_u32 v216, v229, 13, v216\n\t" \
    "s_and_b32 s89, s50, 1\n\t" \
    "v_mfma_f32_32x32x16_bf16 a[176:191], v[80:83], v[80:83], a[64:79]\n\t" \
    "v_xor_b32 v228, s89, v230\n\t" \
    "v_lshl_add_u32 v216, v228, 14, v216\n\t" \
    "v_lshrrev_b32 v189, 5, v1\n\t" \
    "v_lshl_add_u32 v216, v189, 9, v216\n\t" \
    "s_lshl_b32 s88, s63, 15\n\t" \
    "s_lshl_b32 s89, s52, 11\n\t" \
    "s_add_u32 s88, s88, s89\n\t" \
    "s_lshr_b32 s89, s50, 1\n\t" \
    "s_lshl_b32 s89, s89, 10\n\t" \
    "s_add_u32 s88, s88, s89\n\t" \
    "s_and_b32 s89, s50, 1\n\t" \
    "s_lshl_b32 s89, s89, 8\n\t" \
    "s_add_u32 s88, s88, s89\n\t" \
    "s_add_u32 s80, s88, 2752512\n\t" \
    "s_mov_b32 s94, 0x3eb8aa3b\n\t" \
    "v_bfe_u32 v217, v1, 2, 1\n\t" \
    "v_lshl_add_u32 v217, v217, 3, v226\n\t" \
    "v_lshl_add_u32 v217, v229, 2, v217\n\t" \
    "v_lshl_add_u32 v217, v189, 5, v217\n\t" \
    "v_lshlrev_b32 v217, 4, v217\n\t" \
    "v_lshlrev_b32 v228, 4, v73\n\t" \
    "v_lshl_add_u32 v228, v189, 8, v228\n\t" \
    "v_cndmask_b32 v217, v228, v217, s[86:87]\n\t" \
    "v_lshl_add_u32 v217, v230, 3, v217\n\t" \
    "s_and_b32 s89, s50, 1\n\t" \
    "s_lshr_b32 s88, s52, 1\n\t" \
    "s_add_u32 s95, s88, 8\n\t" \
    "s_lshl_b32 s89, s89, s95\n\t" \
    "s_lshl_b32 s88, s88, 11\n\t" \
    "s_add_u32 s95, s88, s89\n\t" \
    "s_lshr_b32 s88, s63, 1\n\t" \
    "s_lshl_b32 s88, s88, 12\n\t" \
    "s_add_u32 s95, s95, s88\n\t" \
    "s_and_b32 s89, s63, 1\n\t" \
    "s_lshl_b32 s89, s89, 10\n\t" \
    "s_add_u32 s95, s95, s89\n\t" \
    "s_add_u32 s95, s95, 262144\n\t" \
    "s_lshl_b32 s82, s63, 2\n\t" \
    "s_add_u32 s82, s82, s52\n\t" \
    "s_lshl_b32 s82, s82, 7\n\t" \
    "s_add_u32 s82, s82, 32768\n\t" \
    "s_lshr_b32 s83, s50, 1\n\t" \
    "s_sub_u32 s83, s91, s83\n\t" \
    "s_lshr_b32 s83, s83, 1\n\t" \
    "s_and_b32 s83, s83, 1\n\t" \
    "s_cmp_eq_u32 s91, 0\n\t" \
    "s_cselect_b32 s83, 0, s83\n\t" \
    "s_waitcnt vmcnt(11)\n\t" \
    "v_readfirstlane_b32 s54, v74\n\t" \
    "v_readfirstlane_b32 s55, v75\n\t" \
    "v_lshlrev_b32 v12, 16, v12\n\t" \
    "v_lshlrev_b32 v13, 16, v13\n\t" \
    "v_lshlrev_b32 v14, 16, v14\n\t" \
    "v_lshlrev_b32 v15, 16, v15\n\t" \
    "s_add_u32 s54, s54, 1\n\t" \
    "s_addc_u32 s55, s55, 0\n\t" \
    "s_lshl_b32 s56, s54, 16\n\t" \
    "s_lshr_b64 s[88:89], s[54:55], 16\n\t" \
    "s_mov_b32 s57, s88\n\t" \
    "s_lshl_b32 s84, s54, 24\n\t" \
    "s_lshr_b64 s[88:89], s[54:55], 8\n\t" \
    "s_mov_b32 s85, s88\n\t" \
    "v_mul_f32 v76, 0xbfb8aa3b, v12\n\t" \
    "v_mul_f32 v77, 0xbfb8aa3b, v13\n\t" \
    "v_mul_f32 v78, 0xbfb8aa3b, v14\n\t" \
    "v_mul_f32 v79, 0xbfb8aa3b, v15\n\t" \
    "v_exp_f32 v76, v76\n\t" \
    "v_exp_f32 v77, v77\n\t" \
    "v_exp_f32 v78, v78\n\t" \
    "v_exp_f32 v79, v79\n\t" \
    "v_add_f32 v76, 1.0, v76\n\t" \
    "v_add_f32 v77, 1.0, v77\n\t" \
    "v_add_f32 v78, 1.0, v78\n\t" \
    "v_add_f32 v79, 1.0, v79\n\t" \
    "v_rcp_f32 v76, v76\n\t" \
    "v_rcp_f32 v77, v77\n\t" \
    "v_rcp_f32 v78, v78\n\t" \
    "v_rcp_f32 v79, v79\n\t" \
    "v_mul_f32 v12, v12, v76\n\t" \
    "v_mul_f32 v13, v13, v77\n\t" \
    "v_mul_f32 v14, v14, v78\n\t" \
    "v_mul_f32 v15, v15, v79\n\t" \
    "v_cvt_pk_bf16_f32 v76, v12, v13\n\t" \
    "v_cvt_pk_bf16_f32 v77, v14, v15\n\t" \
    "v_lshlrev_b32 v12, 16, v76\n\t" \
    "v_and_b32 v13, 0xffff0000, v76\n\t" \
    "v_lshlrev_b32 v14, 16, v77\n\t" \
    "v_and_b32 v15, 0xffff0000, v77\n\t" \
    "s_cmp_eq_u32 s91, 0\n\t" \
    "s_cbranch_scc1 .Lmod_%=\n\t" \
    "s_branch .Lnomod_%=\n\t" \
    ".Lmod_%=:\n\t" \
    "v_lshlrev_b32 v88, 16, v48\n\t" \
    "v_and_b32 v89, 0xffff0000, v48\n\t" \
    "v_lshlrev_b32 v90, 16, v49\n\t" \
    "v_and_b32 v91, 0xffff0000, v49\n\t" \
    "v_lshlrev_b32 v92, 16, v50\n\t" \
    "v_and_b32 v93, 0xffff0000, v50\n\t" \
    "v_lshlrev_b32 v94, 16, v51\n\t" \
    "v_and_b32 v95, 0xffff0000, v51\n\t" \
    "v_pk_fma_f32 v[80:81], v[88:89], v[12:13], 0 op_sel_hi:[1,0,0]\n\t" \
    "v_pk_fma_f32 v[82:83], v[90:91], v[12:13], 0 op_sel_hi:[1,0,0]\n\t" \
    "v_pk_fma_f32 v[84:85], v[92:93], v[12:13], 0 op_sel_hi:[1,0,0]\n\t" \
    "v_pk_fma_f32 v[86:87], v[94:95], v[12:13], 0 op_sel_hi:[1,0,0]\n\t" \
    "v_lshlrev_b32 v88, 16, v52\n\t" \
    "v_and_b32 v89, 0xffff0000, v52\n\t" \
    "v_lshlrev_b32 v90, 16, v53\n\t" \
    "v_and_b32 v91, 0xffff0000, v53\n\t" \
    "v_lshlrev_b32 v92, 16, v54\n\t" \
    "v_and_b32 v93, 0xffff0000, v54\n\t" \
    "v_lshlrev_b32 v94, 16, v55\n\t" \
    "v_and_b32 v95, 0xffff0000, v55\n\t" \
    "v_pk_fma_f32 v[80:81], v[88:89], v[12:13], v[80:81] op_sel:[0,1,0] op_sel_hi:[1,1,1]\n\t" \
    "v_pk_fma_f32 v[82:83], v[90:91], v[12:13], v[82:83] op_sel:[0,1,0] op_sel_hi:[1,1,1]\n\t" \
    "v_pk_fma_f32 v[84:85], v[92:93], v[12:13], v[84:85] op_sel:[0,1,0] op_sel_hi:[1,1,1]\n\t" \
    "v_pk_fma_f32 v[86:87], v[94:95], v[12:13], v[86:87] op_sel:[0,1,0] op_sel_hi:[1,1,1]\n\t" \
    "s_nop 0\n\t" \
    "v_permlane32_swap_b32 v80, v84\n\t" \
    "v_permlane32_swap_b32 v81, v85\n\t" \
    "v_permlane32_swap_b32 v82, v86\n\t" \
    "v_permlane32_swap_b32 v83, v87\n\t" \
    "v_add_f32 v80, v80, v84\n\t" \
    "v_add_f32 v81, v81, v85\n\t" \
    "v_add_f32 v82, v82, v86\n\t" \
    "v_add_f32 v83, v83, v87\n\t" \
    "s_nop 0\n\t" \
    "v_permlane16_swap_b32 v80, v82\n\t" \
    "v_permlane16_swap_b32 v81, v83\n\t" \
    "v_add_f32 v80, v80, v82\n\t" \
    "v_add_f32 v81, v81, v83\n\t" \
    "s_nop 0\n\t" \
    "v_add_f32_dpp v80, v80, v80 row_ror:8 row_mask:0xf bank_mask:0xf\n\t" \
    "v_add_f32_dpp v81, v81, v81 row_ror:8 row_mask:0xf bank_mask:0xf\n\t" \
    "s_nop 0\n\t" \
    "v_add_f32_dpp v80, v80, v80 row_ror:4 row_mask:0xf bank_mask:0xf\n\t" \
    "v_add_f32_dpp v81, v81, v81 row_ror:4 row_mask:0xf bank_mask:0xf\n\t" \
    "s_nop 0\n\t" \
    "v_add_f32_dpp v80, v80, v80 row_ror:2 row_mask:0xf bank_mask:0xf\n\t" \
    "v_add_f32_dpp v81, v81, v81 row_ror:2 row_mask:0xf bank_mask:0xf\n\t" \
    "v_and_b32 v88, 1, v1\n\t" \
    "v_lshlrev_b32 v88, 7, v88\n\t" \
    "v_lshrrev_b32 v89, 4, v1\n\t" \
    "v_lshl_add_u32 v88, v89, 5, v88\n\t" \
    "s_lshl_b32 s96, s50, 2\n\t" \
    "s_add_u32 s96, s96, 159744\n\t" \
    "v_add_u32 v88, s96, v88\n\t" \
    "ds_write2_b32 v88, v80, v81 offset0:0 offset1:4\n\t" \
    "v_and_b32 v89, 15, v1\n\t" \
    "v_lshlrev_b32 v89, 4, v89\n\t" \
    "v_add_u32 v89, 159744, v89\n\t" \
    "s_waitcnt lgkmcnt(0)\n\t" \
    "s_barrier\n\t" \
    "ds_read_b128 v[80:83], v89\n\t" \
    "s_waitcnt lgkmcnt(0)\n\t" \
    "v_add_f32 v80, v80, v81\n\t" \
    "v_add_f32 v82, v82, v83\n\t" \
    "v_add_f32 v76, v80, v82\n\t" \
    "v_lshrrev_b32 v76, 8, v76\n\t" \
    "v_or_b32 v76, s84, v76\n\t" \
    "v_mov_b32 v77, s85\n\t" \
    "v_lshlrev_b32 v78, 3, v0\n\t" \
    "s_lshl_b32 s88, s63, 7\n\t" \
    "s_lshl_b32 s89, s90, 13\n\t" \
    "s_add_u32 s88, s88, s89\n\t" \
    "s_add_u32 s88, s88, 4096\n\t" \
    "v_cmp_gt_u32 vcc, 16, v0\n\t" \
    "s_nop 0\n\t" \
    "s_mov_b64 exec, vcc\n\t" \
    "buffer_store_dwordx2 v[76:77], v78, s[8:11], s88 offen sc1\n\t" \
    "s_mov_b64 exec, -1\n\t" \
    "v_mov_b32 v10, 0\n\t" \
    ".Lnomod_%=:\n\t" \
    "buffer_load_dwordx4 v3, s[20:23], s7 offen lds\n\t" \
    "s_add_u32 m0, m0, 0x400\n\t" \
    "s_addk_i32 s7, 0x1000\n\t" \
    "buffer_load_dwordx4 v3, s[20:23], s7 offen lds\n\t" \
    "s_add_u32 m0, m0, 0x420\n\t" \
    "s_addk_i32 s7, 0x1000\n\t" \
    "buffer_load_dwordx4 v3, s[20:23], s7 offen lds\n\t" \
    "s_add_u32 m0, m0, 0x400\n\t" \
    "s_addk_i32 s7, 0x1000\n\t" \
    "buffer_load_dwordx4 v3, s[20:23], s7 offen lds\n\t" \
    "s_add_u32 m0, m0, 0x420\n\t" \
    "s_addk_i32 s7, 0x1000\n\t" \
    "buffer_load_dwordx4 v3, s[20:23], s7 offen lds\n\t" \
    "s_add_u32 m0, m0, 0x400\n\t" \
    "s_addk_i32 s7, 0x1000\n\t" \
    "buffer_load_dwordx4 v3, s[20:23], s7 offen lds\n\t" \
    "s_add_u32 m0, m0, 0x420\n\t" \
    "s_addk_i32 s7, 0x1000\n\t" \
    "s_waitcnt vmcnt(14)\n\t" \
    "v_lshlrev_b32 v48, 16, v16\n\t" \
    "v_and_b32 v49, 0xffff0000, v16\n\t" \
    "v_lshlrev_b32 v50, 16, v17\n\t" \
    "v_and_b32 v51, 0xffff0000, v17\n\t" \
    "v_lshlrev_b32 v52, 16, v18\n\t" \
    "buffer_load_dwordx4 v3, s[20:23], s7 offen lds\n\t" \
    "s_add_u32 m0, m0, 0x400\n\t" \
    "s_addk_i32 s7, 0x1000\n\t" \
    "v_and_b32 v53, 0xffff0000, v18\n\t" \
    "v_lshlrev_b32 v54, 16, v19\n\t" \
    "v_and_b32 v55, 0xffff0000, v19\n\t" \
    "v_pk_add_f32 v[16:17], v[48:49], v[50:51]\n\t" \
    "v_pk_mul_f32 v[18:19], v[48:49], v[48:49]\n\t" \
    "v_pk_add_f32 v[16:17], v[16:17], v[52:53]\n\t" \
    "buffer_load_dwordx4 v3, s[20:23], s7 offen lds\n\t" \
    "s_add_u32 m0, m0, 0x420\n\t" \
    "s_addk_i32 s7, 0x1000\n\t" \
    "v_pk_fma_f32 v[18:19], v[50:51], v[50:51], v[18:19]\n\t" \
    "v_pk_add_f32 v[16:17], v[16:17], v[54:55]\n\t" \
    "v_pk_fma_f32 v[18:19], v[52:53], v[52:53], v[18:19]\n\t" \
    "v_add_f32 v16, v16, v17\n\t" \
    "v_pk_fma_f32 v[18:19], v[54:55], v[54:55], v[18:19]\n\t" \
    "s_waitcnt vmcnt(15)\n\t" \
    "buffer_load_dwordx4 v3, s[20:23], s7 offen lds\n\t" \
    "s_add_u32 m0, m0, 0x400\n\t" \
    "s_addk_i32 s7, 0x1000\n\t" \
    "v_lshlrev_b32 v56, 16, v20\n\t" \
    "v_and_b32 v57, 0xffff0000, v20\n\t" \
    "v_lshlrev_b32 v58, 16, v21\n\t" \
    "v_and_b32 v59, 0xffff0000, v21\n\t" \
    "v_lshlrev_b32 v60, 16, v22\n\t" \
    "v_and_b32 v61, 0xffff0000, v22\n\t" \
    "buffer_load_dwordx4 v3, s[20:23], s7 offen lds\n\t" \
    "s_add_u32 m0, m0, 0x420\n\t" \
    "s_addk_i32 s7, 0x1000\n\t" \
    "v_lshlrev_b32 v62, 16, v23\n\t" \
    "v_and_b32 v63, 0xffff0000, v23\n\t" \
    "v_pk_add_f32 v[20:21], v[56:57], v[58:59]\n\t" \
    "v_pk_mul_f32 v[22:23], v[56:57], v[56:57]\n\t" \
    "v_pk_add_f32 v[20:21], v[20:21], v[60:61]\n\t" \
    "v_pk_fma_f32 v[22:23], v[58:59], v[58:59], v[22:23]\n\t" \
    "buffer_load_dwordx4 v3, s[20:23], s7 offen lds\n\t" \
    "s_add_u32 m0, m0, 0x400\n\t" \
    "s_addk_i32 s7, 0x1000\n\t" \
    "v_pk_add_f32 v[20:21], v[20:21], v[62:63]\n\t" \
    "v_pk_fma_f32 v[22:23], v[60:61], v[60:61], v[22:23]\n\t" \
    "v_add_f32 v20, v20, v21\n\t" \
    "v_pk_fma_f32 v[22:23], v[62:63], v[62:63], v[22:23]\n\t" \
    "v_add_f32 v18, v18, v19\n\t" \
    "s_waitcnt vmcnt(17)\n\t" \
    "buffer_load_dwordx4 v3, s[20:23], s7 offen lds\n\t" \
    "s_add_u32 m0, m0, 0x420\n\t" \
    "s_addk_i32 s7, 0x1000\n\t" \
    "v_lshlrev_b32 v64, 16, v24\n\t" \
    "v_and_b32 v65, 0xffff0000, v24\n\t" \
    "v_lshlrev_b32 v66, 16, v25\n\t" \
    "v_and_b32 v67, 0xffff0000, v25\n\t" \
    "v_lshlrev_b32 v68, 16, v26\n\t" \
    "v_and_b32 v69, 0xffff0000, v26\n\t" \
    "buffer_load_dwordx4 v3, s[20:23], s7 offen lds\n\t" \
    "s_add_u32 m0, m0, 0x400\n\t" \
    "s_addk_i32 s7, 0x1000\n\t" \
    "v_lshlrev_b32 v70, 16, v27\n\t" \
    "v_and_b32 v71, 0xffff0000, v27\n\t" \
    "v_pk_add_f32 v[24:25], v[64:65], v[66:67]\n\t" \
    "v_pk_mul_f32 v[26:27], v[64:65], v[64:65]\n\t" \
    "v_pk_add_f32 v[24:25], v[24:25], v[68:69]\n\t" \
    "v_pk_fma_f32 v[26:27], v[66:67], v[66:67], v[26:27]\n\t" \
    "buffer_load_dwordx4 v3, s[20:23], s7 offen lds\n\t" \
    "s_add_u32 m0, m0, 0x420\n\t" \
    "s_addk_i32 s7, 0x1000\n\t" \
    "v_pk_add_f32 v[24:25], v[24:25], v[70:71]\n\t" \
    "v_pk_fma_f32 v[26:27], v[68:69], v[68:69], v[26:27]\n\t" \
    "v_add_f32 v24, v24, v25\n\t" \
    "v_pk_fma_f32 v[26:27], v[70:71], v[70:71], v[26:27]\n\t" \
    "v_add_f32 v22, v22, v23\n\t" \
    "s_waitcnt vmcnt(19)\n\t" \
    "buffer_load_dwordx4 v3, s[20:23], s7 offen lds\n\t" \
    "s_add_u32 m0, m0, 0x400\n\t" \
    "s_addk_i32 s7, 0x1000\n\t" \
    "v_lshlrev_b32 v72, 16, v28\n\t" \
    "v_and_b32 v73, 0xffff0000, v28\n\t" \
    "v_lshlrev_b32 v74, 16, v29\n\t" \
    "v_and_b32 v75, 0xffff0000, v29\n\t" \
    "v_lshlrev_b32 v76, 16, v30\n\t" \
    "v_and_b32 v77, 0xffff0000, v30\n\t" \
    "buffer_load_dwordx4 v3, s[20:23], s7 offen lds\n\t" \
    "s_add_u32 m0, m0, 0xc720\n\t" \
    "s_addk_i32 s7, 0x1000\n\t" \
    "v_lshlrev_b32 v78, 16, v31\n\t" \
    "v_and_b32 v79, 0xffff0000, v31\n\t" \
    "v_pk_add_f32 v[28:29], v[72:73], v[74:75]\n\t" \
    "v_pk_mul_f32 v[30:31], v[72:73], v[72:73]\n\t" \
    "v_pk_add_f32 v[28:29], v[28:29], v[76:77]\n\t" \
    "v_pk_fma_f32 v[30:31], v[74:75], v[74:75], v[30:31]\n\t" \
    "buffer_load_dwordx4 v3, s[20:23], s7 offen lds\n\t" \
    "s_add_u32 m0, m0, 0x400\n\t" \
    "s_addk_i32 s7, 0x1000\n\t" \
    "v_pk_add_f32 v[28:29], v[28:29], v[78:79]\n\t" \
    "v_pk_fma_f32 v[30:31], v[76:77], v[76:77], v[30:31]\n\t" \
    "v_add_f32 v28, v28, v29\n\t" \
    "v_pk_fma_f32 v[30:31], v[78:79], v[78:79], v[30:31]\n\t" \
    "v_add_f32 v26, v26, v27\n\t" \
    "s_waitcnt vmcnt(21)\n\t" \
    "buffer_load_dwordx4 v3, s[20:23], s7 offen lds\n\t" \
    "s_add_u32 m0, m0, 0x420\n\t" \
    "s_addk_i32 s7, 0x1000\n\t" \
    "v_lshlrev_b32 v80, 16, v32\n\t" \
    "v_and_b32 v81, 0xffff0000, v32\n\t" \
    "v_lshlrev_b32 v82, 16, v33\n\t" \
    "v_and_b32 v83, 0xffff0000, v33\n\t" \
    "v_lshlrev_b32 v84, 16, v34\n\t" \
    "v_and_b32 v85, 0xffff0000, v34\n\t" \
    "buffer_load_dwordx4 v3, s[20:23], s7 offen lds\n\t" \
    "s_add_u32 m0, m0, 0x400\n\t" \
    "s_addk_i32 s7, 0x1000\n\t" \
    "v_lshlrev_b32 v86, 16, v35\n\t" \
    "v_and_b32 v87, 0xffff0000, v35\n\t" \
    "v_pk_add_f32 v[32:33], v[80:81], v[82:83]\n\t" \
    "v_pk_mul_f32 v[34:35], v[80:81], v[80:81]\n\t" \
    "v_pk_add_f32 v[32:33], v[32:33], v[84:85]\n\t" \
    "v_pk_fma_f32 v[34:35], v[82:83], v[82:83], v[34:35]\n\t" \
    "buffer_load_dwordx4 v3, s[20:23], s7 offen lds\n\t" \
    "s_add_u32 m0, m0, 0x420\n\t" \
    "s_addk_i32 s7, 0x1000\n\t" \
    "v_pk_add_f32 v[32:33], v[32:33], v[86:87]\n\t" \
    "v_pk_fma_f32 v[34:35], v[84:85], v[84:85], v[34:35]\n\t" \
    "v_add_f32 v32, v32, v33\n\t" \
    "v_pk_fma_f32 v[34:35], v[86:87], v[86:87], v[34:35]\n\t" \
    "v_add_f32 v30, v30, v31\n\t" \
    "s_waitcnt vmcnt(23)\n\t" \
    "buffer_load_dwordx4 v3, s[20:23], s7 offen lds\n\t" \
    "s_add_u32 m0, m0, 0x400\n\t" \
    "s_addk_i32 s7, 0x1000\n\t" \
    "v_lshlrev_b32 v88, 16, v36\n\t" \
    "v_and_b32 v89, 0xffff0000, v36\n\t" \
    "v_lshlrev_b32 v90, 16, v37\n\t" \
    "v_and_b32 v91, 0xffff0000, v37\n\t" \
    "v_lshlrev_b32 v92, 16, v38\n\t" \
    "v_and_b32 v93, 0xffff0000, v38\n\t" \
    "buffer_load_dwordx4 v3, s[20:23], s7 offen lds\n\t" \
    "s_add_u32 m0, m0, 0x420\n\t" \
    "s_addk_i32 s7, 0x1000\n\t" \
    "v_lshlrev_b32 v94, 16, v39\n\t" \
    "v_and_b32 v95, 0xffff0000, v39\n\t" \
    "v_pk_add_f32 v[36:37], v[88:89], v[90:91]\n\t" \
    "v_pk_mul_f32 v[38:39], v[88:89], v[88:89]\n\t" \
    "v_pk_add_f32 v[36:37], v[36:37], v[92:93]\n\t" \
    "v_pk_fma_f32 v[38:39], v[90:91], v[90:91], v[38:39]\n\t" \
    "buffer_load_dwordx4 v3, s[20:23], s7 offen lds\n\t" \
    "s_add_u32 m0, m0, 0x400\n\t" \
    "s_addk_i32 s7, 0x1000\n\t" \
    "v_pk_add_f32 v[36:37], v[36:37], v[94:95]\n\t" \
    "v_pk_fma_f32 v[38:39], v[92:93], v[92:93], v[38:39]\n\t" \
    "v_add_f32 v36, v36, v37\n\t" \
    "v_pk_fma_f32 v[38:39], v[94:95], v[94:95], v[38:39]\n\t" \
    "v_add_f32 v34, v34, v35\n\t" \
    "s_waitcnt vmcnt(25)\n\t" \
    "buffer_load_dwordx4 v3, s[20:23], s7 offen lds\n\t" \
    "s_add_u32 m0, m0, 0x420\n\t" \
    "s_addk_i32 s7, 0x1000\n\t" \
    "v_lshlrev_b32 v96, 16, v40\n\t" \
    "v_and_b32 v97, 0xffff0000, v40\n\t" \
    "v_lshlrev_b32 v98, 16, v41\n\t" \
    "v_and_b32 v99, 0xffff0000, v41\n\t" \
    "v_lshlrev_b32 v100, 16, v42\n\t" \
    "v_and_b32 v101, 0xffff0000, v42\n\t" \
    "buffer_load_dwordx4 v3, s[20:23], s7 offen lds\n\t" \
    "s_add_u32 m0, m0, 0x400\n\t" \
    "s_addk_i32 s7, 0x1000\n\t" \
    "v_lshlrev_b32 v102, 16, v43\n\t" \
    "v_and_b32 v103, 0xffff0000, v43\n\t" \
    "v_pk_add_f32 v[40:41], v[96:97], v[98:99]\n\t" \
    "v_pk_mul_f32 v[42:43], v[96:97], v[96:97]\n\t" \
    "v_pk_add_f32 v[40:41], v[40:41], v[100:101]\n\t" \
    "v_pk_fma_f32 v[42:43], v[98:99], v[98:99], v[42:43]\n\t" \
    "buffer_load_dwordx4 v3, s[20:23], s7 offen lds\n\t" \
    "s_add_u32 m0, m0, 0x420\n\t" \
    "s_addk_i32 s7, 0x1000\n\t" \
    "v_pk_add_f32 v[40:41], v[40:41], v[102:103]\n\t" \
    "v_pk_fma_f32 v[42:43], v[100:101], v[100:101], v[42:43]\n\t" \
    "v_add_f32 v40, v40, v41\n\t" \
    "v_pk_fma_f32 v[42:43], v[102:103], v[102:103], v[42:43]\n\t" \
    "v_add_f32 v38, v38, v39\n\t" \
    "s_waitcnt vmcnt(27)\n\t" \
    "buffer_load_dwordx4 v3, s[20:23], s7 offen lds\n\t" \
    "s_add_u32 m0, m0, 0x400\n\t" \
    "s_addk_i32 s7, 0x1000\n\t" \
    "v_lshlrev_b32 v104, 16, v44\n\t" \
    "v_and_b32 v105, 0xffff0000, v44\n\t" \
    "v_lshlrev_b32 v106, 16, v45\n\t" \
    "v_and_b32 v107, 0xffff0000, v45\n\t" \
    "v_lshlrev_b32 v108, 16, v46\n\t" \
    "v_and_b32 v109, 0xffff0000, v46\n\t" \
    "buffer_load_dwordx4 v3, s[20:23], s7 offen lds\n\t" \
    "s_add_u32 m0, m0, 0x420\n\t" \
    "s_addk_i32 s7, 0x1000\n\t" \
    "v_lshlrev_b32 v110, 16, v47\n\t" \
    "v_and_b32 v111, 0xffff0000, v47\n\t" \
    "v_pk_add_f32 v[44:45], v[104:105], v[106:107]\n\t" \
    "v_pk_mul_f32 v[46:47], v[104:105], v[104:105]\n\t" \
    "v_pk_add_f32 v[44:45], v[44:45], v[108:109]\n\t" \
    "v_pk_fma_f32 v[46:47], v[106:107], v[106:107], v[46:47]\n\t" \
    "buffer_load_dwordx4 v3, s[20:23], s7 offen lds\n\t" \
    "s_add_u32 m0, m0, 0x400\n\t" \
    "s_addk_i32 s7, 0x1000\n\t" \
    "v_pk_add_f32 v[44:45], v[44:45], v[110:111]\n\t" \
    "v_pk_fma_f32 v[46:47], v[108:109], v[108:109], v[46:47]\n\t" \
    "v_add_f32 v44, v44, v45\n\t" \
    "v_pk_fma_f32 v[46:47], v[110:111], v[110:111], v[46:47]\n\t" \
    "v_add_f32 v42, v42, v43\n\t" \
    "v_add_f32 v46, v46, v47\n\t" \
    "buffer_load_dwordx4 v3, s[20:23], s7 offen lds\n\t" \
    "s_add_u32 m0, m0, 0x420\n\t" \
    "s_addk_i32 s7, 0x1000\n\t" \
    "v_permlane32_swap_b32 v16, v18\n\t" \
    "v_permlane32_swap_b32 v20, v22\n\t" \
    "v_permlane32_swap_b32 v24, v26\n\t" \
    "v_permlane32_swap_b32 v28, v30\n\t" \
    "v_permlane32_swap_b32 v32, v34\n\t" \
    "v_permlane32_swap_b32 v36, v38\n\t" \
    "buffer_load_dwordx4 v3, s[20:23], s7 offen lds\n\t" \
    "s_add_u32 m0, m0, 0x400\n\t" \
    "s_addk_i32 s7, 0x1000\n\t" \
    "v_permlane32_swap_b32 v40, v42\n\t" \
    "v_permlane32_swap_b32 v44, v46\n\t" \
    "v_add_f32 v16, v16, v18\n\t" \
    "v_add_f32 v20, v20, v22\n\t" \
    "v_add_f32 v24, v24, v26\n\t" \
    "v_add_f32 v28, v28, v30\n\t" \
    "buffer_load_dwordx4 v3, s[20:23], s7 offen lds\n\t" \
    "s_add_u32 m0, m0, 0x420\n\t" \
    "s_addk_i32 s7, 0x1000\n\t" \
    "v_add_f32 v32, v32, v34\n\t" \
    "v_add_f32 v36, v36, v38\n\t" \
    "v_add_f32 v40, v40, v42\n\t" \
    "v_add_f32 v44, v44, v46\n\t" \
    "v_permlane16_swap_b32 v16, v32\n\t" \
    "v_permlane16_swap_b32 v20, v36\n\t" \
    "v_permlane16_swap_b32 v24, v40\n\t" \
    "v_permlane16_swap_b32 v28, v44\n\t" \
    "v_add_f32 v16, v16, v32\n\t" \
    "v_add_f32 v20, v20, v36\n\t" \
    "v_add_f32 v24, v24, v40\n\t" \
    "v_add_f32 v28, v28, v44\n\t" \
    "v_add_f32_dpp v16, v16, v16 row_ror:8 row_mask:0xf bank_mask:0xf\n\t" \
    "v_add_f32_dpp v20, v20, v20 row_ror:8 row_mask:0xf bank_mask:0xf\n\t" \
    "v_add_f32_dpp v24, v24, v24 row_ror:8 row_mask:0xf bank_mask:0xf\n\t" \
    "v_add_f32_dpp v28, v28, v28 row_ror:8 row_mask:0xf bank_mask:0xf\n\t" \
    "v_add_f32_dpp v16, v16, v16 row_ror:4 row_mask:0xf bank_mask:0xf\n\t" \
    "v_add_f32_dpp v20, v20, v20 row_ror:4 row_mask:0xf bank_mask:0xf\n\t" \
    "v_add_f32_dpp v24, v24, v24 row_ror:4 row_mask:0xf bank_mask:0xf\n\t" \
    "v_add_f32_dpp v28, v28, v28 row_ror:4 row_mask:0xf bank_mask:0xf\n\t" \
    "v_add_f32_dpp v16, v16, v16 row_ror:2 row_mask:0xf bank_mask:0xf\n\t" \
    "v_add_f32_dpp v20, v20, v20 row_ror:2 row_mask:0xf bank_mask:0xf\n\t" \
    "v_add_f32_dpp v24, v24, v24 row_ror:2 row_mask:0xf bank_mask:0xf\n\t" \
    "v_add_f32_dpp v28, v28, v28 row_ror:2 row_mask:0xf bank_mask:0xf\n\t" \
    "v_add_f32_dpp v16, v16, v16 row_ror:1 row_mask:0xf bank_mask:0xf\n\t" \
    "v_add_f32_dpp v20, v20, v20 row_ror:1 row_mask:0xf bank_mask:0xf\n\t" \
    "v_add_f32_dpp v24, v24, v24 row_ror:1 row_mask:0xf bank_mask:0xf\n\t" \
    "v_add_f32_dpp v28, v28, v28 row_ror:1 row_mask:0xf bank_mask:0xf\n\t" \
    "v_mul_f32 v16, 0x3b000000, v16\n\t" \
    "v_mul_f32 v20, 0x3b000000, v20\n\t" \
    "v_mul_f32 v24, 0x3b000000, v24\n\t" \
    "v_mul_f32 v28, 0x3b000000, v28\n\t" \
    "v_mov_b32 v17, v16\n\t" \
    "v_mov_b32 v21, v20\n\t" \
    "v_mov_b32 v25, v24\n\t" \
    "v_mov_b32 v29, v28\n\t" \
    "v_permlane32_swap_b32 v16, v17\n\t" \
    "v_permlane32_swap_b32 v20, v21\n\t" \
    "v_permlane32_swap_b32 v24, v25\n\t" \
    "v_permlane32_swap_b32 v28, v29\n\t" \
    "v_fma_f32 v17, -v16, v16, v17\n\t" \
    "v_fma_f32 v21, -v20, v20, v21\n\t" \
    "v_fma_f32 v25, -v24, v24, v25\n\t" \
    "v_fma_f32 v29, -v28, v28, v29\n\t" \
    "v_max_f32 v17, 0, v17\n\t" \
    "v_max_f32 v21, 0, v21\n\t" \
    "v_max_f32 v25, 0, v25\n\t" \
    "v_max_f32 v29, 0, v29\n\t" \
    "v_add_f32 v17, 0x3727c5ac, v17\n\t" \
    "v_add_f32 v21, 0x3727c5ac, v21\n\t" \
    "v_add_f32 v25, 0x3727c5ac, v25\n\t" \
    "v_add_f32 v29, 0x3727c5ac, v29\n\t" \
    "v_rsq_f32 v17, v17\n\t" \
    "v_rsq_f32 v21, v21\n\t" \
    "v_rsq_f32 v25, v25\n\t" \
    "v_rsq_f32 v29, v29\n\t" \
    "v_mul_f32 v16, -v16, v17\n\t" \
    "v_mul_f32 v20, -v20, v21\n\t" \
    "v_mul_f32 v24, -v24, v25\n\t" \
    "v_mul_f32 v28, -v28, v29\n\t" \
    "v_readlane_b32 s64, v17, 0\n\t" \
    "v_readlane_b32 s65, v16, 0\n\t" \
    "v_readlane_b32 s66, v21, 0\n\t" \
    "v_readlane_b32 s67, v20, 0\n\t" \
    "v_readlane_b32 s68, v25, 0\n\t" \
    "v_readlane_b32 s69, v24, 0\n\t" \
    "v_readlane_b32 s70, v29, 0\n\t" \
    "v_readlane_b32 s71, v28, 0\n\t" \
    "v_readlane_b32 s72, v17, 16\n\t" \
    "v_readlane_b32 s73, v16, 16\n\t" \
    "v_readlane_b32 s74, v21, 16\n\t" \
    "v_readlane_b32 s75, v20, 16\n\t" \
    "v_readlane_b32 s76, v25, 16\n\t" \
    "v_readlane_b32 s77, v24, 16\n\t" \
    "v_readlane_b32 s78, v29, 16\n\t" \
    "v_readlane_b32 s79, v28, 16\n\t" \
    "v_pk_fma_f32 v[48:49], v[48:49], s[64:65], s[64:65] op_sel:[0,0,1] op_sel_hi:[1,0,1]\n\t" \
    "v_pk_fma_f32 v[50:51], v[50:51], s[64:65], s[64:65] op_sel:[0,0,1] op_sel_hi:[1,0,1]\n\t" \
    "v_pk_fma_f32 v[52:53], v[52:53], s[64:65], s[64:65] op_sel:[0,0,1] op_sel_hi:[1,0,1]\n\t" \
    "v_pk_fma_f32 v[54:55], v[54:55], s[64:65], s[64:65] op_sel:[0,0,1] op_sel_hi:[1,0,1]\n\t" \
    "v_pk_fma_f32 v[56:57], v[56:57], s[66:67], s[66:67] op_sel:[0,0,1] op_sel_hi:[1,0,1]\n\t" \
    "v_pk_fma_f32 v[58:59], v[58:59], s[66:67], s[66:67] op_sel:[0,0,1] op_sel_hi:[1,0,1]\n\t" \
    "v_pk_fma_f32 v[60:61], v[60:61], s[66:67], s[66:67] op_sel:[0,0,1] op_sel_hi:[1,0,1]\n\t" \
    "v_pk_fma_f32 v[62:63], v[62:63], s[66:67], s[66:67] op_sel:[0,0,1] op_sel_hi:[1,0,1]\n\t" \
    "v_pk_fma_f32 v[64:65], v[64:65], s[68:69], s[68:69] op_sel:[0,0,1] op_sel_hi:[1,0,1]\n\t" \
    "v_pk_fma_f32 v[66:67], v[66:67], s[68:69], s[68:69] op_sel:[0,0,1] op_sel_hi:[1,0,1]\n\t" \
    "v_pk_fma_f32 v[68:69], v[68:69], s[68:69], s[68:69] op_sel:[0,0,1] op_sel_hi:[1,0,1]\n\t" \
    "v_pk_fma_f32 v[70:71], v[70:71], s[68:69], s[68:69] op_sel:[0,0,1] op_sel_hi:[1,0,1]\n\t" \
    "v_pk_fma_f32 v[72:73], v[72:73], s[70:71], s[70:71] op_sel:[0,0,1] op_sel_hi:[1,0,1]\n\t" \
    "v_pk_fma_f32 v[74:75], v[74:75], s[70:71], s[70:71] op_sel:[0,0,1] op_sel_hi:[1,0,1]\n\t" \
    "v_pk_fma_f32 v[76:77], v[76:77], s[70:71], s[70:71] op_sel:[0,0,1] op_sel_hi:[1,0,1]\n\t" \
    "v_pk_fma_f32 v[78:79], v[78:79], s[70:71], s[70:71] op_sel:[0,0,1] op_sel_hi:[1,0,1]\n\t" \
    "v_pk_fma_f32 v[80:81], v[80:81], s[72:73], s[72:73] op_sel:[0,0,1] op_sel_hi:[1,0,1]\n\t" \
    "v_pk_fma_f32 v[82:83], v[82:83], s[72:73], s[72:73] op_sel:[0,0,1] op_sel_hi:[1,0,1]\n\t" \
    "v_pk_fma_f32 v[84:85], v[84:85], s[72:73], s[72:73] op_sel:[0,0,1] op_sel_hi:[1,0,1]\n\t" \
    "v_pk_fma_f32 v[86:87], v[86:87], s[72:73], s[72:73] op_sel:[0,0,1] op_sel_hi:[1,0,1]\n\t" \
    "v_pk_fma_f32 v[88:89], v[88:89], s[74:75], s[74:75] op_sel:[0,0,1] op_sel_hi:[1,0,1]\n\t" \
    "v_pk_fma_f32 v[90:91], v[90:91], s[74:75], s[74:75] op_sel:[0,0,1] op_sel_hi:[1,0,1]\n\t" \
    "v_pk_fma_f32 v[92:93], v[92:93], s[74:75], s[74:75] op_sel:[0,0,1] op_sel_hi:[1,0,1]\n\t" \
    "v_pk_fma_f32 v[94:95], v[94:95], s[74:75], s[74:75] op_sel:[0,0,1] op_sel_hi:[1,0,1]\n\t" \
    "v_pk_fma_f32 v[96:97], v[96:97], s[76:77], s[76:77] op_sel:[0,0,1] op_sel_hi:[1,0,1]\n\t" \
    "v_pk_fma_f32 v[98:99], v[98:99], s[76:77], s[76:77] op_sel:[0,0,1] op_sel_hi:[1,0,1]\n\t" \
    "v_pk_fma_f32 v[100:101], v[100:101], s[76:77], s[76:77] op_sel:[0,0,1] op_sel_hi:[1,0,1]\n\t" \
    "v_pk_fma_f32 v[102:103], v[102:103], s[76:77], s[76:77] op_sel:[0,0,1] op_sel_hi:[1,0,1]\n\t" \
    "v_pk_fma_f32 v[104:105], v[104:105], s[78:79], s[78:79] op_sel:[0,0,1] op_sel_hi:[1,0,1]\n\t" \
    "v_pk_fma_f32 v[106:107], v[106:107], s[78:79], s[78:79] op_sel:[0,0,1] op_sel_hi:[1,0,1]\n\t" \
    "v_pk_fma_f32 v[108:109], v[108:109], s[78:79], s[78:79] op_sel:[0,0,1] op_sel_hi:[1,0,1]\n\t" \
    "v_pk_fma_f32 v[110:111], v[110:111], s[78:79], s[78:79] op_sel:[0,0,1] op_sel_hi:[1,0,1]\n\t" \
    "s_waitcnt vmcnt(31)\n\t" \
    "s_mov_b32 s88, 4096\n\t" \
    "s_mov_b32 s89, 12288\n\t" \
    "s_waitcnt vmcnt(16)\n\t" \
    "buffer_load_dwordx4 v[16:19], v191, s[8:11], s88 offen offset:0 sc1\n\t" \
    "buffer_load_dwordx4 v[20:23], v191, s[8:11], s88 offen offset:16 sc1\n\t" \
    "buffer_load_dwordx4 v[24:27], v191, s[8:11], s89 offen offset:0 sc1\n\t" \
    "buffer_load_dwordx4 v[28:31], v191, s[8:11], s89 offen offset:16 sc1\n\t" \
    "s_waitcnt lgkmcnt(0)\n\t" \
    "s_barrier\n\t" \
    "ds_read_b64_tr_b16 a[0:1], v220 offset:0\n\t" \
    "ds_read_b64_tr_b16 a[2:3], v220 offset:1024\n\t" \
    "s_lshl_b32 s98, s48, 4\n\t" \
    "s_add_u32 s98, s98, s49\n\t" \
    "s_add_u32 s98, s98, 56\n\t" \
    "s_lshl_b32 s98, s98, 5\n\t" \
    "buffer_load_dwordx4 v[232:235], v10, s[16:19], s98 offen\n\t" \
    "ds_read_b64_tr_b16 a[192:193], v220 offset:32\n\t" \
    "ds_read_b64_tr_b16 a[194:195], v220 offset:1056\n\t" \
    "s_add_u32 s98, s98, 786432\n\t" \
    "buffer_load_dwordx4 v[236:239], v10, s[16:19], s98 offen\n\t" \
    "ds_read_b64_tr_b16 a[4:5], v220 offset:8320\n\t" \
    "ds_read_b64_tr_b16 a[6:7], v220 offset:9344\n\t" \
    "s_add_u32 s98, s98, 786432\n\t" \
    "buffer_load_dwordx4 v[240:243], v10, s[16:19], s98 offen\n\t" \
    "ds_read_b64_tr_b16 a[196:197], v220 offset:8352\n\t" \
    "ds_read_b64_tr_b16 a[198:199], v220 offset:9376\n\t" \
    "s_add_u32 s98, s98, 786432\n\t" \
    "buffer_load_dwordx4 v[244:247], v10, s[16:19], s98 offen\n\t" \
    "ds_read_b64_tr_b16 a[8:9], v220 offset:16640\n\t" \
    "ds_read_b64_tr_b16 a[10:11], v220 offset:17664\n\t" \
    "s_mul_i32 s90, s48, 28\n\t" \
    "s_max_u32 s98, s49, 4\n\t" \
    "s_add_u32 s90, s90, s98\n\t" \
    "s_sub_u32 s90, s90, 4\n\t" \
    "s_lshr_b32 s98, s90, 2\n\t" \
    "s_lshl_b32 s98, s98, 15\n\t" \
    "s_lshl_b32 s99, s50, 13\n\t" \
    "s_add_u32 s98, s98, s99\n\t" \
    "s_and_b32 s99, s90, 3\n\t" \
    "s_lshl_b32 s99, s99, 8\n\t" \
    "s_add_u32 s98, s98, s99\n\t" \
    "buffer_load_dwordx4 v[248:251], v11, s[36:39], s98 offen\n\t" \
    "ds_read_b64_tr_b16 a[200:201], v220 offset:16672\n\t" \
    "ds_read_b64_tr_b16 a[202:203], v220 offset:17696\n\t" \
    "s_add_u32 s98, s98, 0x1000\n\t" \
    "buffer_load_dwordx4 v[252:255], v11, s[36:39], s98 offen\n\t" \
    "ds_read_b64_tr_b16 a[12:13], v220 offset:24960\n\t" \
    "ds_read_b64_tr_b16 a[14:15], v220 offset:25984\n\t" \
    "ds_read_b64_tr_b16 a[204:205], v220 offset:24992\n\t" \
    "ds_read_b64_tr_b16 a[206:207], v220 offset:26016\n\t" \
    "ds_read_b64_tr_b16 a[16:17], v220 offset:33280\n\t" \
    "ds_read_b64_tr_b16 a[18:19], v220 offset:34304\n\t" \
    "ds_read_b64_tr_b16 a[208:209], v220 offset:33312\n\t" \
    "ds_read_b64_tr_b16 a[210:211], v220 offset:34336\n\t" \
    "ds_read_b64_tr_b16 a[20:21], v220 offset:41600\n\t" \
    "ds_read_b64_tr_b16 a[22:23], v220 offset:42624\n\t" \
    "ds_read_b64_tr_b16 a[212:213], v220 offset:41632\n\t" \
    "ds_read_b64_tr_b16 a[214:215], v220 offset:42656\n\t" \
    "ds_read_b64_tr_b16 a[24:25], v220 offset:49920\n\t" \
    "ds_read_b64_tr_b16 a[26:27], v220 offset:50944\n\t" \
    "ds_read_b64_tr_b16 a[216:217], v220 offset:49952\n\t" \
    "ds_read_b64_tr_b16 a[218:219], v220 offset:50976\n\t" \
    "ds_read_b64_tr_b16 a[28:29], v220 offset:58240\n\t" \
    "ds_read_b64_tr_b16 a[30:31], v220 offset:59264\n\t" \
    "ds_read_b64_tr_b16 a[220:221], v220 offset:58272\n\t" \
    "ds_read_b64_tr_b16 a[222:223], v220 offset:59296\n\t" \
    "s_cmp_eq_u32 s83, 0\n\t" \
    "s_cbranch_scc1 .Lga2_%=\n\t" \
    "ds_read_b64_tr_b16 v[112:113], v224 offset:0\n\t" \
    "ds_read_b64_tr_b16 v[114:115], v224 offset:128\n\t" \
    "ds_read_b64_tr_b16 v[116:117], v224 offset:1024\n\t" \
    "ds_read_b64_tr_b16 v[118:119], v224 offset:1152\n\t" \
    "ds_read_b64_tr_b16 v[120:121], v224 offset:2048\n\t" \
    "ds_read_b64_tr_b16 v[122:123], v224 offset:2176\n\t" \
    "ds_read_b64_tr_b16 v[124:125], v224 offset:3072\n\t" \
    "ds_read_b64_tr_b16 v[126:127], v224 offset:3200\n\t" \
    "ds_read_b64_tr_b16 v[128:129], v224 offset:4096\n\t" \
    "ds_read_b64_tr_b16 v[130:131], v224 offset:4224\n\t" \
    "ds_read_b64_tr_b16 v[132:133], v224 offset:5120\n\t" \
    "ds_read_b64_tr_b16 v[134:135], v224 offset:5248\n\t" \
    "ds_read_b64_tr_b16 v[136:137], v224 offset:6144\n\t" \
    "ds_read_b64_tr_b16 v[138:139], v224 offset:6272\n\t" \
    "ds_read_b64_tr_b16 v[140:141], v224 offset:7168\n\t" \
    "ds_read_b64_tr_b16 v[142:143], v224 offset:7296\n\t" \
    "ds_read_b64_tr_b16 v[144:145], v224 offset:8192\n\t" \
    "ds_read_b64_tr_b16 v[146:147], v224 offset:8320\n\t" \
    "ds_read_b64_tr_b16 v[148:149], v224 offset:9216\n\t" \
    "ds_read_b64_tr_b16 v[150:151], v224 offset:9344\n\t" \
    "ds_read_b64_tr_b16 v[152:153], v224 offset:10240\n\t" \
    "ds_read_b64_tr_b16 v[154:155], v224 offset:10368\n\t" \
    "ds_read_b64_tr_b16 v[156:157], v224 offset:11264\n\t" \
    "ds_read_b64_tr_b16 v[158:159], v224 offset:11392\n\t" \
    "ds_read_b64_tr_b16 v[160:161], v224 offset:12288\n\t" \
    "ds_read_b64_tr_b16 v[162:163], v224 offset:12416\n\t" \
    "ds_read_b64_tr_b16 v[164:165], v224 offset:13312\n\t" \
    "ds_read_b64_tr_b16 v[166:167], v224 offset:13440\n\t" \
    "ds_read_b64_tr_b16 v[168:169], v224 offset:14336\n\t" \
    "ds_read_b64_tr_b16 v[170:171], v224 offset:14464\n\t" \
    "ds_read_b64_tr_b16 v[172:173], v224 offset:15360\n\t" \
    "ds_read_b64_tr_b16 v[174:175], v224 offset:15488\n\t" \
    ".Lga2_%=:\n\t" \
    "s_waitcnt vmcnt(6)\n\t" \
    "s_branch .Lga1_%=\n\t" \
    ".Lga_%=:\n\t" \
    "buffer_load_dwordx4 v[16:19], v191, s[8:11], s88 offen offset:0 sc1\n\t" \
    "buffer_load_dwordx4 v[20:23], v191, s[8:11], s88 offen offset:16 sc1\n\t" \
    "buffer_load_dwordx4 v[24:27], v191, s[8:11], s89 offen offset:0 sc1\n\t" \
    "buffer_load_dwordx4 v[28:31], v191, s[8:11], s89 offen offset:16 sc1\n\t" \
    "s_waitcnt vmcnt(0)\n\t" \
    ".Lga1_%=:\n\t" \
    "v_xor_b32 v16, s84, v16\n\t" \
    "v_xor_b32 v18, s84, v18\n\t" \
    "v_xor_b32 v20, s84, v20\n\t" \
    "v_xor_b32 v22, s84, v22\n\t" \
    "v_xor_b32 v24, s84, v24\n\t" \
    "v_xor_b32 v26, s84, v26\n\t" \
    "v_xor_b32 v28, s84, v28\n\t" \
    "v_xor_b32 v30, s84, v30\n\t" \
    "v_xor_b32 v17, s85, v17\n\t" \
    "v_xor_b32 v19, s85, v19\n\t" \
    "v_xor_b32 v21, s85, v21\n\t" \
    "v_xor_b32 v23, s85, v23\n\t" \
    "v_xor_b32 v25, s85, v25\n\t" \
    "v_xor_b32 v27, s85, v27\n\t" \
    "v_xor_b32 v29, s85, v29\n\t" \
    "v_xor_b32 v31, s85, v31\n\t" \
    "v_or3_b32 v32, v16, v18, v20\n\t" \
    "v_or3_b32 v33, v22, v24, v26\n\t" \
    "v_or3_b32 v34, v17, v19, v21\n\t" \
    "v_or3_b32 v35, v23, v25, v27\n\t" \
    "v_or3_b32 v32, v32, v28, v30\n\t" \
    "v_or3_b32 v34, v34, v29, v31\n\t" \
    "v_or_b32 v32, v32, v33\n\t" \
    "v_lshrrev_b32 v32, 24, v32\n\t" \
    "v_or3_b32 v32, v32, v34, v35\n\t" \
    "v_cmp_ne_u32 vcc, 0, v32\n\t" \
    "s_nop 0\n\t" \
    "s_cbranch_vccz .Lga_%=_ok\n\t" \
    "s_sub_u32 s62, s62, 1\n\t" \
    "s_cmp_lt_i32 s62, 0\n\t" \
    "s_cbranch_scc1 .Lga_%=_ok\n\t" \
    "s_sleep 4\n\t" \
    "s_branch .Lga_%=\n\t" \
    ".Lga_%=_ok:\n\t" \
    "v_lshlrev_b32 v16, 8, v16\n\t" \
    "v_lshlrev_b32 v18, 8, v18\n\t" \
    "v_lshlrev_b32 v20, 8, v20\n\t" \
    "v_lshlrev_b32 v22, 8, v22\n\t" \
    "v_lshlrev_b32 v24, 8, v24\n\t" \
    "v_lshlrev_b32 v26, 8, v26\n\t" \
    "v_lshlrev_b32 v28, 8, v28\n\t" \
    "v_lshlrev_b32 v30, 8, v30\n\t" \
    "v_add_f32 v192, v16, v24\n\t" \
    "v_add_f32 v193, v18, v26\n\t" \
    "v_add_f32 v194, v20, v28\n\t" \
    "v_add_f32 v195, v22, v30\n\t" \
    "v_cvt_pk_bf16_f32 v32, v192, v193\n\t" \
    "v_cvt_pk_bf16_f32 v33, v194, v195\n\t" \
    "v_lshlrev_b32 v192, 16, v32\n\t" \
    "v_and_b32 v193, 0xffff0000, v32\n\t" \
    "v_lshlrev_b32 v194, 16, v33\n\t" \
    "v_and_b32 v195, 0xffff0000, v33\n\t" \
    "s_cmp_lt_u32 s50, 2\n\t" \
    "s_cbranch_scc1 .Lsa_%=\n\t" \
    "v_add_f32 v192, 1.0, v192\n\t" \
    "v_add_f32 v193, 1.0, v193\n\t" \
    "v_add_f32 v194, 1.0, v194\n\t" \
    "v_add_f32 v195, 1.0, v195\n\t" \
    "v_cvt_pk_bf16_f32 v32, v192, v193\n\t" \
    "v_cvt_pk_bf16_f32 v33, v194, v195\n\t" \
    "v_lshlrev_b32 v192, 16, v32\n\t" \
    "v_and_b32 v193, 0xffff0000, v32\n\t" \
    "v_lshlrev_b32 v194, 16, v33\n\t" \
    "v_and_b32 v195, 0xffff0000, v33\n\t" \
    ".Lsa_%=:\n\t" \
    "ds_write_b128 v190, v[192:195]\n\t" \
    "s_waitcnt lgkmcnt(0)\n\t" \
    "s_barrier\n\t" \
      \
    "ds_read_b128 v[16:19], v188\n\t" \
    "ds_read_b128 v[24:27], v188 offset:2048\n\t" \
    "ds_read_b128 v[20:23], v188 offset:16\n\t" \
    "ds_read_b128 v[28:31], v188 offset:2064\n\t" \
    "s_waitcnt lgkmcnt(2)\n\t" \
    "v_pk_fma_f32 v[48:49], v[48:49], v[24:25], v[16:17]\n\t" \
    "v_pk_fma_f32 v[56:57], v[56:57], v[24:25], v[16:17]\n\t" \
    "v_pk_fma_f32 v[50:51], v[50:51], v[26:27], v[18:19]\n\t" \
    "v_pk_fma_f32 v[58:59], v[58:59], v[26:27], v[18:19]\n\t" \
    "s_waitcnt lgkmcnt(0)\n\t" \
    "v_pk_fma_f32 v[52:53], v[52:53], v[28:29], v[20:21]\n\t" \
    "v_pk_fma_f32 v[60:61], v[60:61], v[28:29], v[20:21]\n\t" \
    "v_pk_fma_f32 v[54:55], v[54:55], v[30:31], v[22:23]\n\t" \
    "v_pk_fma_f32 v[62:63], v[62:63], v[30:31], v[22:23]\n\t" \
    "v_cvt_pk_bf16_f32 v48, v48, v49\n\t" \
    "v_pk_fma_f32 v[64:65], v[64:65], v[24:25], v[16:17]\n\t" \
    "v_cvt_pk_bf16_f32 v49, v50, v51\n\t" \
    "v_pk_fma_f32 v[66:67], v[66:67], v[26:27], v[18:19]\n\t" \
    "v_cvt_pk_bf16_f32 v50, v52, v53\n\t" \
    "v_pk_fma_f32 v[68:69], v[68:69], v[28:29], v[20:21]\n\t" \
    "v_cvt_pk_bf16_f32 v51, v54, v55\n\t" \
    "v_pk_fma_f32 v[70:71], v[70:71], v[30:31], v[22:23]\n\t" \
    "ds_write_b128 v176, v[48:51]\n\t" \
    "v_cvt_pk_bf16_f32 v56, v56, v57\n\t" \
    "v_pk_fma_f32 v[72:73], v[72:73], v[24:25], v[16:17]\n\t" \
    "v_cvt_pk_bf16_f32 v57, v58, v59\n\t" \
    "v_pk_fma_f32 v[74:75], v[74:75], v[26:27], v[18:19]\n\t" \
    "v_cvt_pk_bf16_f32 v58, v60, v61\n\t" \
    "v_pk_fma_f32 v[76:77], v[76:77], v[28:29], v[20:21]\n\t" \
    "v_cvt_pk_bf16_f32 v59, v62, v63\n\t" \
    "v_pk_fma_f32 v[78:79], v[78:79], v[30:31], v[22:23]\n\t" \
    "ds_write_b128 v177, v[56:59]\n\t" \
    "v_cvt_pk_bf16_f32 v64, v64, v65\n\t" \
    "v_pk_fma_f32 v[80:81], v[80:81], v[24:25], v[16:17]\n\t" \
    "v_cvt_pk_bf16_f32 v65, v66, v67\n\t" \
    "v_pk_fma_f32 v[82:83], v[82:83], v[26:27], v[18:19]\n\t" \
    "v_cvt_pk_bf16_f32 v66, v68, v69\n\t" \
    "v_pk_fma_f32 v[84:85], v[84:85], v[28:29], v[20:21]\n\t" \
    "v_cvt_pk_bf16_f32 v67, v70, v71\n\t" \
    "v_pk_fma_f32 v[86:87], v[86:87], v[30:31], v[22:23]\n\t" \
    "ds_write_b128 v178, v[64:67]\n\t" \
    "v_cvt_pk_bf16_f32 v72, v72, v73\n\t" \
    "v_pk_fma_f32 v[88:89], v[88:89], v[24:25], v[16:17]\n\t" \
    "v_cvt_pk_bf16_f32 v73, v74, v75\n\t" \
    "v_pk_fma_f32 v[90:91], v[90:91], v[26:27], v[18:19]\n\t" \
    "v_cvt_pk_bf16_f32 v74, v76, v77\n\t" \
    "v_pk_fma_f32 v[92:93], v[92:93], v[28:29], v[20:21]\n\t" \
    "v_cvt_pk_bf16_f32 v75, v78, v79\n\t" \
    "v_pk_fma_f32 v[94:95], v[94:95], v[30:31], v[22:23]\n\t" \
    "ds_write_b128 v179, v[72:75]\n\t" \
    "v_cvt_pk_bf16_f32 v80, v80, v81\n\t" \
    "v_pk_fma_f32 v[96:97], v[96:97], v[24:25], v[16:17]\n\t" \
    "v_cvt_pk_bf16_f32 v81, v82, v83\n\t" \
    "v_pk_fma_f32 v[98:99], v[98:99], v[26:27], v[18:19]\n\t" \
    "v_cvt_pk_bf16_f32 v82, v84, v85\n\t" \
    "v_pk_fma_f32 v[100:101], v[100:101], v[28:29], v[20:21]\n\t" \
    "v_cvt_pk_bf16_f32 v83, v86, v87\n\t" \
    "v_pk_fma_f32 v[102:103], v[102:103], v[30:31], v[22:23]\n\t" \
    "ds_write_b128 v180, v[80:83]\n\t" \
    "v_cvt_pk_bf16_f32 v88, v88, v89\n\t" \
    "v_pk_fma_f32 v[104:105], v[104:105], v[24:25], v[16:17]\n\t" \
    "v_cvt_pk_bf16_f32 v89, v90, v91\n\t" \
    "v_pk_fma_f32 v[106:107], v[106:107], v[26:27], v[18:19]\n\t" \
    "v_cvt_pk_bf16_f32 v90, v92, v93\n\t" \
    "v_pk_fma_f32 v[108:109], v[108:109], v[28:29], v[20:21]\n\t" \
    "v_cvt_pk_bf16_f32 v91, v94, v95\n\t" \
    "v_pk_fma_f32 v[110:111], v[110:111], v[30:31], v[22:23]\n\t" \
    "ds_write_b128 v181, v[88:91]\n\t" \
    "v_cvt_pk_bf16_f32 v96, v96, v97\n\t" \
    "v_cvt_pk_bf16_f32 v97, v98, v99\n\t" \
    "v_cvt_pk_bf16_f32 v98, v100, v101\n\t" \
    "v_cvt_pk_bf16_f32 v99, v102, v103\n\t" \
    "ds_write_b128 v182, v[96:99]\n\t" \
    "v_cvt_pk_bf16_f32 v104, v104, v105\n\t" \
    "v_cvt_pk_bf16_f32 v105, v106, v107\n\t" \
    "v_cvt_pk_bf16_f32 v106, v108, v109\n\t" \
    "v_cvt_pk_bf16_f32 v107, v110, v111\n\t" \
    "ds_write_b128 v183, v[104:107]\n\t" \
    "s_waitcnt vmcnt(6)\n\t" \
    "s_waitcnt lgkmcnt(0)\n\t" \
    "s_barrier\n\t" \
    "ds_read_b128 v[16:19], v184 offset:0\n\t" \
    "ds_read_b128 v[80:83], v196 offset:0\n\t" \
    "ds_read_b128 v[20:23], v185 offset:1024\n\t" \
    "ds_read_b128 v[84:87], v197 offset:1024\n\t" \
    "ds_read_b128 v[32:35], v184 offset:4096\n\t" \
    "ds_read_b128 v[88:91], v196 offset:4096\n\t" \
    "ds_read_b128 v[36:39], v185 offset:5120\n\t" \
    "ds_read_b128 v[92:95], v197 offset:5120\n\t" \
    "ds_read_b128 v[48:51], v184 offset:8192\n\t" \
    "ds_read_b128 v[96:99], v196 offset:8192\n\t" \
    "s_waitcnt lgkmcnt(9)\n\t" \
    "v_mfma_f32_16x16x32_bf16 v[200:203], a[0:3], v[16:19], 0\n\t" \
    "ds_read_b128 v[52:55], v185 offset:9216\n\t" \
    "v_mfma_f32_16x16x32_bf16 v[204:207], a[192:195], v[16:19], 0\n\t" \
    "ds_read_b64_tr_b16 a[32:33], v221 offset:0\n\t" \
    "ds_read_b64_tr_b16 a[34:35], v221 offset:1024\n\t" \
    "s_waitcnt lgkmcnt(11)\n\t" \
    "v_mfma_f32_16x16x32_bf16 v[208:211], a[0:3], v[80:83], 0\n\t" \
    "ds_read_b128 v[100:103], v197 offset:9216\n\t" \
    "v_mfma_f32_16x16x32_bf16 v[212:215], a[192:195], v[80:83], 0\n\t" \
    "ds_read_b64_tr_b16 a[224:225], v221 offset:32\n\t" \
    "ds_read_b64_tr_b16 a[226:227], v221 offset:1056\n\t" \
    "s_waitcnt lgkmcnt(13)\n\t" \
    "v_mfma_f32_16x16x32_bf16 v[200:203], a[4:7], v[20:23], v[200:203]\n\t" \
    "ds_read_b128 v[64:67], v184 offset:12288\n\t" \
    "v_mfma_f32_16x16x32_bf16 v[204:207], a[196:199], v[20:23], v[204:207]\n\t" \
    "ds_read_b64_tr_b16 a[36:37], v221 offset:8320\n\t" \
    "ds_read_b64_tr_b16 a[38:39], v221 offset:9344\n\t" \
    "s_waitcnt lgkmcnt(15)\n\t" \
    "v_mfma_f32_16x16x32_bf16 v[208:211], a[4:7], v[84:87], v[208:211]\n\t" \
    "ds_read_b128 v[104:107], v196 offset:12288\n\t" \
    "v_mfma_f32_16x16x32_bf16 v[212:215], a[196:199], v[84:87], v[212:215]\n\t" \
    "ds_read_b64_tr_b16 a[228:229], v221 offset:8352\n\t" \
    "ds_read_b64_tr_b16 a[230:231], v221 offset:9376\n\t" \
    "s_waitcnt lgkmcnt(15)\n\t" \
    "v_mfma_f32_16x16x32_bf16 v[200:203], a[8:11], v[32:35], v[200:203]\n\t" \
    "ds_read_b128 v[68:71], v185 offset:13312\n\t" \
    "v_mfma_f32_16x16x32_bf16 v[204:207], a[200:203], v[32:35], v[204:207]\n\t" \
    "ds_read_b64_tr_b16 a[40:41], v221 offset:16640\n\t" \
    "ds_read_b64_tr_b16 a[42:43], v221 offset:17664\n\t" \
    "s_waitcnt lgkmcnt(15)\n\t" \
    "v_mfma_f32_16x16x32_bf16 v[208:211], a[8:11], v[88:91], v[208:211]\n\t" \
    "ds_read_b128 v[108:111], v197 offset:13312\n\t" \
    "v_mfma_f32_16x16x32_bf16 v[212:215], a[200:203], v[88:91], v[212:215]\n\t" \
    "ds_read_b64_tr_b16 a[232:233], v221 offset:16672\n\t" \
    "ds_read_b64_tr_b16 a[234:235], v221 offset:17696\n\t" \
    "s_waitcnt lgkmcnt(15)\n\t" \
    "v_mfma_f32_16x16x32_bf16 v[200:203], a[12:15], v[36:39], v[200:203]\n\t" \
    "ds_read_b128 v[24:27], v186 offset:2048\n\t" \
    "v_mfma_f32_16x16x32_bf16 v[204:207], a[204:207], v[36:39], v[204:207]\n\t" \
    "ds_read_b64_tr_b16 a[44:45], v221 offset:24960\n\t" \
    "ds_read_b64_tr_b16 a[46:47], v221 offset:25984\n\t" \
    "s_waitcnt lgkmcnt(15)\n\t" \
    "v_mfma_f32_16x16x32_bf16 v[208:211], a[12:15], v[92:95], v[208:211]\n\t" \
    "ds_read_b128 v[80:83], v198 offset:2048\n\t" \
    "v_mfma_f32_16x16x32_bf16 v[212:215], a[204:207], v[92:95], v[212:215]\n\t" \
    "ds_read_b64_tr_b16 a[236:237], v221 offset:24992\n\t" \
    "ds_read_b64_tr_b16 a[238:239], v221 offset:26016\n\t" \
    "s_waitcnt lgkmcnt(15)\n\t" \
    "v_mfma_f32_16x16x32_bf16 v[200:203], a[16:19], v[48:51], v[200:203]\n\t" \
    "ds_read_b128 v[28:31], v187 offset:3072\n\t" \
    "v_mfma_f32_16x16x32_bf16 v[204:207], a[208:211], v[48:51], v[204:207]\n\t" \
    "ds_read_b64_tr_b16 a[48:49], v221 offset:33280\n\t" \
    "ds_read_b64_tr_b16 a[50:51], v221 offset:34304\n\t" \
    "s_waitcnt lgkmcnt(15)\n\t" \
    "v_mfma_f32_16x16x32_bf16 v[208:211], a[16:19], v[96:99], v[208:211]\n\t" \
    "ds_read_b128 v[84:87], v199 offset:3072\n\t" \
    "v_mfma_f32_16x16x32_bf16 v[212:215], a[208:211], v[96:99], v[212:215]\n\t" \
    "ds_read_b64_tr_b16 a[240:241], v221 offset:33312\n\t" \
    "ds_read_b64_tr_b16 a[242:243], v221 offset:34336\n\t" \
    "s_waitcnt lgkmcnt(15)\n\t" \
    "v_mfma_f32_16x16x32_bf16 v[200:203], a[20:23], v[52:55], v[200:203]\n\t" \
    "ds_read_b128 v[40:43], v186 offset:6144\n\t" \
    "v_mfma_f32_16x16x32_bf16 v[204:207], a[212:215], v[52:55], v[204:207]\n\t" \
    "ds_read_b64_tr_b16 a[52:53], v221 offset:41600\n\t" \
    "ds_read_b64_tr_b16 a[54:55], v221 offset:42624\n\t" \
    "s_waitcnt lgkmcnt(15)\n\t" \
    "v_mfma_f32_16x16x32_bf16 v[208:211], a[20:23], v[100:103], v[208:211]\n\t" \
    "ds_read_b128 v[88:91], v198 offset:6144\n\t" \
    "v_mfma_f32_16x16x32_bf16 v[212:215], a[212:215], v[100:103], v[212:215]\n\t" \
    "ds_read_b64_tr_b16 a[244:245], v221 offset:41632\n\t" \
    "ds_read_b64_tr_b16 a[246:247], v221 offset:42656\n\t" \
    "s_waitcnt lgkmcnt(15)\n\t" \
    "v_mfma_f32_16x16x32_bf16 v[200:203], a[24:27], v[64:67], v[200:203]\n\t" \
    "ds_read_b128 v[44:47], v187 offset:7168\n\t" \
    "v_mfma_f32_16x16x32_bf16 v[204:207], a[216:219], v[64:67], v[204:207]\n\t" \
    "ds_read_b64_tr_b16 a[56:57], v221 offset:49920\n\t" \
    "s_waitcnt lgkmcnt(15)\n\t" \
    "v_mfma_f32_16x16x32_bf16 v[208:211], a[24:27], v[104:107], v[208:211]\n\t" \
    "ds_read_b128 v[92:95], v199 offset:7168\n\t" \
    "v_mfma_f32_16x16x32_bf16 v[212:215], a[216:219], v[104:107], v[212:215]\n\t" \
    "ds_read_b64_tr_b16 a[58:59], v221 offset:50944\n\t" \
    "s_waitcnt lgkmcnt(15)\n\t" \
    "v_mfma_f32_16x16x32_bf16 v[200:203], a[28:31], v[68:71], v[200:203]\n\t" \
    "ds_read_b128 v[56:59], v186 offset:10240\n\t" \
    "v_mfma_f32_16x16x32_bf16 v[204:207], a[220:223], v[68:71], v[204:207]\n\t" \
    "ds_read_b64_tr_b16 a[248:249], v221 offset:49952\n\t" \
    "s_waitcnt lgkmcnt(15)\n\t" \
    "v_mfma_f32_16x16x32_bf16 v[208:211], a[28:31], v[108:111], v[208:211]\n\t" \
    "ds_read_b128 v[96:99], v198 offset:10240\n\t" \
    "v_mfma_f32_16x16x32_bf16 v[212:215], a[220:223], v[108:111], v[212:215]\n\t" \
    "ds_read_b64_tr_b16 a[250:251], v221 offset:50976\n\t" \
    "s_waitcnt lgkmcnt(15)\n\t" \
    "v_mfma_f32_16x16x32_bf16 v[200:203], a[32:35], v[24:27], v[200:203]\n\t" \
    "ds_read_b128 v[60:63], v187 offset:11264\n\t" \
    "v_mfma_f32_16x16x32_bf16 v[204:207], a[224:227], v[24:27], v[204:207]\n\t" \
    "ds_read_b64_tr_b16 a[60:61], v221 offset:58240\n\t" \
    "s_waitcnt lgkmcnt(15)\n\t" \
    "v_mfma_f32_16x16x32_bf16 v[208:211], a[32:35], v[80:83], v[208:211]\n\t" \
    "ds_read_b128 v[100:103], v199 offset:11264\n\t" \
    "v_mfma_f32_16x16x32_bf16 v[212:215], a[224:227], v[80:83], v[212:215]\n\t" \
    "ds_read_b64_tr_b16 a[62:63], v221 offset:59264\n\t" \
    "s_waitcnt lgkmcnt(15)\n\t" \
    "v_mfma_f32_16x16x32_bf16 v[200:203], a[36:39], v[28:31], v[200:203]\n\t" \
    "ds_read_b128 v[72:75], v186 offset:14336\n\t" \
    "v_mfma_f32_16x16x32_bf16 v[204:207], a[228:231], v[28:31], v[204:207]\n\t" \
    "ds_read_b64_tr_b16 a[252:253], v221 offset:58272\n\t" \
    "s_waitcnt lgkmcnt(15)\n\t" \
    "v_mfma_f32_16x16x32_bf16 v[208:211], a[36:39], v[84:87], v[208:211]\n\t" \
    "ds_read_b128 v[104:107], v198 offset:14336\n\t" \
    "v_mfma_f32_16x16x32_bf16 v[212:215], a[228:231], v[84:87], v[212:215]\n\t" \
    "ds_read_b64_tr_b16 a[254:255], v221 offset:59296\n\t" \
    "s_waitcnt lgkmcnt(15)\n\t" \
    "v_mfma_f32_16x16x32_bf16 v[200:203], a[40:43], v[40:43], v[200:203]\n\t" \
    "ds_read_b128 v[76:79], v187 offset:15360\n\t" \
    "v_mfma_f32_16x16x32_bf16 v[204:207], a[232:235], v[40:43], v[204:207]\n\t" \
    "s_waitcnt lgkmcnt(15)\n\t" \
    "v_mfma_f32_16x16x32_bf16 v[208:211], a[40:43], v[88:91], v[208:211]\n\t" \
    "ds_read_b128 v[108:111], v199 offset:15360\n\t" \
    "v_mfma_f32_16x16x32_bf16 v[212:215], a[232:235], v[88:91], v[212:215]\n\t" \
    "s_waitcnt lgkmcnt(15)\n\t" \
    "v_mfma_f32_16x16x32_bf16 v[200:203], a[44:47], v[44:47], v[200:203]\n\t" \
    "v_mfma_f32_16x16x32_bf16 v[204:207], a[236:239], v[44:47], v[204:207]\n\t" \
    "s_waitcnt lgkmcnt(15)\n\t" \
    "v_mfma_f32_16x16x32_bf16 v[208:211], a[44:47], v[92:95], v[208:211]\n\t" \
    "v_mfma_f32_16x16x32_bf16 v[212:215], a[236:239], v[92:95], v[212:215]\n\t" \
    "s_waitcnt lgkmcnt(13)\n\t" \
    "v_mfma_f32_16x16x32_bf16 v[200:203], a[48:51], v[56:59], v[200:203]\n\t" \
    "v_mfma_f32_16x16x32_bf16 v[204:207], a[240:243], v[56:59], v[204:207]\n\t" \
    "s_waitcnt lgkmcnt(11)\n\t" \
    "v_mfma_f32_16x16x32_bf16 v[208:211], a[48:51], v[96:99], v[208:211]\n\t" \
    "v_mfma_f32_16x16x32_bf16 v[212:215], a[240:243], v[96:99], v[212:215]\n\t" \
    "s_waitcnt lgkmcnt(9)\n\t" \
    "v_mfma_f32_16x16x32_bf16 v[200:203], a[52:55], v[60:63], v[200:203]\n\t" \
    "v_mfma_f32_16x16x32_bf16 v[204:207], a[244:247], v[60:63], v[204:207]\n\t" \
    "s_waitcnt lgkmcnt(7)\n\t" \
    "v_mfma_f32_16x16x32_bf16 v[208:211], a[52:55], v[100:103], v[208:211]\n\t" \
    "v_mfma_f32_16x16x32_bf16 v[212:215], a[244:247], v[100:103], v[212:215]\n\t" \
    "s_waitcnt lgkmcnt(5)\n\t" \
    "v_mfma_f32_16x16x32_bf16 v[200:203], a[56:59], v[72:75], v[200:203]\n\t" \
    "v_mfma_f32_16x16x32_bf16 v[204:207], a[248:251], v[72:75], v[204:207]\n\t" \
    "s_waitcnt lgkmcnt(3)\n\t" \
    "v_mfma_f32_16x16x32_bf16 v[208:211], a[56:59], v[104:107], v[208:211]\n\t" \
    "v_mfma_f32_16x16x32_bf16 v[212:215], a[248:251], v[104:107], v[212:215]\n\t" \
    "s_waitcnt lgkmcnt(2)\n\t" \
    "s_waitcnt lgkmcnt(1)\n\t" \
    "v_mfma_f32_16x16x32_bf16 v[200:203], a[60:63], v[76:79], v[200:203]\n\t" \
    "v_mfma_f32_16x16x32_bf16 v[204:207], a[252:255], v[76:79], v[204:207]\n\t" \
    "s_waitcnt lgkmcnt(0)\n\t" \
    "v_mfma_f32_16x16x32_bf16 v[208:211], a[60:63], v[108:111], v[208:211]\n\t" \
    "v_mfma_f32_16x16x32_bf16 v[212:215], a[252:255], v[108:111], v[212:215]\n\t" \
    "v_mov_b32 v93, 0\n\t" \
    "s_cmp_eq_u32 s83, 0\n\t" \
    "s_cbranch_scc1 .Lnkv_%=\n\t" \
    "s_cmp_lt_u32 s52, 2\n\t" \
    "s_cbranch_scc0 .Lisv_%=\n\t" \
    "v_mfma_f32_16x16x32_bf16 v[96:99], v[112:115], v[16:19], 0\n\t" \
    "v_mfma_f32_16x16x32_bf16 v[96:99], v[116:119], v[20:23], v[96:99]\n\t" \
    "v_mfma_f32_16x16x32_bf16 v[96:99], v[120:123], v[24:27], v[96:99]\n\t" \
    "v_mfma_f32_16x16x32_bf16 v[96:99], v[124:127], v[28:31], v[96:99]\n\t" \
    "v_mfma_f32_16x16x32_bf16 v[96:99], v[128:131], v[32:35], v[96:99]\n\t" \
    "v_mfma_f32_16x16x32_bf16 v[96:99], v[132:135], v[36:39], v[96:99]\n\t" \
    "v_mul_f32 v200, s94, v200\n\t" \
    "v_mul_f32 v201, s94, v201\n\t" \
    "v_mfma_f32_16x16x32_bf16 v[96:99], v[136:139], v[40:43], v[96:99]\n\t" \
    "v_mul_f32 v202, s94, v202\n\t" \
    "v_mul_f32 v203, s94, v203\n\t" \
    "v_mfma_f32_16x16x32_bf16 v[96:99], v[140:143], v[44:47], v[96:99]\n\t" \
    "v_mul_f32 v204, s94, v204\n\t" \
    "v_mul_f32 v205, s94, v205\n\t" \
    "v_mfma_f32_16x16x32_bf16 v[96:99], v[144:147], v[48:51], v[96:99]\n\t" \
    "v_mul_f32 v206, s94, v206\n\t" \
    "v_mul_f32 v207, s94, v207\n\t" \
    "v_mfma_f32_16x16x32_bf16 v[96:99], v[148:151], v[52:55], v[96:99]\n\t" \
    "v_mul_f32 v208, s94, v208\n\t" \
    "v_mul_f32 v209, s94, v209\n\t" \
    "v_mfma_f32_16x16x32_bf16 v[96:99], v[152:155], v[56:59], v[96:99]\n\t" \
    "v_mul_f32 v210, s94, v210\n\t" \
    "v_mul_f32 v211, s94, v211\n\t" \
    "v_mfma_f32_16x16x32_bf16 v[96:99], v[156:159], v[60:63], v[96:99]\n\t" \
    "v_mul_f32 v212, s94, v212\n\t" \
    "v_mul_f32 v213, s94, v213\n\t" \
    "v_mfma_f32_16x16x32_bf16 v[96:99], v[160:163], v[64:67], v[96:99]\n\t" \
    "v_mul_f32 v214, s94, v214\n\t" \
    "v_mul_f32 v215, s94, v215\n\t" \
    "v_mfma_f32_16x16x32_bf16 v[96:99], v[164:167], v[68:71], v[96:99]\n\t" \
    "v_cvt_pk_bf16_f32 v84, v200, v201\n\t" \
    "v_cvt_pk_bf16_f32 v85, v202, v203\n\t" \
    "v_mfma_f32_16x16x32_bf16 v[96:99], v[168:171], v[72:75], v[96:99]\n\t" \
    "v_cvt_pk_bf16_f32 v86, v208, v209\n\t" \
    "v_cvt_pk_bf16_f32 v87, v210, v211\n\t" \
    "v_mfma_f32_16x16x32_bf16 v[96:99], v[172:175], v[76:79], v[96:99]\n\t" \
    "v_cvt_pk_bf16_f32 v88, v204, v205\n\t" \
    "v_cvt_pk_bf16_f32 v89, v206, v207\n\t" \
    "v_cvt_pk_bf16_f32 v90, v212, v213\n\t" \
    "v_cvt_pk_bf16_f32 v91, v214, v215\n\t" \
    "v_permlane16_swap_b32 v84, v86\n\t" \
    "v_permlane16_swap_b32 v85, v87\n\t" \
    "buffer_store_dwordx4 v[84:87], v216, s[8:11], s80 offen offset:0 sc1\n\t" \
    "v_permlane16_swap_b32 v88, v90\n\t" \
    "v_permlane16_swap_b32 v89, v91\n\t" \
    "buffer_store_dwordx4 v[88:91], v216, s[8:11], s80 offen offset:128 sc1\n\t" \
    "v_cvt_pk_bf16_f32 v102, v96, v97\n\t" \
    "v_cvt_pk_bf16_f32 v103, v98, v99\n\t" \
    "buffer_store_dwordx2 v[102:103], v217, s[8:11], s95 offen sc1\n\t" \
    "v_mul_f32 v93, v96, v96\n\t" \
    "v_fmac_f32 v93, v97, v97\n\t" \
    "v_fmac_f32 v93, v98, v98\n\t" \
    "v_fmac_f32 v93, v99, v99\n\t" \
    "v_mov_b32 v94, v93\n\t" \
    "s_nop 1\n\t" \
    "v_permlane16_swap_b32 v93, v94\n\t" \
    "v_add_f32 v93, v93, v94\n\t" \
    "v_mov_b32 v94, v93\n\t" \
    "s_nop 1\n\t" \
    "v_permlane32_swap_b32 v93, v94\n\t" \
    "v_add_f32 v93, v93, v94\n\t" \
    "s_branch .Lkvd_%=\n\t" \
    ".Lisv_%=:\n\t" \
    "v_mfma_f32_16x16x32_bf16 v[96:99], v[16:19], v[112:115], 0\n\t" \
    "v_mfma_f32_16x16x32_bf16 v[96:99], v[20:23], v[116:119], v[96:99]\n\t" \
    "v_mfma_f32_16x16x32_bf16 v[96:99], v[24:27], v[120:123], v[96:99]\n\t" \
    "v_mfma_f32_16x16x32_bf16 v[96:99], v[28:31], v[124:127], v[96:99]\n\t" \
    "v_mfma_f32_16x16x32_bf16 v[96:99], v[32:35], v[128:131], v[96:99]\n\t" \
    "v_mfma_f32_16x16x32_bf16 v[96:99], v[36:39], v[132:135], v[96:99]\n\t" \
    "v_mul_f32 v200, s94, v200\n\t" \
    "v_mul_f32 v201, s94, v201\n\t" \
    "v_mfma_f32_16x16x32_bf16 v[96:99], v[40:43], v[136:139], v[96:99]\n\t" \
    "v_mul_f32 v202, s94, v202\n\t" \
    "v_mul_f32 v203, s94, v203\n\t" \
    "v_mfma_f32_16x16x32_bf16 v[96:99], v[44:47], v[140:143], v[96:99]\n\t" \
    "v_mul_f32 v204, s94, v204\n\t" \
    "v_mul_f32 v205, s94, v205\n\t" \
    "v_mfma_f32_16x16x32_bf16 v[96:99], v[48:51], v[144:147], v[96:99]\n\t" \
    "v_mul_f32 v206, s94, v206\n\t" \
    "v_mul_f32 v207, s94, v207\n\t" \
    "v_mfma_f32_16x16x32_bf16 v[96:99], v[52:55], v[148:151], v[96:99]\n\t" \
    "v_mul_f32 v208, s94, v208\n\t" \
    "v_mul_f32 v209, s94, v209\n\t" \
    "v_mfma_f32_16x16x32_bf16 v[96:99], v[56:59], v[152:155], v[96:99]\n\t" \
    "v_mul_f32 v210, s94, v210\n\t" \
    "v_mul_f32 v211, s94, v211\n\t" \
    "v_mfma_f32_16x16x32_bf16 v[96:99], v[60:63], v[156:159], v[96:99]\n\t" \
    "v_mul_f32 v212, s94, v212\n\t" \
    "v_mul_f32 v213, s94, v213\n\t" \
    "v_mfma_f32_16x16x32_bf16 v[96:99], v[64:67], v[160:163], v[96:99]\n\t" \
    "v_mul_f32 v214, s94, v214\n\t" \
    "v_mul_f32 v215, s94, v215\n\t" \
    "v_mfma_f32_16x16x32_bf16 v[96:99], v[68:71], v[164:167], v[96:99]\n\t" \
    "v_cvt_pk_bf16_f32 v84, v200, v201\n\t" \
    "v_cvt_pk_bf16_f32 v85, v202, v203\n\t" \
    "v_mfma_f32_16x16x32_bf16 v[96:99], v[72:75], v[168:171], v[96:99]\n\t" \
    "v_cvt_pk_bf16_f32 v86, v208, v209\n\t" \
    "v_cvt_pk_bf16_f32 v87, v210, v211\n\t" \
    "v_mfma_f32_16x16x32_bf16 v[96:99], v[76:79], v[172:175], v[96:99]\n\t" \
    "v_cvt_pk_bf16_f32 v88, v204, v205\n\t" \
    "v_cvt_pk_bf16_f32 v89, v206, v207\n\t" \
    "v_cvt_pk_bf16_f32 v90, v212, v213\n\t" \
    "v_cvt_pk_bf16_f32 v91, v214, v215\n\t" \
    "v_permlane16_swap_b32 v84, v86\n\t" \
    "v_permlane16_swap_b32 v85, v87\n\t" \
    "buffer_store_dwordx4 v[84:87], v216, s[8:11], s80 offen offset:0 sc1\n\t" \
    "v_permlane16_swap_b32 v88, v90\n\t" \
    "v_permlane16_swap_b32 v89, v91\n\t" \
    "buffer_store_dwordx4 v[88:91], v216, s[8:11], s80 offen offset:128 sc1\n\t" \
    "v_cvt_pk_bf16_f32 v102, v96, v97\n\t" \
    "v_cvt_pk_bf16_f32 v103, v98, v99\n\t" \
    "buffer_store_dwordx2 v[102:103], v217, s[8:11], s95 offen sc1\n\t" \
    "s_branch .Lkvd_%=\n\t" \
    ".Lnkv_%=:\n\t" \
    "s_nop 5\n\t" \
    "v_mul_f32 v200, s94, v200\n\t" \
    "v_mul_f32 v201, s94, v201\n\t" \
    "v_mul_f32 v202, s94, v202\n\t" \
    "v_mul_f32 v203, s94, v203\n\t" \
    "v_mul_f32 v204, s94, v204\n\t" \
    "v_mul_f32 v205, s94, v205\n\t" \
    "v_mul_f32 v206, s94, v206\n\t" \
    "v_mul_f32 v207, s94, v207\n\t" \
    "v_mul_f32 v208, s94, v208\n\t" \
    "v_mul_f32 v209, s94, v209\n\t" \
    "v_mul_f32 v210, s94, v210\n\t" \
    "v_mul_f32 v211, s94, v211\n\t" \
    "v_mul_f32 v212, s94, v212\n\t" \
    "v_mul_f32 v213, s94, v213\n\t" \
    "v_mul_f32 v214, s94, v214\n\t" \
    "v_mul_f32 v215, s94, v215\n\t" \
    "v_cvt_pk_bf16_f32 v84, v200, v201\n\t" \
    "v_cvt_pk_bf16_f32 v85, v202, v203\n\t" \
    "v_cvt_pk_bf16_f32 v86, v208, v209\n\t" \
    "v_cvt_pk_bf16_f32 v87, v210, v211\n\t" \
    "v_cvt_pk_bf16_f32 v88, v204, v205\n\t" \
    "v_cvt_pk_bf16_f32 v89, v206, v207\n\t" \
    "v_cvt_pk_bf16_f32 v90, v212, v213\n\t" \
    "v_cvt_pk_bf16_f32 v91, v214, v215\n\t" \
    "v_permlane16_swap_b32 v84, v86\n\t" \
    "v_permlane16_swap_b32 v85, v87\n\t" \
    "buffer_store_dwordx4 v[84:87], v216, s[8:11], s80 offen offset:0 sc1\n\t" \
    "v_permlane16_swap_b32 v88, v90\n\t" \
    "v_permlane16_swap_b32 v89, v91\n\t" \
    "buffer_store_dwordx4 v[88:91], v216, s[8:11], s80 offen offset:128 sc1\n\t" \
    ".Lkvd_%=:\n\t" \
    "s_cmp_lt_u32 s49, 24\n\t" \
    "s_cbranch_scc1 .Lnowo3_%=\n\t" \
    "ds_read_b64_tr_b16 v[220:221], v219 offset:0\n\t" \
    "ds_read_b64_tr_b16 v[222:223], v219 offset:1024\n\t" \
    "ds_read_b64_tr_b16 v[224:225], v219 offset:8\n\t" \
    "ds_read_b64_tr_b16 v[226:227], v219 offset:1032\n\t" \
    "s_lshr_b32 s88, s63, 3\n\t" \
    "s_lshl_b32 s88, s88, 2\n\t" \
    "s_and_b32 s89, s63, 3\n\t" \
    "s_add_u32 s88, s88, s89\n\t" \
    "s_lshl_b32 s88, s88, 1\n\t" \
    "s_bfe_u32 s89, s63, 0x10002\n\t" \
    "s_add_u32 s88, s88, s89\n\t" \
    "s_lshl_b32 s88, s88, 13\n\t" \
    "s_lshl_b32 s89, s50, 11\n\t" \
    "s_add_u32 s88, s88, s89\n\t" \
    "s_add_u32 s88, s88, 393216\n\t" \
    "s_waitcnt lgkmcnt(0)\n\t" \
    "buffer_store_dwordx4 v[220:223], v2, s[8:11], s88 offen sc1\n\t" \
    "buffer_store_dwordx4 v[224:227], v2, s[8:11], s88 offen offset:1024 sc1\n\t" \
    ".Lnowo3_%=:\n\t" \
    "v_mul_f32 v104, v200, v200\n\t" \
    "v_mul_f32 v105, v204, v204\n\t" \
    "v_mul_f32 v106, v208, v208\n\t" \
    "v_mul_f32 v107, v212, v212\n\t" \
    "v_fmac_f32 v104, v201, v201\n\t" \
    "v_fmac_f32 v105, v205, v205\n\t" \
    "v_fmac_f32 v106, v209, v209\n\t" \
    "v_fmac_f32 v107, v213, v213\n\t" \
    "v_fmac_f32 v104, v202, v202\n\t" \
    "v_fmac_f32 v105, v206, v206\n\t" \
    "v_fmac_f32 v106, v210, v210\n\t" \
    "v_fmac_f32 v107, v214, v214\n\t" \
    "v_fmac_f32 v104, v203, v203\n\t" \
    "v_fmac_f32 v105, v207, v207\n\t" \
    "v_fmac_f32 v106, v211, v211\n\t" \
    "v_fmac_f32 v107, v215, v215\n\t" \
    "s_nop 0\n\t" \
    "v_permlane32_swap_b32 v104, v106\n\t" \
    "v_permlane32_swap_b32 v105, v107\n\t" \
    "v_add_f32 v104, v104, v106\n\t" \
    "v_add_f32 v105, v105, v107\n\t" \
    "s_nop 1\n\t" \
    "v_permlane16_swap_b32 v104, v105\n\t" \
    "v_add_f32 v92, v104, v105\n\t" \
    "v_and_b32 v92, 0x7fffffff, v92\n\t" \
    "v_and_b32 v93, 0x7fffffff, v93\n\t" \
    "s_nop 0\n\t" \
    "v_max_u32_dpp v92, v92, v92 row_ror:8 row_mask:0xf bank_mask:0xf\n\t" \
    "v_max_u32_dpp v93, v93, v93 row_ror:8 row_mask:0xf bank_mask:0xf\n\t" \
    "s_nop 0\n\t" \
    "v_max_u32_dpp v92, v92, v92 row_ror:4 row_mask:0xf bank_mask:0xf\n\t" \
    "v_max_u32_dpp v93, v93, v93 row_ror:4 row_mask:0xf bank_mask:0xf\n\t" \
    "s_nop 0\n\t" \
    "v_max_u32_dpp v92, v92, v92 row_ror:2 row_mask:0xf bank_mask:0xf\n\t" \
    "v_max_u32_dpp v93, v93, v93 row_ror:2 row_mask:0xf bank_mask:0xf\n\t" \
    "s_nop 0\n\t" \
    "v_max_u32_dpp v92, v92, v92 row_ror:1 row_mask:0xf bank_mask:0xf\n\t" \
    "v_max_u32_dpp v93, v93, v93 row_ror:1 row_mask:0xf bank_mask:0xf\n\t" \
    "v_mov_b32 v94, v92\n\t" \
    "s_nop 1\n\t" \
    "v_permlane16_swap_b32 v92, v94\n\t" \
    "v_max_u32 v92, v92, v94\n\t" \
    "v_mov_b32 v94, v92\n\t" \
    "s_nop 1\n\t" \
    "v_permlane32_swap_b32 v92, v94\n\t" \
    "v_max_u32 v92, v92, v94\n\t" \
    "s_lshl_b32 s88, s50, 3\n\t" \
    "s_add_u32 s88, s88, 160000\n\t" \
    "v_mov_b32 v94, s88\n\t" \
    "ds_write_b64 v94, v[92:93]\n\t" \
    "v_mov_b32 v94, 160000\n\t" \
    "s_waitcnt lgkmcnt(0)\n\t" \
    "s_barrier\n\t" \
    "ds_read_b128 v[96:99], v94\n\t" \
    "ds_read_b128 v[100:103], v94 offset:16\n\t" \
    "s_waitcnt lgkmcnt(0)\n\t" \
    "v_max3_u32 v96, v96, v98, v100\n\t" \
    "v_max3_u32 v98, v97, v99, v101\n\t" \
    "v_max_u32 v96, v96, v102\n\t" \
    "v_max_u32 v98, v98, v103\n\t" \
    "v_min_u32 v96, 0x7f800000, v96\n\t" \
    "v_min_u32 v98, 0x7f800000, v98\n\t" \
    "v_add_u32 v96, 0xffff, v96\n\t" \
    "v_add_u32 v98, 0xffff, v98\n\t" \
    "v_lshrrev_b32 v96, 16, v96\n\t" \
    "v_lshrrev_b32 v98, 16, v98\n\t" \
    "v_or_b32 v96, s56, v96\n\t" \
    "v_or_b32 v98, s56, v98\n\t" \
    "v_mov_b32 v97, s57\n\t" \
    "v_mov_b32 v99, s57\n\t" \
    "s_waitcnt vmcnt(0)\n\t" \
    "s_barrier\n\t" \
    "s_mov_b64 exec, s[92:93]\n\t" \
    "buffer_store_dwordx4 v[96:99], v2, s[8:11], s82 offen sc1\n\t" \
    "s_mov_b64 exec, -1\n\t" \
    "s_lshl_b32 s88, s50, 11\n\t" \
    "s_add_u32 s88, s88, 147456\n\t" \
    "v_add_u32 v17, s88, v2\n\t" \
    "ds_write_b128 v17, v[248:251]\n\t" \
    "ds_write_b128 v17, v[252:255] offset:1024\n\t" \
    "v_mov_b32 v36, 0\n\t" \
    "v_and_b32 v35, 31, v1\n\t" \
    "v_lshlrev_b32 v35, 2, v35\n\t" \
    "v_add_u32 v37, 157312, v35\n\t" \
    "ds_write_b32 v37, v36\n\t" \
    "v_add_u32 v37, 160064, v35\n\t" \
    "ds_write_b32 v37, v36\n\t" \
    "v_lshlrev_b32 v56, 16, v232\n\t" \
    "v_and_b32 v57, 0xffff0000, v232\n\t" \
    "v_lshlrev_b32 v58, 16, v233\n\t" \
    "v_and_b32 v59, 0xffff0000, v233\n\t" \
    "v_lshlrev_b32 v60, 16, v234\n\t" \
    "v_and_b32 v61, 0xffff0000, v234\n\t" \
    "v_lshlrev_b32 v62, 16, v235\n\t" \
    "v_and_b32 v63, 0xffff0000, v235\n\t" \
    "v_pk_fma_f32 v[48:49], v[56:57], v[12:13], 0 op_sel_hi:[1,0,0]\n\t" \
    "v_pk_fma_f32 v[50:51], v[58:59], v[12:13], 0 op_sel_hi:[1,0,0]\n\t" \
    "v_pk_fma_f32 v[52:53], v[60:61], v[12:13], 0 op_sel_hi:[1,0,0]\n\t" \
    "v_pk_fma_f32 v[54:55], v[62:63], v[12:13], 0 op_sel_hi:[1,0,0]\n\t" \
    "v_lshlrev_b32 v56, 16, v236\n\t" \
    "v_and_b32 v57, 0xffff0000, v236\n\t" \
    "v_lshlrev_b32 v58, 16, v237\n\t" \
    "v_and_b32 v59, 0xffff0000, v237\n\t" \
    "v_lshlrev_b32 v60, 16, v238\n\t" \
    "v_and_b32 v61, 0xffff0000, v238\n\t" \
    "v_lshlrev_b32 v62, 16, v239\n\t" \
    "v_and_b32 v63, 0xffff0000, v239\n\t" \
    "v_pk_fma_f32 v[48:49], v[56:57], v[12:13], v[48:49] op_sel:[0,1,0] op_sel_hi:[1,1,1]\n\t" \
    "v_pk_fma_f32 v[50:51], v[58:59], v[12:13], v[50:51] op_sel:[0,1,0] op_sel_hi:[1,1,1]\n\t" \
    "v_pk_fma_f32 v[52:53], v[60:61], v[12:13], v[52:53] op_sel:[0,1,0] op_sel_hi:[1,1,1]\n\t" \
    "v_pk_fma_f32 v[54:55], v[62:63], v[12:13], v[54:55] op_sel:[0,1,0] op_sel_hi:[1,1,1]\n\t" \
    "v_lshlrev_b32 v56, 16, v240\n\t" \
    "v_and_b32 v57, 0xffff0000, v240\n\t" \
    "v_lshlrev_b32 v58, 16, v241\n\t" \
    "v_and_b32 v59, 0xffff0000, v241\n\t" \
    "v_lshlrev_b32 v60, 16, v242\n\t" \
    "v_and_b32 v61, 0xffff0000, v242\n\t" \
    "v_lshlrev_b32 v62, 16, v243\n\t" \
    "v_and_b32 v63, 0xffff0000, v243\n\t" \
    "v_pk_fma_f32 v[48:49], v[56:57], v[14:15], v[48:49] op_sel_hi:[1,0,1]\n\t" \
    "v_pk_fma_f32 v[50:51], v[58:59], v[14:15], v[50:51] op_sel_hi:[1,0,1]\n\t" \
    "v_pk_fma_f32 v[52:53], v[60:61], v[14:15], v[52:53] op_sel_hi:[1,0,1]\n\t" \
    "v_pk_fma_f32 v[54:55], v[62:63], v[14:15], v[54:55] op_sel_hi:[1,0,1]\n\t" \
    "v_lshlrev_b32 v56, 16, v244\n\t" \
    "v_and_b32 v57, 0xffff0000, v244\n\t" \
    "v_lshlrev_b32 v58, 16, v245\n\t" \
    "v_and_b32 v59, 0xffff0000, v245\n\t" \
    "v_lshlrev_b32 v60, 16, v246\n\t" \
    "v_and_b32 v61, 0xffff0000, v246\n\t" \
    "v_lshlrev_b32 v62, 16, v247\n\t" \
    "v_and_b32 v63, 0xffff0000, v247\n\t" \
    "v_pk_fma_f32 v[48:49], v[56:57], v[14:15], v[48:49] op_sel:[0,1,0] op_sel_hi:[1,1,1]\n\t" \
    "v_pk_fma_f32 v[50:51], v[58:59], v[14:15], v[50:51] op_sel:[0,1,0] op_sel_hi:[1,1,1]\n\t" \
    "v_pk_fma_f32 v[52:53], v[60:61], v[14:15], v[52:53] op_sel:[0,1,0] op_sel_hi:[1,1,1]\n\t" \
    "v_pk_fma_f32 v[54:55], v[62:63], v[14:15], v[54:55] op_sel:[0,1,0] op_sel_hi:[1,1,1]\n\t" \
    "s_nop 0\n\t" \
    "v_permlane32_swap_b32 v48, v52\n\t" \
    "v_permlane32_swap_b32 v49, v53\n\t" \
    "v_permlane32_swap_b32 v50, v54\n\t" \
    "v_permlane32_swap_b32 v51, v55\n\t" \
    "v_add_f32 v48, v48, v52\n\t" \
    "v_add_f32 v49, v49, v53\n\t" \
    "v_add_f32 v50, v50, v54\n\t" \
    "v_add_f32 v51, v51, v55\n\t" \
    "s_nop 0\n\t" \
    "v_permlane16_swap_b32 v48, v50\n\t" \
    "v_permlane16_swap_b32 v49, v51\n\t" \
    "v_add_f32 v48, v48, v50\n\t" \
    "v_add_f32 v49, v49, v51\n\t" \
    "s_nop 0\n\t" \
    "v_add_f32_dpp v48, v48, v48 row_ror:8 row_mask:0xf bank_mask:0xf\n\t" \
    "v_add_f32_dpp v49, v49, v49 row_ror:8 row_mask:0xf bank_mask:0xf\n\t" \
    "s_nop 0\n\t" \
    "v_add_f32_dpp v48, v48, v48 row_ror:4 row_mask:0xf bank_mask:0xf\n\t" \
    "v_add_f32_dpp v49, v49, v49 row_ror:4 row_mask:0xf bank_mask:0xf\n\t" \
    "s_nop 0\n\t" \
    "v_add_f32_dpp v48, v48, v48 row_ror:2 row_mask:0xf bank_mask:0xf\n\t" \
    "v_add_f32_dpp v49, v49, v49 row_ror:2 row_mask:0xf bank_mask:0xf\n\t" \
    "v_and_b32 v56, 1, v1\n\t" \
    "v_lshlrev_b32 v56, 7, v56\n\t" \
    "v_lshrrev_b32 v57, 4, v1\n\t" \
    "v_lshl_add_u32 v56, v57, 5, v56\n\t" \
    "s_lshl_b32 s96, s50, 2\n\t" \
    "s_add_u32 s96, s96, 159744\n\t" \
    "v_add_u32 v56, s96, v56\n\t" \
    "ds_write2_b32 v56, v48, v49 offset0:0 offset1:4\n\t" \
    "v_and_b32 v57, 15, v1\n\t" \
    "v_lshlrev_b32 v57, 4, v57\n\t" \
    "v_add_u32 v57, 159744, v57\n\t" \
    "s_waitcnt lgkmcnt(0)\n\t" \
    "s_barrier\n\t" \
    "ds_read_b128 v[48:51], v57\n\t" \
    "s_waitcnt lgkmcnt(0)\n\t" \
    "v_add_f32 v48, v48, v49\n\t" \
    "v_add_f32 v50, v50, v51\n\t" \
    "v_add_f32 v32, v48, v50\n\t" \
    "v_cvt_pk_bf16_f32 v32, v32, v32\n\t" \
    "v_lshlrev_b32 v32, 16, v32\n\t" \
    "s_sub_u32 s88, s49, 8\n\t" \
    "s_cmp_lt_u32 s88, 16\n\t" \
    "s_cbranch_scc0 .Lnm2b_%=\n\t" \
    "v_add_f32 v33, 1.0, v32\n\t" \
    "v_cvt_pk_bf16_f32 v33, v33, v33\n\t" \
    "v_lshlrev_b32 v33, 16, v33\n\t" \
    "s_lshr_b32 s89, s48, 1\n\t" \
    "s_cmp_eq_u32 s89, 2\n\t" \
    "s_cselect_b64 vcc, -1, 0\n\t" \
    "v_cndmask_b32 v32, v32, v33, vcc\n\t" \
    "s_cmp_eq_u32 s89, 1\n\t" \
    "s_cselect_b32 s91, 0x3fb8aa3b, 1.0\n\t" \
    "s_cmp_eq_u32 s89, 3\n\t" \
    "s_cselect_b32 s91, 0x3f317218, s91\n\t" \
    "v_mul_f32 v32, s91, v32\n\t" \
    "s_lshl_b32 s89, s48, 4\n\t" \
    "s_add_u32 s88, s88, s89\n\t" \
    "s_lshl_b32 s88, s88, 6\n\t" \
    "s_add_u32 s88, s88, 196608\n\t" \
    "v_lshlrev_b32 v33, 2, v0\n\t" \
    "v_cmp_gt_u32 vcc, 16, v0\n\t" \
    "s_nop 0\n\t" \
    "s_mov_b64 exec, vcc\n\t" \
    "buffer_store_dword v32, v33, s[8:11], s88 offen sc1\n\t" \
    "s_mov_b64 exec, -1\n\t" \
    ".Lnm2b_%=:\n\t" \
    "s_cmp_lt_u32 s49, 4\n\t" \
    "s_cbranch_scc1 .Lnw2b_%=\n\t" \
    "v_lshrrev_b32 v34, 2, v1\n\t" \
    "v_lshlrev_b32 v34, 8, v34\n\t" \
    "v_and_b32 v35, 3, v1\n\t" \
    "v_lshl_add_u32 v34, v35, 4, v34\n\t" \
    "s_lshl_b32 s88, s50, 6\n\t" \
    "s_add_u32 s88, s88, 147456\n\t" \
    "v_add_u32 v34, s88, v34\n\t" \
    "ds_read_b64_tr_b16 v[48:49], v34 offset:0\n\t" \
    "ds_read_b64_tr_b16 v[50:51], v34 offset:4096\n\t" \
    "ds_read_b64_tr_b16 v[52:53], v34 offset:8\n\t" \
    "ds_read_b64_tr_b16 v[54:55], v34 offset:4104\n\t" \
    "s_and_b32 s88, s90, 3\n\t" \
    "s_lshl_b32 s88, s88, 1\n\t" \
    "s_lshr_b32 s89, s50, 1\n\t" \
    "s_add_u32 s88, s88, s89\n\t" \
    "s_mul_i32 s88, s88, 56\n\t" \
    "s_lshr_b32 s89, s90, 2\n\t" \
    "s_add_u32 s88, s88, s89\n\t" \
    "s_lshl_b32 s88, s88, 12\n\t" \
    "s_and_b32 s89, s50, 1\n\t" \
    "s_lshl_b32 s89, s89, 11\n\t" \
    "s_add_u32 s88, s88, s89\n\t" \
    "s_add_u32 s88, s88, 917504\n\t" \
    "s_waitcnt lgkmcnt(0)\n\t" \
    "buffer_store_dwordx4 v[48:51], v2, s[8:11], s88 offen sc1\n\t" \
    "buffer_store_dwordx4 v[52:55], v2, s[8:11], s88 offen offset:1024 sc1\n\t" \
    ".Lnw2b_%=:\n\t" \
      \
    "v_and_b32 v28, 31, v1\n\t" \
    "v_lshrrev_b32 v29, 5, v1\n\t" \
    "v_lshrrev_b32 v30, 4, v1\n\t" \
    "v_and_b32 v31, 15, v1\n\t" \
    "v_and_b32 v32, 7, v1\n\t" \
    "v_lshrrev_b32 v33, 1, v1\n\t" \
    "v_lshlrev_b32 v241, 4, v31\n\t" \
    "v_lshl_add_u32 v241, v29, 8, v241\n\t" \
    "s_lshl_b32 s88, s50, 11\n\t" \
    "v_add_u32 v255, s88, v2\n\t" \
    "v_add_u32 v242, 139264, v255\n\t" \
    "v_add_u32 v254, 131072, v255\n\t" \
    "s_lshl_b32 s88, s50, 2\n\t" \
    "s_add_u32 s89, s88, 160080\n\t" \
    "v_mov_b32 v76, s89\n\t" \
    "v_add_u32 v77, 16, v76\n\t" \
    "s_add_u32 s88, s88, 155648\n\t" \
    "v_lshlrev_b32 v243, 4, v28\n\t" \
    "v_add_u32 v243, s88, v243\n\t" \
    "s_lshl_b32 s88, s50, 9\n\t" \
    "s_add_u32 s88, s88, 139264\n\t" \
    "v_lshlrev_b32 v244, 3, v1\n\t" \
    "v_add_u32 v244, s88, v244\n\t" \
    "v_lshlrev_b32 v245, 4, v33\n\t" \
    "v_add_u32 v245, 155648, v245\n\t" \
    "v_and_b32 v34, 7, v33\n\t" \
    "v_mul_u32_u24 v246, 144, v34\n\t" \
    "v_lshl_add_u32 v246, v30, 5, v246\n\t" \
    "v_and_b32 v34, 1, v1\n\t" \
    "v_lshl_add_u32 v246, v34, 2, v246\n\t" \
    "s_lshl_b32 s88, s50, 3\n\t" \
    "s_add_u32 s88, s88, 156160\n\t" \
    "v_add_u32 v246, s88, v246\n\t" \
    "v_min_u32 v34, 8, v31\n\t" \
    "v_mul_u32_u24 v247, 144, v34\n\t" \
    "v_xor_b32 v34, 8, v31\n\t" \
    "v_min_u32 v34, 8, v34\n\t" \
    "v_mul_u32_u24 v248, 144, v34\n\t" \
    "v_lshlrev_b32 v34, 4, v30\n\t" \
    "v_add_u32 v34, 156160, v34\n\t" \
    "v_add_u32 v247, v247, v34\n\t" \
    "v_add_u32 v248, v248, v34\n\t" \
    "v_lshlrev_b32 v34, 1, v30\n\t" \
    "v_xor_b32 v34, v34, v31\n\t" \
    "v_lshlrev_b32 v34, 4, v34\n\t" \
    "v_mul_u32_u24 v250, 3584, v30\n\t" \
    "v_add_u32 v250, v250, v34\n\t" \
    "v_xor_b32 v34, 0x80, v34\n\t" \
    "v_mul_u32_u24 v251, 3584, v30\n\t" \
    "v_add_u32 v251, v251, v34\n\t" \
    "v_lshlrev_b32 v25, 10, v32\n\t" \
    "v_bfe_u32 v34, v1, 3, 1\n\t" \
    "v_lshl_add_u32 v25, v34, 7, v25\n\t" \
    "v_lshl_add_u32 v25, v30, 4, v25\n\t" \
    "v_lshlrev_b32 v27, 8, v34\n\t" \
    "v_lshl_add_u32 v27, v30, 5, v27\n\t" \
    "s_lshl_b32 s88, s50, 9\n\t" \
    "v_add_u32 v27, s88, v27\n\t" \
    "v_and_b32 v38, 3, v1\n\t" \
    "v_lshlrev_b32 v78, 2, v38\n\t" \
    "v_add_u32 v78, 160080, v78\n\t" \
    "s_lshl_b32 s88, s50, 12\n\t" \
    "s_add_u32 s64, s88, 262144\n\t" \
    "s_lshl_b32 s66, s53, 13\n\t" \
    "s_add_u32 s66, s66, 2752512\n\t" \
    "s_mul_i32 s68, s59, 224\n\t" \
    "s_mul_i32 s88, s50, 458752\n\t" \
    "s_add_u32 s68, s68, s88\n\t" \
    "s_lshl_b32 s69, s50, 15\n\t" \
    "s_mov_b32 s70, 0\n\t" \
    "s_mov_b32 s76, 0xffff\n\t" \
    "s_mov_b32 s77, 0xffff\n\t" \
    "v_lshrrev_b32 v34, 3, v1\n\t" \
    "v_lshlrev_b32 v34, 5, v34\n\t" \
    "s_lshl_b32 s88, s50, 3\n\t" \
    "v_add3_u32 v34, v34, v32, s88\n\t" \
    "s_lshl_b32 s88, s60, 2\n\t" \
    "s_add_u32 s88, s88, 96\n\t" \
    "v_add_u32 v35, s88, v38\n\t" \
    "v_cmp_gt_u32 vcc, 24, v1\n\t" \
    "s_nop 1\n\t" \
    "v_cndmask_b32 v34, v35, v34, vcc\n\t" \
    "v_lshrrev_b32 v35, 4, v34\n\t" \
    "v_lshlrev_b32 v35, 5, v35\n\t" \
    "v_and_b32 v34, 15, v34\n\t" \
    "v_add3_u32 v34, v34, v35, 8\n\t" \
    "v_min_u32 v35, 13, v28\n\t" \
    "s_mul_i32 s88, s50, 14\n\t" \
    "v_add_u32 v35, s88, v35\n\t" \
    "s_lshr_b32 s88, s60, 1\n\t" \
    "v_lshl_add_u32 v35, v35, 2, s88\n\t" \
    "v_mul_u32_u24 v36, 2341, v35\n\t" \
    "v_lshrrev_b32 v36, 16, v36\n\t" \
    "v_lshl_add_u32 v35, v36, 2, v35\n\t" \
    "v_add_u32 v35, 4, v35\n\t" \
    "v_cmp_gt_u32 vcc, 32, v1\n\t" \
    "s_nop 1\n\t" \
    "v_cndmask_b32 v34, v35, v34, vcc\n\t" \
    "v_lshlrev_b32 v24, 7, v34\n\t" \
    "v_add_u32 v24, 65536, v24\n\t" \
    "v_lshlrev_b32 v37, 11, v31\n\t" \
    "s_lshl_b32 s88, s50, 9\n\t" \
    "s_add_u32 s88, s88, 33152\n\t" \
    "v_add_u32 v37, s88, v37\n\t" \
    "s_lshl_b32 s88, s50, 10\n\t" \
    "s_add_u32 s88, s88, 32768\n\t" \
    "v_lshrrev_b32 v39, 2, v1\n\t" \
    "v_lshlrev_b32 v39, 12, v39\n\t" \
    "v_and_b32 v34, 2, v1\n\t" \
    "v_lshl_add_u32 v39, v34, 8, v39\n\t" \
    "v_and_b32 v34, 1, v1\n\t" \
    "v_lshl_add_u32 v39, v34, 7, v39\n\t" \
    "s_add_u32 s88, s88, 0x88\n\t" \
    "v_add_u32 v39, s88, v39\n\t" \
    "s_lshr_b32 s89, s53, 2\n\t" \
    "s_lshl_b32 s89, s89, 9\n\t" \
    "s_add_u32 s89, s89, 32768\n\t" \
    "v_lshlrev_b32 v38, 7, v38\n\t" \
    "v_add_u32 v38, s89, v38\n\t" \
    "v_cndmask_b32 v39, v38, v39, vcc\n\t" \
    "s_mul_i32 s16, s60, 56\n\t" \
    "s_mul_i32 s88, s50, 14\n\t" \
    "s_add_u32 s16, s16, s88\n\t" \
    "s_lshl_b32 s16, s16, 12\n\t" \
    "s_add_u32 s16, s16, 917504\n\t" \
    "s_add_u32 s17, s16, 4096\n\t" \
    "s_add_u32 s18, s16, 8192\n\t" \
    "s_add_u32 s19, s16, 12288\n\t" \
    "s_add_u32 s20, s16, 16384\n\t" \
    "s_add_u32 s21, s16, 20480\n\t" \
    "s_add_u32 s22, s16, 24576\n\t" \
    "s_add_u32 s23, s16, 28672\n\t" \
    "s_add_u32 s24, s16, 32768\n\t" \
    "s_add_u32 s25, s16, 36864\n\t" \
    "s_add_u32 s26, s16, 40960\n\t" \
    "s_add_u32 s27, s16, 45056\n\t" \
    "s_add_u32 s28, s16, 49152\n\t" \
    "s_add_u32 s29, s16, 53248\n\t" \
    "v_mov_b32 v192, 0\n\t" \
    "v_mov_b32 v193, 0\n\t" \
    "v_mov_b32 v194, 0\n\t" \
    "v_mov_b32 v195, 0\n\t" \
    "v_mov_b32 v196, 0\n\t" \
    "v_mov_b32 v197, 0\n\t" \
    "v_mov_b32 v198, 0\n\t" \
    "v_mov_b32 v199, 0\n\t" \
    "v_mov_b32 v200, 0\n\t" \
    "v_mov_b32 v201, 0\n\t" \
    "v_mov_b32 v202, 0\n\t" \
    "v_mov_b32 v203, 0\n\t" \
    "v_mov_b32 v204, 0\n\t" \
    "v_mov_b32 v205, 0\n\t" \
    "v_mov_b32 v206, 0\n\t" \
    "v_mov_b32 v207, 0\n\t" \
    "s_lshl_b32 s65, s51, 14\n\t" \
    "s_add_u32 s80, s64, s65\n\t" \
    "s_add_u32 s65, s65, 0x4000\n\t" \
    "s_and_b32 s65, s65, 0x1ffff\n\t" \
    "s_add_u32 s81, s64, s65\n\t" \
    "s_add_u32 s65, s65, 0x4000\n\t" \
    "s_and_b32 s65, s65, 0x1ffff\n\t" \
    "s_add_u32 s82, s64, s65\n\t" \
    "s_add_u32 s65, s65, 0x4000\n\t" \
    "s_and_b32 s65, s65, 0x1ffff\n\t" \
    "s_add_u32 s83, s64, s65\n\t" \
    "s_add_u32 s65, s65, 0x4000\n\t" \
    "s_and_b32 s65, s65, 0x1ffff\n\t" \
    "s_add_u32 s84, s64, s65\n\t" \
    "s_add_u32 s65, s65, 0x4000\n\t" \
    "s_and_b32 s65, s65, 0x1ffff\n\t" \
    "s_add_u32 s85, s64, s65\n\t" \
    "s_add_u32 s65, s65, 0x4000\n\t" \
    "s_and_b32 s65, s65, 0x1ffff\n\t" \
    "s_add_u32 s86, s64, s65\n\t" \
    "s_add_u32 s65, s65, 0x4000\n\t" \
    "s_and_b32 s65, s65, 0x1ffff\n\t" \
    "s_add_u32 s87, s64, s65\n\t" \
    "s_add_u32 s65, s65, 0x4000\n\t" \
    "s_and_b32 s65, s65, 0x1ffff\n\t" \
    "v_min_u32 v81, 27, v1\n\t" \
    "v_lshlrev_b32 v81, 7, v81\n\t" \
    "s_and_b32 s94, s49, 3\n\t" \
    "s_mul_i32 s94, s94, 14336\n\t" \
    "s_add_u32 s94, s94, s16\n\t" \
    "s_sub_u32 s94, s94, 7168\n\t" \
    "s_lshl_b32 s79, s53, 7\n\t" \
    "s_add_u32 s79, s79, 65536\n\t" \
    "s_lshl_b32 s78, s53, 13\n\t" \
    "s_lshl_b32 s89, s50, 8\n\t" \
    "s_add_u32 s78, s78, s89\n\t" \
    "s_lshl_b32 s88, s50, 2\n\t" \
    "s_add_u32 s88, s88, 160064\n\t" \
    "v_mov_b32 v46, s88\n\t" \
    "v_mov_b32 v47, 160064\n\t" \
    "s_mov_b32 s71, 1\n\t" \
    "v_mov_b32 v79, 1\n\t" \
    "s_lshl_b32 s67, s50, 14\n\t" \
    "s_add_u32 s67, s67, 393216\n\t" \
    "v_add_u32 v249, 132096, v2\n\t" \
    "s_mov_b32 s72, s67\n\t" \
    "s_add_u32 s73, s67, 0x1000\n\t" \
    "s_add_u32 s74, s67, 0x2000\n\t" \
    "s_add_u32 s75, s67, 0x3000\n\t" \
    "s_add_u32 s67, s67, 0x10000\n\t" \
    "v_mov_b32 v10, s54\n\t" \
    "v_mov_b32 v11, s55\n\t" \
    "v_mov_b32 v12, s54\n\t" \
    "v_mov_b32 v13, s55\n\t" \
    ".Lpb_%=:\n\t" \
    "buffer_load_dwordx2 v[40:41], v39, s[8:11], 0 offen sc1\n\t" \
    "s_waitcnt vmcnt(0)\n\t" \
    "v_xor_b32 v40, s56, v40\n\t" \
    "v_xor_b32 v41, s57, v41\n\t" \
    "v_lshrrev_b32 v44, 16, v40\n\t" \
    "v_or_b32 v44, v44, v41\n\t" \
    "v_cmp_ne_u32 vcc, 0, v44\n\t" \
    "s_nop 0\n\t" \
    "s_cbranch_vccz .Lpb_%=_ok\n\t" \
    "s_sub_u32 s62, s62, 1\n\t" \
    "s_cmp_lt_i32 s62, 0\n\t" \
    "s_cbranch_scc1 .Lpb_%=_ok\n\t" \
    "s_sleep 4\n\t" \
    "s_branch .Lpb_%=\n\t" \
    ".Lpb_%=_ok:\n\t" \
      \
    ".Lp2_%=:\n\t" \
    "buffer_load_dwordx4 v[180:183], v2, s[8:11], s66 offen sc1\n\t" \
    "buffer_load_dwordx4 a[0:3], v2, s[8:11], s80 offen offset:0 sc1\n\t" \
    "buffer_load_dwordx4 a[4:7], v2, s[8:11], s80 offen offset:1024 sc1\n\t" \
    "s_mov_b64 exec, s[76:77]\n\t" \
    "buffer_load_dwordx4 a[64:67], v241, s[8:11], s80 offen offset:2048 sc1\n\t" \
    "buffer_load_dwordx4 a[68:71], v241, s[8:11], s80 offen offset:2560 sc1\n\t" \
    "s_mov_b64 exec, -1\n\t" \
    "buffer_load_dwordx4 a[8:11], v2, s[8:11], s81 offen offset:0 sc1\n\t" \
    "s_mov_b64 exec, s[76:77]\n\t" \
    "buffer_load_dwordx4 a[72:75], v241, s[8:11], s80 offen offset:3072 sc1\n\t" \
    "buffer_load_dwordx4 a[76:79], v241, s[8:11], s80 offen offset:3584 sc1\n\t" \
    "s_mov_b64 exec, -1\n\t" \
    "s_cmp_lg_u32 s70, 0\n\t" \
    "s_cbranch_scc1 .Lms_%=\n\t" \
    "v_max_u32_dpp v40, v40, v40 row_ror:8 row_mask:0xf bank_mask:0xf\n\t" \
    "s_nop 1\n\t" \
    "v_max_u32_dpp v40, v40, v40 row_ror:4 row_mask:0xf bank_mask:0xf\n\t" \
    "s_nop 1\n\t" \
    "v_max_u32_dpp v40, v40, v40 row_ror:2 row_mask:0xf bank_mask:0xf\n\t" \
    "s_nop 1\n\t" \
    "v_max_u32_dpp v40, v40, v40 row_ror:1 row_mask:0xf bank_mask:0xf\n\t" \
    "v_mov_b32 v41, v40\n\t" \
    "s_nop 1\n\t" \
    "v_permlane16_swap_b32 v40, v41\n\t" \
    "v_max_u32 v40, v40, v41\n\t" \
    "v_mov_b32 v42, v40\n\t" \
    "s_nop 1\n\t" \
    "v_permlane32_swap_b32 v40, v42\n\t" \
    "v_lshlrev_b32 v40, 16, v40\n\t" \
    "v_lshlrev_b32 v42, 16, v42\n\t" \
    "v_mul_f32 v40, v40, v42\n\t" \
    "v_sqrt_f32 v40, v40\n\t" \
    "s_nop 0\n\t" \
    "v_mul_f32 v40, 0x3f8147ae, v40\n\t" \
    "v_and_b32 v40, 0x7fffffff, v40\n\t" \
    "v_min_u32 v40, 0x7f800000, v40\n\t" \
    "v_xor_b32 v252, 0x80000000, v40\n\t" \
    ".Lmr_%=:\n\t" \
    "ds_write_b32 v46, v40\n\t" \
    "v_mov_b32 v100, v252\n\t" \
    "v_mov_b32 v101, v252\n\t" \
    "v_mov_b32 v102, v252\n\t" \
    "v_mov_b32 v103, v252\n\t" \
    "v_mov_b32 v104, v252\n\t" \
    "v_mov_b32 v105, v252\n\t" \
    "buffer_load_dwordx4 a[12:15], v2, s[8:11], s81 offen offset:1024 sc1\n\t" \
    "s_mov_b64 exec, s[76:77]\n\t" \
    "buffer_load_dwordx4 a[80:83], v241, s[8:11], s81 offen offset:2048 sc1\n\t" \
    "buffer_load_dwordx4 a[84:87], v241, s[8:11], s81 offen offset:2560 sc1\n\t" \
    "s_mov_b64 exec, -1\n\t" \
    "v_mov_b32 v106, v252\n\t" \
    "v_mov_b32 v107, v252\n\t" \
    "v_mov_b32 v108, v252\n\t" \
    "v_mov_b32 v109, v252\n\t" \
    "v_mov_b32 v110, v252\n\t" \
    "v_mov_b32 v111, v252\n\t" \
    "buffer_load_dwordx4 a[16:19], v2, s[8:11], s82 offen offset:0 sc1\n\t" \
    "s_mov_b64 exec, s[76:77]\n\t" \
    "buffer_load_dwordx4 a[88:91], v241, s[8:11], s81 offen offset:3072 sc1\n\t" \
    "buffer_load_dwordx4 a[92:95], v241, s[8:11], s81 offen offset:3584 sc1\n\t" \
    "s_mov_b64 exec, -1\n\t" \
    "v_mov_b32 v112, v252\n\t" \
    "v_mov_b32 v113, v252\n\t" \
    "v_mov_b32 v114, v252\n\t" \
    "v_mov_b32 v115, v252\n\t" \
    "s_mov_b32 s88, 196608\n\t" \
    "s_mov_b32 s89, 198656\n\t" \
    "buffer_load_dwordx4 a[20:23], v2, s[8:11], s82 offen offset:1024 sc1\n\t" \
    "s_mov_b64 exec, s[76:77]\n\t" \
    "buffer_load_dwordx4 a[96:99], v241, s[8:11], s82 offen offset:2048 sc1\n\t" \
    "buffer_load_dwordx4 a[100:103], v241, s[8:11], s82 offen offset:2560 sc1\n\t" \
    "s_mov_b64 exec, -1\n\t" \
    "s_mov_b32 s90, 200704\n\t" \
    "s_add_u32 s95, s70, 1\n\t" \
    "v_mov_b32 v43, s95\n\t" \
    "buffer_load_dwordx4 a[24:27], v2, s[8:11], s83 offen offset:0 sc1\n\t" \
    "s_mov_b64 exec, s[76:77]\n\t" \
    "buffer_load_dwordx4 a[104:107], v241, s[8:11], s82 offen offset:3072 sc1\n\t" \
    "buffer_load_dwordx4 a[108:111], v241, s[8:11], s82 offen offset:3584 sc1\n\t" \
    "s_mov_b64 exec, -1\n\t" \
    "s_waitcnt vmcnt(18)\n\t" \
    "v_mfma_f32_32x32x16_bf16 v[148:163], a[0:3], v[180:183], v[100:115]\n\t" \
    "s_nop 11\n\t" \
    "v_exp_f32 v148, v148\n\t" \
    "v_exp_f32 v149, v149\n\t" \
    "v_exp_f32 v150, v150\n\t" \
    "s_waitcnt vmcnt(17)\n\t" \
    "v_exp_f32 v151, v151\n\t" \
    "v_cvt_pk_bf16_f32 v184, v148, v149\n\t" \
    "v_exp_f32 v152, v152\n\t" \
    "v_exp_f32 v153, v153\n\t" \
    "v_cvt_pk_bf16_f32 v185, v150, v151\n\t" \
    "v_exp_f32 v154, v154\n\t" \
    "v_mfma_f32_32x32x16_bf16 v[164:179], a[4:7], v[180:183], v[100:115]\n\t" \
    "s_waitcnt vmcnt(15)\n\t" \
    "buffer_load_dwordx4 a[28:31], v2, s[8:11], s83 offen offset:1024 sc1\n\t" \
    "v_exp_f32 v155, v155\n\t" \
    "v_cvt_pk_bf16_f32 v186, v152, v153\n\t" \
    "v_exp_f32 v156, v156\n\t" \
    "v_cvt_pk_bf16_f32 v187, v154, v155\n\t" \
    "v_exp_f32 v157, v157\n\t" \
    "v_exp_f32 v158, v158\n\t" \
    "v_mfma_f32_32x32x16_bf16 v[132:147], a[64:67], v[184:187], 0\n\t" \
    "s_mov_b64 exec, s[76:77]\n\t" \
    "buffer_load_dwordx4 a[112:115], v241, s[8:11], s83 offen offset:2048 sc1\n\t" \
    "buffer_load_dwordx4 a[116:119], v241, s[8:11], s83 offen offset:2560 sc1\n\t" \
    "s_mov_b64 exec, -1\n\t" \
    "v_exp_f32 v159, v159\n\t" \
    "v_cvt_pk_bf16_f32 v188, v156, v157\n\t" \
    "v_exp_f32 v160, v160\n\t" \
    "v_exp_f32 v161, v161\n\t" \
    "v_cvt_pk_bf16_f32 v189, v158, v159\n\t" \
    "v_exp_f32 v162, v162\n\t" \
    "v_exp_f32 v163, v163\n\t" \
    "v_cvt_pk_bf16_f32 v190, v160, v161\n\t" \
    "v_exp_f32 v164, v164\n\t" \
    "v_cvt_pk_bf16_f32 v191, v162, v163\n\t" \
    "v_exp_f32 v165, v165\n\t" \
    "v_exp_f32 v166, v166\n\t" \
    "v_mfma_f32_32x32x16_bf16 v[116:131], a[68:71], v[188:191], v[132:147]\n\t" \
    "s_waitcnt vmcnt(17)\n\t" \
    "buffer_load_dwordx4 v[84:87], v255, s[8:11], s66 offen sc1\n\t" \
    "v_exp_f32 v167, v167\n\t" \
    "v_cvt_pk_bf16_f32 v184, v164, v165\n\t" \
    "v_exp_f32 v168, v168\n\t" \
    "v_exp_f32 v169, v169\n\t" \
    "v_cvt_pk_bf16_f32 v185, v166, v167\n\t" \
    "v_exp_f32 v170, v170\n\t" \
    "v_mfma_f32_32x32x16_bf16 v[148:163], a[8:11], v[180:183], v[100:115]\n\t" \
    "s_waitcnt vmcnt(16)\n\t" \
    "buffer_load_dwordx4 a[32:35], v2, s[8:11], s84 offen offset:0 sc1\n\t" \
    "v_exp_f32 v171, v171\n\t" \
    "v_cvt_pk_bf16_f32 v186, v168, v169\n\t" \
    "v_exp_f32 v172, v172\n\t" \
    "v_cvt_pk_bf16_f32 v187, v170, v171\n\t" \
    "v_exp_f32 v173, v173\n\t" \
    "v_exp_f32 v174, v174\n\t" \
    "v_mfma_f32_32x32x16_bf16 v[116:131], a[72:75], v[184:187], v[116:131]\n\t" \
    "s_mov_b64 exec, s[76:77]\n\t" \
    "buffer_load_dwordx4 a[120:123], v241, s[8:11], s83 offen offset:3072 sc1\n\t" \
    "buffer_load_dwordx4 a[124:127], v241, s[8:11], s83 offen offset:3584 sc1\n\t" \
    "s_mov_b64 exec, -1\n\t" \
    "v_exp_f32 v175, v175\n\t" \
    "v_cvt_pk_bf16_f32 v188, v172, v173\n\t" \
    "v_exp_f32 v176, v176\n\t" \
    "v_exp_f32 v177, v177\n\t" \
    "v_cvt_pk_bf16_f32 v189, v174, v175\n\t" \
    "v_exp_f32 v178, v178\n\t" \
    "v_exp_f32 v179, v179\n\t" \
    "v_cvt_pk_bf16_f32 v190, v176, v177\n\t" \
    "v_exp_f32 v148, v148\n\t" \
    "v_cvt_pk_bf16_f32 v191, v178, v179\n\t" \
    "v_exp_f32 v149, v149\n\t" \
    "v_exp_f32 v150, v150\n\t" \
    "v_mfma_f32_32x32x16_bf16 v[116:131], a[76:79], v[188:191], v[116:131]\n\t" \
    "s_waitcnt vmcnt(18)\n\t" \
    "buffer_load_dwordx4 v[88:91], v255, s[8:11], s66 offen offset:1024 sc1\n\t" \
    "v_exp_f32 v151, v151\n\t" \
    "v_cvt_pk_bf16_f32 v184, v148, v149\n\t" \
    "v_exp_f32 v152, v152\n\t" \
    "v_exp_f32 v153, v153\n\t" \
    "v_cvt_pk_bf16_f32 v185, v150, v151\n\t" \
    "v_exp_f32 v154, v154\n\t" \
    "v_mfma_f32_32x32x16_bf16 v[164:179], a[12:15], v[180:183], v[100:115]\n\t" \
    "s_waitcnt vmcnt(17)\n\t" \
    "buffer_load_dwordx4 a[36:39], v2, s[8:11], s84 offen offset:1024 sc1\n\t" \
    "v_exp_f32 v155, v155\n\t" \
    "v_cvt_pk_bf16_f32 v186, v152, v153\n\t" \
    "v_exp_f32 v156, v156\n\t" \
    "v_cvt_pk_bf16_f32 v187, v154, v155\n\t" \
    "v_exp_f32 v157, v157\n\t" \
    "v_exp_f32 v158, v158\n\t" \
    "v_mfma_f32_32x32x16_bf16 v[116:131], a[80:83], v[184:187], v[116:131]\n\t" \
    "s_mov_b64 exec, s[76:77]\n\t" \
    "buffer_load_dwordx4 a[128:131], v241, s[8:11], s84 offen offset:2048 sc1\n\t" \
    "buffer_load_dwordx4 a[132:135], v241, s[8:11], s84 offen offset:2560 sc1\n\t" \
    "s_mov_b64 exec, -1\n\t" \
    "v_exp_f32 v159, v159\n\t" \
    "v_cvt_pk_bf16_f32 v188, v156, v157\n\t" \
    "v_exp_f32 v160, v160\n\t" \
    "v_exp_f32 v161, v161\n\t" \
    "v_cvt_pk_bf16_f32 v189, v158, v159\n\t" \
    "v_exp_f32 v162, v162\n\t" \
    "v_exp_f32 v163, v163\n\t" \
    "v_cvt_pk_bf16_f32 v190, v160, v161\n\t" \
    "v_exp_f32 v164, v164\n\t" \
    "v_cvt_pk_bf16_f32 v191, v162, v163\n\t" \
    "v_exp_f32 v165, v165\n\t" \
    "v_exp_f32 v166, v166\n\t" \
    "v_mfma_f32_32x32x16_bf16 v[116:131], a[84:87], v[188:191], v[116:131]\n\t" \
    "s_waitcnt vmcnt(19)\n\t" \
    "buffer_load_dwordx2 v[44:45], v37, s[8:11], 0 offen sc1\n\t" \
    "v_exp_f32 v167, v167\n\t" \
    "v_cvt_pk_bf16_f32 v184, v164, v165\n\t" \
    "v_exp_f32 v168, v168\n\t" \
    "v_exp_f32 v169, v169\n\t" \
    "v_cvt_pk_bf16_f32 v185, v166, v167\n\t" \
    "v_exp_f32 v170, v170\n\t" \
    "v_mfma_f32_32x32x16_bf16 v[148:163], a[16:19], v[180:183], v[100:115]\n\t" \
    "s_waitcnt vmcnt(18)\n\t" \
    "buffer_load_dwordx4 a[40:43], v2, s[8:11], s85 offen offset:0 sc1\n\t" \
    "v_exp_f32 v171, v171\n\t" \
    "v_cvt_pk_bf16_f32 v186, v168, v169\n\t" \
    "v_exp_f32 v172, v172\n\t" \
    "v_cvt_pk_bf16_f32 v187, v170, v171\n\t" \
    "v_exp_f32 v173, v173\n\t" \
    "v_exp_f32 v174, v174\n\t" \
    "v_mfma_f32_32x32x16_bf16 v[116:131], a[88:91], v[184:187], v[116:131]\n\t" \
    "s_mov_b64 exec, s[76:77]\n\t" \
    "buffer_load_dwordx4 a[136:139], v241, s[8:11], s84 offen offset:3072 sc1\n\t" \
    "buffer_load_dwordx4 a[140:143], v241, s[8:11], s84 offen offset:3584 sc1\n\t" \
    "s_mov_b64 exec, -1\n\t" \
    "v_exp_f32 v175, v175\n\t" \
    "v_cvt_pk_bf16_f32 v188, v172, v173\n\t" \
    "v_exp_f32 v176, v176\n\t" \
    "v_exp_f32 v177, v177\n\t" \
    "v_cvt_pk_bf16_f32 v189, v174, v175\n\t" \
    "v_exp_f32 v178, v178\n\t" \
    "v_exp_f32 v179, v179\n\t" \
    "v_cvt_pk_bf16_f32 v190, v176, v177\n\t" \
    "v_exp_f32 v148, v148\n\t" \
    "v_cvt_pk_bf16_f32 v191, v178, v179\n\t" \
    "v_exp_f32 v149, v149\n\t" \
    "v_exp_f32 v150, v150\n\t" \
    "v_mfma_f32_32x32x16_bf16 v[116:131], a[92:95], v[188:191], v[116:131]\n\t" \
    "s_waitcnt vmcnt(20)\n\t" \
    "v_exp_f32 v151, v151\n\t" \
    "v_cvt_pk_bf16_f32 v184, v148, v149\n\t" \
    "v_exp_f32 v152, v152\n\t" \
    "v_exp_f32 v153, v153\n\t" \
    "v_cvt_pk_bf16_f32 v185, v150, v151\n\t" \
    "v_exp_f32 v154, v154\n\t" \
    "v_mfma_f32_32x32x16_bf16 v[164:179], a[20:23], v[180:183], v[100:115]\n\t" \
    "s_waitcnt vmcnt(18)\n\t" \
    "buffer_load_dwordx4 a[44:47], v2, s[8:11], s85 offen offset:1024 sc1\n\t" \
    "v_exp_f32 v155, v155\n\t" \
    "v_cvt_pk_bf16_f32 v186, v152, v153\n\t" \
    "v_exp_f32 v156, v156\n\t" \
    "v_cvt_pk_bf16_f32 v187, v154, v155\n\t" \
    "v_exp_f32 v157, v157\n\t" \
    "v_exp_f32 v158, v158\n\t" \
    "v_mfma_f32_32x32x16_bf16 v[116:131], a[96:99], v[184:187], v[116:131]\n\t" \
    "s_mov_b64 exec, s[76:77]\n\t" \
    "buffer_load_dwordx4 a[144:147], v241, s[8:11], s85 offen offset:2048 sc1\n\t" \
    "buffer_load_dwordx4 a[148:151], v241, s[8:11], s85 offen offset:2560 sc1\n\t" \
    "s_mov_b64 exec, -1\n\t" \
    "v_exp_f32 v159, v159\n\t" \
    "v_cvt_pk_bf16_f32 v188, v156, v157\n\t" \
    "v_exp_f32 v160, v160\n\t" \
    "v_exp_f32 v161, v161\n\t" \
    "v_cvt_pk_bf16_f32 v189, v158, v159\n\t" \
    "v_exp_f32 v162, v162\n\t" \
    "v_exp_f32 v163, v163\n\t" \
    "v_cvt_pk_bf16_f32 v190, v160, v161\n\t" \
    "v_exp_f32 v164, v164\n\t" \
    "v_cvt_pk_bf16_f32 v191, v162, v163\n\t" \
    "v_exp_f32 v165, v165\n\t" \
    "v_exp_f32 v166, v166\n\t" \
    "v_mfma_f32_32x32x16_bf16 v[116:131], a[100:103], v[188:191], v[116:131]\n\t" \
    "s_waitcnt vmcnt(20)\n\t" \
    "v_exp_f32 v167, v167\n\t" \
    "v_cvt_pk_bf16_f32 v184, v164, v165\n\t" \
    "v_exp_f32 v168, v168\n\t" \
    "v_exp_f32 v169, v169\n\t" \
    "v_cvt_pk_bf16_f32 v185, v166, v167\n\t" \
    "v_exp_f32 v170, v170\n\t" \
    "v_mfma_f32_32x32x16_bf16 v[148:163], a[24:27], v[180:183], v[100:115]\n\t" \
    "s_waitcnt vmcnt(18)\n\t" \
    "buffer_load_dwordx4 a[48:51], v2, s[8:11], s86 offen offset:0 sc1\n\t" \
    "v_exp_f32 v171, v171\n\t" \
    "v_cvt_pk_bf16_f32 v186, v168, v169\n\t" \
    "v_exp_f32 v172, v172\n\t" \
    "v_cvt_pk_bf16_f32 v187, v170, v171\n\t" \
    "v_exp_f32 v173, v173\n\t" \
    "v_exp_f32 v174, v174\n\t" \
    "v_mfma_f32_32x32x16_bf16 v[116:131], a[104:107], v[184:187], v[116:131]\n\t" \
    "s_mov_b64 exec, s[76:77]\n\t" \
    "buffer_load_dwordx4 a[152:155], v241, s[8:11], s85 offen offset:3072 sc1\n\t" \
    "buffer_load_dwordx4 a[156:159], v241, s[8:11], s85 offen offset:3584 sc1\n\t" \
    "s_mov_b64 exec, -1\n\t" \
    "v_exp_f32 v175, v175\n\t" \
    "v_cvt_pk_bf16_f32 v188, v172, v173\n\t" \
    "v_exp_f32 v176, v176\n\t" \
    "v_exp_f32 v177, v177\n\t" \
    "v_cvt_pk_bf16_f32 v189, v174, v175\n\t" \
    "v_exp_f32 v178, v178\n\t" \
    "v_exp_f32 v179, v179\n\t" \
    "v_cvt_pk_bf16_f32 v190, v176, v177\n\t" \
    "v_exp_f32 v148, v148\n\t" \
    "v_cvt_pk_bf16_f32 v191, v178, v179\n\t" \
    "v_exp_f32 v149, v149\n\t" \
    "v_exp_f32 v150, v150\n\t" \
    "v_mfma_f32_32x32x16_bf16 v[116:131], a[108:111], v[188:191], v[116:131]\n\t" \
    "s_waitcnt vmcnt(20)\n\t" \
    "v_exp_f32 v151, v151\n\t" \
    "v_cvt_pk_bf16_f32 v184, v148, v149\n\t" \
    "v_exp_f32 v152, v152\n\t" \
    "v_exp_f32 v153, v153\n\t" \
    "v_cvt_pk_bf16_f32 v185, v150, v151\n\t" \
    "v_exp_f32 v154, v154\n\t" \
    "v_mfma_f32_32x32x16_bf16 v[164:179], a[28:31], v[180:183], v[100:115]\n\t" \
    "s_waitcnt vmcnt(18)\n\t" \
    "buffer_load_dwordx4 a[52:55], v2, s[8:11], s86 offen offset:1024 sc1\n\t" \
    "v_exp_f32 v155, v155\n\t" \
    "v_cvt_pk_bf16_f32 v186, v152, v153\n\t" \
    "v_exp_f32 v156, v156\n\t" \
    "v_cvt_pk_bf16_f32 v187, v154, v155\n\t" \
    "v_exp_f32 v157, v157\n\t" \
    "v_exp_f32 v158, v158\n\t" \
    "v_mfma_f32_32x32x16_bf16 v[116:131], a[112:115], v[184:187], v[116:131]\n\t" \
    "s_mov_b64 exec, s[76:77]\n\t" \
    "buffer_load_dwordx4 a[160:163], v241, s[8:11], s86 offen offset:2048 sc1\n\t" \
    "buffer_load_dwordx4 a[164:167], v241, s[8:11], s86 offen offset:2560 sc1\n\t" \
    "s_mov_b64 exec, -1\n\t" \
    "v_exp_f32 v159, v159\n\t" \
    "v_cvt_pk_bf16_f32 v188, v156, v157\n\t" \
    "v_exp_f32 v160, v160\n\t" \
    "v_exp_f32 v161, v161\n\t" \
    "v_cvt_pk_bf16_f32 v189, v158, v159\n\t" \
    "v_exp_f32 v162, v162\n\t" \
    "v_exp_f32 v163, v163\n\t" \
    "v_cvt_pk_bf16_f32 v190, v160, v161\n\t" \
    "v_exp_f32 v164, v164\n\t" \
    "v_cvt_pk_bf16_f32 v191, v162, v163\n\t" \
    "v_exp_f32 v165, v165\n\t" \
    "v_exp_f32 v166, v166\n\t" \
    "v_mfma_f32_32x32x16_bf16 v[116:131], a[116:119], v[188:191], v[116:131]\n\t" \
    "s_waitcnt vmcnt(19)\n\t" \
    "s_waitcnt vmcnt(16)\n\t" \
    "ds_write_b128 v254, v[84:87]\n\t" \
    "v_exp_f32 v167, v167\n\t" \
    "v_cvt_pk_bf16_f32 v184, v164, v165\n\t" \
    "v_exp_f32 v168, v168\n\t" \
    "v_exp_f32 v169, v169\n\t" \
    "v_cvt_pk_bf16_f32 v185, v166, v167\n\t" \
    "v_exp_f32 v170, v170\n\t" \
    "v_mfma_f32_32x32x16_bf16 v[148:163], a[32:35], v[180:183], v[100:115]\n\t" \
    "buffer_load_dwordx4 a[56:59], v2, s[8:11], s87 offen offset:0 sc1\n\t" \
    "v_exp_f32 v171, v171\n\t" \
    "v_cvt_pk_bf16_f32 v186, v168, v169\n\t" \
    "v_exp_f32 v172, v172\n\t" \
    "v_cvt_pk_bf16_f32 v187, v170, v171\n\t" \
    "v_exp_f32 v173, v173\n\t" \
    "v_exp_f32 v174, v174\n\t" \
    "v_mfma_f32_32x32x16_bf16 v[116:131], a[120:123], v[184:187], v[116:131]\n\t" \
    "s_mov_b64 exec, s[76:77]\n\t" \
    "buffer_load_dwordx4 a[168:171], v241, s[8:11], s86 offen offset:3072 sc1\n\t" \
    "buffer_load_dwordx4 a[172:175], v241, s[8:11], s86 offen offset:3584 sc1\n\t" \
    "s_mov_b64 exec, -1\n\t" \
    "s_waitcnt vmcnt(15)\n\t" \
    "v_exp_f32 v175, v175\n\t" \
    "v_cvt_pk_bf16_f32 v188, v172, v173\n\t" \
    "v_xor_b32 v44, s56, v44\n\t" \
    "v_xor_b32 v45, s57, v45\n\t" \
    "v_lshrrev_b32 v44, 16, v44\n\t" \
    "v_or_b32 v44, v44, v45\n\t" \
    "v_cmp_ne_u32 vcc, 0, v44\n\t" \
    "v_exp_f32 v176, v176\n\t" \
    "v_exp_f32 v177, v177\n\t" \
    "v_cvt_pk_bf16_f32 v189, v174, v175\n\t" \
    "v_exp_f32 v178, v178\n\t" \
    "v_exp_f32 v179, v179\n\t" \
    "v_cvt_pk_bf16_f32 v190, v176, v177\n\t" \
    "v_exp_f32 v148, v148\n\t" \
    "v_cvt_pk_bf16_f32 v191, v178, v179\n\t" \
    "v_exp_f32 v149, v149\n\t" \
    "v_exp_f32 v150, v150\n\t" \
    "v_mfma_f32_32x32x16_bf16 v[116:131], a[124:127], v[188:191], v[116:131]\n\t" \
    "s_cbranch_vccnz .Lfa_%=\n\t" \
    ".Lfar_%=:\n\t" \
    "ds_write_b128 v254, v[88:91] offset:1024\n\t" \
    "ds_write_b32 v76, v43 offset:48\n\t" \
    "v_exp_f32 v151, v151\n\t" \
    "v_cvt_pk_bf16_f32 v184, v148, v149\n\t" \
    "v_exp_f32 v152, v152\n\t" \
    "v_exp_f32 v153, v153\n\t" \
    "v_cvt_pk_bf16_f32 v185, v150, v151\n\t" \
    "v_exp_f32 v154, v154\n\t" \
    "v_mfma_f32_32x32x16_bf16 v[164:179], a[36:39], v[180:183], v[100:115]\n\t" \
    "buffer_load_dwordx4 a[60:63], v2, s[8:11], s87 offen offset:1024 sc1\n\t" \
    "v_exp_f32 v155, v155\n\t" \
    "v_cvt_pk_bf16_f32 v186, v152, v153\n\t" \
    "v_exp_f32 v156, v156\n\t" \
    "v_cvt_pk_bf16_f32 v187, v154, v155\n\t" \
    "v_exp_f32 v157, v157\n\t" \
    "v_exp_f32 v158, v158\n\t" \
    "v_mfma_f32_32x32x16_bf16 v[116:131], a[128:131], v[184:187], v[116:131]\n\t" \
    "s_mov_b64 exec, s[76:77]\n\t" \
    "buffer_load_dwordx4 a[176:179], v241, s[8:11], s87 offen offset:2048 sc1\n\t" \
    "buffer_load_dwordx4 a[180:183], v241, s[8:11], s87 offen offset:2560 sc1\n\t" \
    "s_mov_b64 exec, -1\n\t" \
    "v_exp_f32 v159, v159\n\t" \
    "v_cvt_pk_bf16_f32 v188, v156, v157\n\t" \
    "v_exp_f32 v160, v160\n\t" \
    "v_exp_f32 v161, v161\n\t" \
    "v_cvt_pk_bf16_f32 v189, v158, v159\n\t" \
    "v_exp_f32 v162, v162\n\t" \
    "v_exp_f32 v163, v163\n\t" \
    "v_cvt_pk_bf16_f32 v190, v160, v161\n\t" \
    "v_exp_f32 v164, v164\n\t" \
    "v_cvt_pk_bf16_f32 v191, v162, v163\n\t" \
    "v_exp_f32 v165, v165\n\t" \
    "v_exp_f32 v166, v166\n\t" \
    "v_mfma_f32_32x32x16_bf16 v[116:131], a[132:135], v[188:191], v[116:131]\n\t" \
    "s_waitcnt vmcnt(17)\n\t" \
    "v_exp_f32 v167, v167\n\t" \
    "v_cvt_pk_bf16_f32 v184, v164, v165\n\t" \
    "v_exp_f32 v168, v168\n\t" \
    "v_exp_f32 v169, v169\n\t" \
    "v_cvt_pk_bf16_f32 v185, v166, v167\n\t" \
    "v_exp_f32 v170, v170\n\t" \
    "v_mfma_f32_32x32x16_bf16 v[148:163], a[40:43], v[180:183], v[100:115]\n\t" \
    "s_waitcnt vmcnt(15)\n\t" \
    "v_exp_f32 v171, v171\n\t" \
    "v_cvt_pk_bf16_f32 v186, v168, v169\n\t" \
    "v_exp_f32 v172, v172\n\t" \
    "v_cvt_pk_bf16_f32 v187, v170, v171\n\t" \
    "v_exp_f32 v173, v173\n\t" \
    "v_exp_f32 v174, v174\n\t" \
    "v_mfma_f32_32x32x16_bf16 v[116:131], a[136:139], v[184:187], v[116:131]\n\t" \
    "s_mov_b64 exec, s[76:77]\n\t" \
    "buffer_load_dwordx4 a[184:187], v241, s[8:11], s87 offen offset:3072 sc1\n\t" \
    "buffer_load_dwordx4 a[188:191], v241, s[8:11], s87 offen offset:3584 sc1\n\t" \
    "s_mov_b64 exec, -1\n\t" \
    "v_exp_f32 v175, v175\n\t" \
    "v_cvt_pk_bf16_f32 v188, v172, v173\n\t" \
    "v_exp_f32 v176, v176\n\t" \
    "v_exp_f32 v177, v177\n\t" \
    "v_cvt_pk_bf16_f32 v189, v174, v175\n\t" \
    "v_exp_f32 v178, v178\n\t" \
    "v_exp_f32 v179, v179\n\t" \
    "v_cvt_pk_bf16_f32 v190, v176, v177\n\t" \
    "v_exp_f32 v148, v148\n\t" \
    "v_cvt_pk_bf16_f32 v191, v178, v179\n\t" \
    "v_exp_f32 v149, v149\n\t" \
    "v_exp_f32 v150, v150\n\t" \
    "v_mfma_f32_32x32x16_bf16 v[116:131], a[140:143], v[188:191], v[116:131]\n\t" \
    "s_waitcnt vmcnt(16)\n\t" \
    "ds_read_b32 v80, v78 offset:48\n\t" \
    "ds_read_b128 v[236:239], v47\n\t" \
    "v_exp_f32 v151, v151\n\t" \
    "v_cvt_pk_bf16_f32 v184, v148, v149\n\t" \
    "v_exp_f32 v152, v152\n\t" \
    "v_exp_f32 v153, v153\n\t" \
    "v_cvt_pk_bf16_f32 v185, v150, v151\n\t" \
    "v_exp_f32 v154, v154\n\t" \
    "v_mfma_f32_32x32x16_bf16 v[164:179], a[44:47], v[180:183], v[100:115]\n\t" \
    "s_waitcnt vmcnt(14)\n\t" \
    "v_exp_f32 v155, v155\n\t" \
    "v_cvt_pk_bf16_f32 v186, v152, v153\n\t" \
    "v_exp_f32 v156, v156\n\t" \
    "v_cvt_pk_bf16_f32 v187, v154, v155\n\t" \
    "v_exp_f32 v157, v157\n\t" \
    "v_exp_f32 v158, v158\n\t" \
    "v_mfma_f32_32x32x16_bf16 v[116:131], a[144:147], v[184:187], v[116:131]\n\t" \
    "s_waitcnt lgkmcnt(0)\n\t" \
    "v_exp_f32 v159, v159\n\t" \
    "v_cvt_pk_bf16_f32 v188, v156, v157\n\t" \
    "v_exp_f32 v160, v160\n\t" \
    "v_exp_f32 v161, v161\n\t" \
    "v_cvt_pk_bf16_f32 v189, v158, v159\n\t" \
    "v_cmp_ne_u32 vcc, s95, v80\n\t" \
    "v_exp_f32 v162, v162\n\t" \
    "v_exp_f32 v163, v163\n\t" \
    "v_cvt_pk_bf16_f32 v190, v160, v161\n\t" \
    "v_exp_f32 v164, v164\n\t" \
    "v_cvt_pk_bf16_f32 v191, v162, v163\n\t" \
    "v_exp_f32 v165, v165\n\t" \
    "v_exp_f32 v166, v166\n\t" \
    "v_mfma_f32_32x32x16_bf16 v[116:131], a[148:151], v[188:191], v[116:131]\n\t" \
    "s_waitcnt vmcnt(13)\n\t" \
    "s_cbranch_vccnz .Ls0_%=\n\t" \
    ".Ls0r_%=:\n\t" \
    "v_exp_f32 v167, v167\n\t" \
    "v_cvt_pk_bf16_f32 v184, v164, v165\n\t" \
    "v_exp_f32 v168, v168\n\t" \
    "v_exp_f32 v169, v169\n\t" \
    "v_cvt_pk_bf16_f32 v185, v166, v167\n\t" \
    "v_exp_f32 v170, v170\n\t" \
    "v_mfma_f32_32x32x16_bf16 v[148:163], a[48:51], v[180:183], v[100:115]\n\t" \
    "s_waitcnt vmcnt(11)\n\t" \
    "buffer_load_dwordx4 a[192:195], v2, s[8:11], s72 offen offset:0 sc1\n\t" \
    "buffer_load_dwordx4 a[196:199], v2, s[8:11], s72 offen offset:1024 sc1\n\t" \
    "v_exp_f32 v171, v171\n\t" \
    "v_cvt_pk_bf16_f32 v186, v168, v169\n\t" \
    "v_exp_f32 v172, v172\n\t" \
    "v_cvt_pk_bf16_f32 v187, v170, v171\n\t" \
    "v_exp_f32 v173, v173\n\t" \
    "v_exp_f32 v174, v174\n\t" \
    "v_mfma_f32_32x32x16_bf16 v[116:131], a[152:155], v[184:187], v[116:131]\n\t" \
    "s_mov_b64 exec, s[92:93]\n\t" \
    "buffer_store_dwordx4 v[10:13], v2, s[8:11], s79 offen sc1\n\t" \
    "s_mov_b64 exec, -1\n\t" \
    "v_exp_f32 v175, v175\n\t" \
    "v_cvt_pk_bf16_f32 v188, v172, v173\n\t" \
    "v_max3_f32 v82, v236, v237, v238\n\t" \
    "v_max_f32 v82, v82, v239\n\t" \
    "v_sub_f32 v236, v236, v82\n\t" \
    "v_sub_f32 v237, v237, v82\n\t" \
    "v_sub_f32 v238, v238, v82\n\t" \
    "v_sub_f32 v239, v239, v82\n\t" \
    "v_exp_f32 v176, v176\n\t" \
    "v_exp_f32 v177, v177\n\t" \
    "v_cvt_pk_bf16_f32 v189, v174, v175\n\t" \
    "v_exp_f32 v236, v236\n\t" \
    "v_exp_f32 v237, v237\n\t" \
    "v_exp_f32 v238, v238\n\t" \
    "v_exp_f32 v239, v239\n\t" \
    "v_cmp_lt_f32 vcc, 0x42400000, v82\n\t" \
    "v_exp_f32 v178, v178\n\t" \
    "v_exp_f32 v179, v179\n\t" \
    "v_cvt_pk_bf16_f32 v190, v176, v177\n\t" \
    "v_exp_f32 v148, v148\n\t" \
    "v_cvt_pk_bf16_f32 v191, v178, v179\n\t" \
    "v_exp_f32 v149, v149\n\t" \
    "v_exp_f32 v150, v150\n\t" \
    "v_mfma_f32_32x32x16_bf16 v[116:131], a[156:159], v[188:191], v[116:131]\n\t" \
    "s_waitcnt vmcnt(12)\n\t" \
      \
    "s_cbranch_vccnz .Lslow_%=\n\t" \
    "buffer_load_dwordx4 a[200:203], v2, s[8:11], s72 offen offset:2048 sc1\n\t" \
    "v_exp_f32 v151, v151\n\t" \
    "v_cvt_pk_bf16_f32 v184, v148, v149\n\t" \
    "v_exp_f32 v152, v152\n\t" \
    "v_exp_f32 v153, v153\n\t" \
    "v_cvt_pk_bf16_f32 v185, v150, v151\n\t" \
    "v_exp_f32 v154, v154\n\t" \
    "v_mfma_f32_32x32x16_bf16 v[164:179], a[52:55], v[180:183], v[100:115]\n\t" \
    "s_waitcnt vmcnt(11)\n\t" \
    "buffer_load_dwordx4 a[204:207], v2, s[8:11], s72 offen offset:3072 sc1\n\t" \
    "v_exp_f32 v155, v155\n\t" \
    "v_cvt_pk_bf16_f32 v186, v152, v153\n\t" \
    "v_exp_f32 v156, v156\n\t" \
    "v_cvt_pk_bf16_f32 v187, v154, v155\n\t" \
    "v_exp_f32 v157, v157\n\t" \
    "v_exp_f32 v158, v158\n\t" \
    "v_mfma_f32_32x32x16_bf16 v[116:131], a[160:163], v[184:187], v[116:131]\n\t" \
    "buffer_load_dwordx4 a[208:211], v2, s[8:11], s73 offen offset:0 sc1\n\t" \
    "buffer_load_dwordx4 a[212:215], v2, s[8:11], s73 offen offset:1024 sc1\n\t" \
    "v_exp_f32 v159, v159\n\t" \
    "v_cvt_pk_bf16_f32 v188, v156, v157\n\t" \
    "v_exp_f32 v160, v160\n\t" \
    "v_exp_f32 v161, v161\n\t" \
    "v_cvt_pk_bf16_f32 v189, v158, v159\n\t" \
    "v_exp_f32 v162, v162\n\t" \
    "v_exp_f32 v163, v163\n\t" \
    "v_cvt_pk_bf16_f32 v190, v160, v161\n\t" \
    "v_exp_f32 v164, v164\n\t" \
    "v_cvt_pk_bf16_f32 v191, v162, v163\n\t" \
    "v_exp_f32 v165, v165\n\t" \
    "v_exp_f32 v166, v166\n\t" \
    "v_mfma_f32_32x32x16_bf16 v[116:131], a[164:167], v[188:191], v[116:131]\n\t" \
    "s_waitcnt vmcnt(13)\n\t" \
    "buffer_load_dwordx4 a[216:219], v2, s[8:11], s73 offen offset:2048 sc1\n\t" \
    "v_exp_f32 v167, v167\n\t" \
    "v_cvt_pk_bf16_f32 v184, v164, v165\n\t" \
    "v_exp_f32 v168, v168\n\t" \
    "v_exp_f32 v169, v169\n\t" \
    "v_cvt_pk_bf16_f32 v185, v166, v167\n\t" \
    "v_exp_f32 v170, v170\n\t" \
    "v_mfma_f32_32x32x16_bf16 v[148:163], a[56:59], v[180:183], v[100:115]\n\t" \
    "s_waitcnt vmcnt(12)\n\t" \
    "buffer_load_dwordx4 a[220:223], v2, s[8:11], s73 offen offset:3072 sc1\n\t" \
    "v_exp_f32 v171, v171\n\t" \
    "v_cvt_pk_bf16_f32 v186, v168, v169\n\t" \
    "v_exp_f32 v172, v172\n\t" \
    "v_cvt_pk_bf16_f32 v187, v170, v171\n\t" \
    "v_exp_f32 v173, v173\n\t" \
    "v_exp_f32 v174, v174\n\t" \
    "v_mfma_f32_32x32x16_bf16 v[116:131], a[168:171], v[184:187], v[116:131]\n\t" \
    "buffer_load_dwordx4 a[224:227], v2, s[8:11], s74 offen offset:0 sc1\n\t" \
    "buffer_load_dwordx4 a[228:231], v2, s[8:11], s74 offen offset:1024 sc1\n\t" \
    "v_exp_f32 v175, v175\n\t" \
    "v_cvt_pk_bf16_f32 v188, v172, v173\n\t" \
    "v_exp_f32 v176, v176\n\t" \
    "v_exp_f32 v177, v177\n\t" \
    "v_cvt_pk_bf16_f32 v189, v174, v175\n\t" \
    "v_exp_f32 v178, v178\n\t" \
    "v_exp_f32 v179, v179\n\t" \
    "v_cvt_pk_bf16_f32 v190, v176, v177\n\t" \
    "v_exp_f32 v148, v148\n\t" \
    "v_cvt_pk_bf16_f32 v191, v178, v179\n\t" \
    "v_exp_f32 v149, v149\n\t" \
    "v_exp_f32 v150, v150\n\t" \
    "v_mfma_f32_32x32x16_bf16 v[116:131], a[172:175], v[188:191], v[116:131]\n\t" \
    "s_waitcnt vmcnt(14)\n\t" \
    "buffer_load_dwordx4 a[232:235], v2, s[8:11], s74 offen offset:2048 sc1\n\t" \
    "v_exp_f32 v151, v151\n\t" \
    "v_cvt_pk_bf16_f32 v184, v148, v149\n\t" \
    "v_exp_f32 v152, v152\n\t" \
    "v_exp_f32 v153, v153\n\t" \
    "v_cvt_pk_bf16_f32 v185, v150, v151\n\t" \
    "v_exp_f32 v154, v154\n\t" \
    "v_mfma_f32_32x32x16_bf16 v[164:179], a[60:63], v[180:183], v[100:115]\n\t" \
    "s_waitcnt vmcnt(13)\n\t" \
    "buffer_load_dwordx4 a[236:239], v2, s[8:11], s74 offen offset:3072 sc1\n\t" \
    "ds_read_b128 v[180:183], v249\n\t" \
    "v_exp_f32 v155, v155\n\t" \
    "v_cvt_pk_bf16_f32 v186, v152, v153\n\t" \
    "v_exp_f32 v156, v156\n\t" \
    "v_cvt_pk_bf16_f32 v187, v154, v155\n\t" \
    "v_exp_f32 v157, v157\n\t" \
    "v_exp_f32 v158, v158\n\t" \
    "v_mfma_f32_32x32x16_bf16 v[116:131], a[176:179], v[184:187], v[116:131]\n\t" \
    "s_cmp_lg_u32 s70, 0\n\t" \
    "buffer_load_dwordx4 a[240:243], v2, s[8:11], s75 offen offset:0 sc1\n\t" \
    "v_exp_f32 v159, v159\n\t" \
    "v_cvt_pk_bf16_f32 v188, v156, v157\n\t" \
    "v_exp_f32 v160, v160\n\t" \
    "v_exp_f32 v161, v161\n\t" \
    "v_cvt_pk_bf16_f32 v189, v158, v159\n\t" \
    "v_exp_f32 v162, v162\n\t" \
    "v_exp_f32 v163, v163\n\t" \
    "v_cvt_pk_bf16_f32 v190, v160, v161\n\t" \
    "v_exp_f32 v164, v164\n\t" \
    "v_cvt_pk_bf16_f32 v191, v162, v163\n\t" \
    "v_exp_f32 v165, v165\n\t" \
    "v_exp_f32 v166, v166\n\t" \
    "v_mfma_f32_32x32x16_bf16 v[116:131], a[180:183], v[188:191], v[116:131]\n\t" \
    "buffer_load_dwordx4 a[244:247], v2, s[8:11], s75 offen offset:1024 sc1\n\t" \
    "s_waitcnt lgkmcnt(0)\n\t" \
    "s_cbranch_scc1 .Lsl0_%=\n\t" \
    ".Lslr0_%=:\n\t" \
    "v_exp_f32 v167, v167\n\t" \
    "v_cvt_pk_bf16_f32 v184, v164, v165\n\t" \
    "v_exp_f32 v168, v168\n\t" \
    "v_exp_f32 v169, v169\n\t" \
    "v_cvt_pk_bf16_f32 v185, v166, v167\n\t" \
    "v_exp_f32 v170, v170\n\t" \
    "v_mfma_f32_32x32x16_bf16 v[148:163], a[0:3], v[180:183], v[100:115]\n\t" \
    "s_waitcnt vmcnt(14)\n\t" \
    "buffer_load_dwordx4 a[248:251], v2, s[8:11], s75 offen offset:2048 sc1\n\t" \
    "v_exp_f32 v171, v171\n\t" \
    "v_cvt_pk_bf16_f32 v186, v168, v169\n\t" \
    "v_exp_f32 v172, v172\n\t" \
    "v_cvt_pk_bf16_f32 v187, v170, v171\n\t" \
    "v_exp_f32 v173, v173\n\t" \
    "v_exp_f32 v174, v174\n\t" \
    "v_mfma_f32_32x32x16_bf16 v[116:131], a[184:187], v[184:187], v[116:131]\n\t" \
    "buffer_load_dwordx4 a[252:255], v2, s[8:11], s75 offen offset:3072 sc1\n\t" \
    "buffer_load_dwordx4 v[16:19], v25, s[12:15], s78 offen\n\t" \
    "buffer_load_dwordx4 v[20:23], v25, s[12:15], s78 offen offset:64\n\t" \
    "v_exp_f32 v175, v175\n\t" \
    "v_cvt_pk_bf16_f32 v188, v172, v173\n\t" \
    "v_add_u32 v249, 0x400, v249\n\t" \
    "v_exp_f32 v176, v176\n\t" \
    "v_exp_f32 v177, v177\n\t" \
    "v_cvt_pk_bf16_f32 v189, v174, v175\n\t" \
    "v_exp_f32 v178, v178\n\t" \
    "v_exp_f32 v179, v179\n\t" \
    "v_cvt_pk_bf16_f32 v190, v176, v177\n\t" \
    ".p2align 6\n\t" \
    "s_nop 0\n\t" \
    "s_nop 0\n\t" \
      \
    ".Lit_%=:\n\t" \
    "v_exp_f32 v148, v148\n\t" \
    "v_cvt_pk_bf16_f32 v191, v178, v179\n\t" \
    "v_exp_f32 v149, v149\n\t" \
    "v_exp_f32 v150, v150\n\t" \
    "v_mfma_f32_32x32x16_bf16 v[116:131], a[188:191], v[188:191], v[116:131]\n\t" \
    "buffer_load_dword v86, v81, s[8:11], s94 offen sc1\n\t" \
    "v_exp_f32 v151, v151\n\t" \
    "v_cvt_pk_bf16_f32 v184, v148, v149\n\t" \
    "v_exp_f32 v152, v152\n\t" \
    "v_exp_f32 v153, v153\n\t" \
    "v_cvt_pk_bf16_f32 v185, v150, v151\n\t" \
    "v_exp_f32 v154, v154\n\t" \
    "v_mfma_f32_32x32x16_bf16 v[164:179], a[4:7], v[180:183], v[100:115]\n\t" \
    "v_exp_f32 v155, v155\n\t" \
    "v_cvt_pk_bf16_f32 v186, v152, v153\n\t" \
    "v_exp_f32 v156, v156\n\t" \
    "v_cvt_pk_bf16_f32 v187, v154, v155\n\t" \
    "v_exp_f32 v157, v157\n\t" \
    "v_exp_f32 v158, v158\n\t" \
    "v_mfma_f32_32x32x16_bf16 v[132:147], a[64:67], v[184:187], 0\n\t" \
    "ds_write_b128 v242, v[116:119]\n\t" \
    "ds_write_b128 v242, v[120:123] offset:1024\n\t" \
    "ds_write_b32 v243, v124\n\t" \
    "ds_write_b32 v76, v79\n\t" \
    "v_exp_f32 v159, v159\n\t" \
    "v_cvt_pk_bf16_f32 v188, v156, v157\n\t" \
    "v_exp_f32 v160, v160\n\t" \
    "v_exp_f32 v161, v161\n\t" \
    "v_cvt_pk_bf16_f32 v189, v158, v159\n\t" \
    "v_exp_f32 v162, v162\n\t" \
    "v_exp_f32 v163, v163\n\t" \
    "v_cvt_pk_bf16_f32 v190, v160, v161\n\t" \
    "v_exp_f32 v164, v164\n\t" \
    "v_cvt_pk_bf16_f32 v191, v162, v163\n\t" \
    "v_exp_f32 v165, v165\n\t" \
    "v_exp_f32 v166, v166\n\t" \
    "v_mfma_f32_32x32x16_bf16 v[116:131], a[68:71], v[188:191], v[132:147]\n\t" \
    "s_mov_b32 m0, s69\n\t" \
    "v_exp_f32 v167, v167\n\t" \
    "v_cvt_pk_bf16_f32 v184, v164, v165\n\t" \
    "v_exp_f32 v168, v168\n\t" \
    "v_exp_f32 v169, v169\n\t" \
    "v_cvt_pk_bf16_f32 v185, v166, v167\n\t" \
    "v_exp_f32 v170, v170\n\t" \
    "v_mfma_f32_32x32x16_bf16 v[148:163], a[8:11], v[180:183], v[100:115]\n\t" \
    "buffer_load_dwordx4 v250, s[44:47], s68 offen lds\n\t" \
    "v_exp_f32 v171, v171\n\t" \
    "v_cvt_pk_bf16_f32 v186, v168, v169\n\t" \
    "v_exp_f32 v172, v172\n\t" \
    "v_cvt_pk_bf16_f32 v187, v170, v171\n\t" \
    "v_exp_f32 v173, v173\n\t" \
    "v_exp_f32 v174, v174\n\t" \
    "v_mfma_f32_32x32x16_bf16 v[116:131], a[72:75], v[184:187], v[116:131]\n\t" \
    "s_add_u32 s69, s69, 0x400\n\t" \
    "v_exp_f32 v175, v175\n\t" \
    "v_cvt_pk_bf16_f32 v188, v172, v173\n\t" \
    "v_exp_f32 v176, v176\n\t" \
    "v_exp_f32 v177, v177\n\t" \
    "v_cvt_pk_bf16_f32 v189, v174, v175\n\t" \
    "v_exp_f32 v178, v178\n\t" \
    "v_exp_f32 v179, v179\n\t" \
    "v_cvt_pk_bf16_f32 v190, v176, v177\n\t" \
    "v_exp_f32 v148, v148\n\t" \
    "v_cvt_pk_bf16_f32 v191, v178, v179\n\t" \
    "v_exp_f32 v149, v149\n\t" \
    "v_exp_f32 v150, v150\n\t" \
    "v_mfma_f32_32x32x16_bf16 v[116:131], a[76:79], v[188:191], v[116:131]\n\t" \
    "s_add_u32 s68, s68, 14336\n\t" \
    "v_exp_f32 v151, v151\n\t" \
    "v_cvt_pk_bf16_f32 v184, v148, v149\n\t" \
    "v_exp_f32 v152, v152\n\t" \
    "v_exp_f32 v153, v153\n\t" \
    "v_cvt_pk_bf16_f32 v185, v150, v151\n\t" \
    "v_exp_f32 v154, v154\n\t" \
    "v_mfma_f32_32x32x16_bf16 v[164:179], a[12:15], v[180:183], v[100:115]\n\t" \
    "s_mov_b32 m0, s69\n\t" \
    "v_exp_f32 v155, v155\n\t" \
    "v_cvt_pk_bf16_f32 v186, v152, v153\n\t" \
    "v_exp_f32 v156, v156\n\t" \
    "v_cvt_pk_bf16_f32 v187, v154, v155\n\t" \
    "v_exp_f32 v157, v157\n\t" \
    "v_exp_f32 v158, v158\n\t" \
    "v_mfma_f32_32x32x16_bf16 v[116:131], a[80:83], v[184:187], v[116:131]\n\t" \
    "ds_read_b32 v80, v78\n\t" \
    "ds_read_b64 v[224:225], v244 offset:0\n\t" \
    "ds_read_b64 v[226:227], v244 offset:2048\n\t" \
    "ds_read_b64 v[228:229], v244 offset:4096\n\t" \
    "ds_read_b64 v[230:231], v244 offset:6144\n\t" \
    "ds_read_b128 v[232:235], v245\n\t" \
    "v_exp_f32 v159, v159\n\t" \
    "v_cvt_pk_bf16_f32 v188, v156, v157\n\t" \
    "v_exp_f32 v160, v160\n\t" \
    "v_exp_f32 v161, v161\n\t" \
    "v_cvt_pk_bf16_f32 v189, v158, v159\n\t" \
    "v_exp_f32 v162, v162\n\t" \
    "v_exp_f32 v163, v163\n\t" \
    "v_cvt_pk_bf16_f32 v190, v160, v161\n\t" \
    "v_exp_f32 v164, v164\n\t" \
    "v_cvt_pk_bf16_f32 v191, v162, v163\n\t" \
    "v_exp_f32 v165, v165\n\t" \
    "v_exp_f32 v166, v166\n\t" \
    "v_mfma_f32_32x32x16_bf16 v[116:131], a[84:87], v[188:191], v[116:131]\n\t" \
    "buffer_load_dwordx4 v250, s[44:47], s68 offen lds\n\t" \
    "v_exp_f32 v167, v167\n\t" \
    "v_cvt_pk_bf16_f32 v184, v164, v165\n\t" \
    "v_exp_f32 v168, v168\n\t" \
    "v_exp_f32 v169, v169\n\t" \
    "v_cvt_pk_bf16_f32 v185, v166, v167\n\t" \
    "v_exp_f32 v170, v170\n\t" \
    "v_mfma_f32_32x32x16_bf16 v[148:163], a[16:19], v[180:183], v[100:115]\n\t" \
    "s_add_u32 s69, s69, 0x400\n\t" \
    "v_exp_f32 v171, v171\n\t" \
    "v_cvt_pk_bf16_f32 v186, v168, v169\n\t" \
    "v_exp_f32 v172, v172\n\t" \
    "v_cvt_pk_bf16_f32 v187, v170, v171\n\t" \
    "v_exp_f32 v173, v173\n\t" \
    "v_exp_f32 v174, v174\n\t" \
    "v_mfma_f32_32x32x16_bf16 v[116:131], a[88:91], v[184:187], v[116:131]\n\t" \
    "s_waitcnt lgkmcnt(0)\n\t" \
    "v_exp_f32 v175, v175\n\t" \
    "v_cvt_pk_bf16_f32 v188, v172, v173\n\t" \
    "v_cmp_ne_u32 vcc, s71, v80\n\t" \
    "v_mul_f32 v232, v232, v236\n\t" \
    "v_mul_f32 v224, v224, v236\n\t" \
    "v_mul_f32 v225, v225, v236\n\t" \
    "v_fmac_f32 v232, v233, v237\n\t" \
    "v_fmac_f32 v224, v226, v237\n\t" \
    "v_fmac_f32 v225, v227, v237\n\t" \
    "v_exp_f32 v176, v176\n\t" \
    "v_exp_f32 v177, v177\n\t" \
    "v_cvt_pk_bf16_f32 v189, v174, v175\n\t" \
    "v_fmac_f32 v232, v234, v238\n\t" \
    "v_fmac_f32 v224, v228, v238\n\t" \
    "v_fmac_f32 v225, v229, v238\n\t" \
    "v_fmac_f32 v232, v235, v239\n\t" \
    "v_fmac_f32 v224, v230, v239\n\t" \
    "v_rcp_f32 v232, v232\n\t" \
    "v_fmac_f32 v225, v231, v239\n\t" \
    "v_exp_f32 v178, v178\n\t" \
    "v_exp_f32 v179, v179\n\t" \
    "v_cvt_pk_bf16_f32 v190, v176, v177\n\t" \
    "v_exp_f32 v148, v148\n\t" \
    "v_cvt_pk_bf16_f32 v191, v178, v179\n\t" \
    "v_exp_f32 v149, v149\n\t" \
    "v_exp_f32 v150, v150\n\t" \
    "v_mfma_f32_32x32x16_bf16 v[116:131], a[92:95], v[188:191], v[116:131]\n\t" \
    "s_cbranch_vccnz .Lr11_%=\n\t" \
    ".Lr1r1_%=:\n\t" \
    "v_exp_f32 v151, v151\n\t" \
    "v_cvt_pk_bf16_f32 v184, v148, v149\n\t" \
    "v_exp_f32 v152, v152\n\t" \
    "v_exp_f32 v153, v153\n\t" \
    "v_cvt_pk_bf16_f32 v185, v150, v151\n\t" \
    "v_exp_f32 v154, v154\n\t" \
    "v_mfma_f32_32x32x16_bf16 v[164:179], a[20:23], v[180:183], v[100:115]\n\t" \
    "s_add_u32 s68, s68, 14336\n\t" \
    "v_exp_f32 v155, v155\n\t" \
    "v_cvt_pk_bf16_f32 v186, v152, v153\n\t" \
    "v_exp_f32 v156, v156\n\t" \
    "v_cvt_pk_bf16_f32 v187, v154, v155\n\t" \
    "v_exp_f32 v157, v157\n\t" \
    "v_exp_f32 v158, v158\n\t" \
    "v_mfma_f32_32x32x16_bf16 v[116:131], a[96:99], v[184:187], v[116:131]\n\t" \
    "s_mov_b32 m0, s69\n\t" \
    "v_exp_f32 v159, v159\n\t" \
    "v_cvt_pk_bf16_f32 v188, v156, v157\n\t" \
    "v_mul_f32 v224, v224, v232\n\t" \
    "v_mul_f32 v225, v225, v232\n\t" \
    "v_cvt_pk_bf16_f32 v224, v224, v225\n\t" \
    "v_exp_f32 v160, v160\n\t" \
    "v_exp_f32 v161, v161\n\t" \
    "v_cvt_pk_bf16_f32 v189, v158, v159\n\t" \
    "v_exp_f32 v162, v162\n\t" \
    "v_exp_f32 v163, v163\n\t" \
    "v_cvt_pk_bf16_f32 v190, v160, v161\n\t" \
    "v_exp_f32 v164, v164\n\t" \
    "v_cvt_pk_bf16_f32 v191, v162, v163\n\t" \
    "v_exp_f32 v165, v165\n\t" \
    "v_exp_f32 v166, v166\n\t" \
    "v_mfma_f32_32x32x16_bf16 v[116:131], a[100:103], v[188:191], v[116:131]\n\t" \
    "ds_write_b32 v246, v224\n\t" \
    "ds_write_b32 v77, v79\n\t" \
    "v_exp_f32 v167, v167\n\t" \
    "v_cvt_pk_bf16_f32 v184, v164, v165\n\t" \
    "v_exp_f32 v168, v168\n\t" \
    "v_exp_f32 v169, v169\n\t" \
    "v_cvt_pk_bf16_f32 v185, v166, v167\n\t" \
    "v_exp_f32 v170, v170\n\t" \
    "v_mfma_f32_32x32x16_bf16 v[148:163], a[24:27], v[180:183], v[100:115]\n\t" \
    "buffer_load_dwordx4 v251, s[44:47], s68 offen lds\n\t" \
    "v_exp_f32 v171, v171\n\t" \
    "v_cvt_pk_bf16_f32 v186, v168, v169\n\t" \
    "v_exp_f32 v172, v172\n\t" \
    "v_cvt_pk_bf16_f32 v187, v170, v171\n\t" \
    "v_exp_f32 v173, v173\n\t" \
    "v_exp_f32 v174, v174\n\t" \
    "v_mfma_f32_32x32x16_bf16 v[116:131], a[104:107], v[184:187], v[116:131]\n\t" \
    "s_add_u32 s69, s69, 0x400\n\t" \
    "v_exp_f32 v175, v175\n\t" \
    "v_cvt_pk_bf16_f32 v188, v172, v173\n\t" \
    "v_exp_f32 v176, v176\n\t" \
    "v_exp_f32 v177, v177\n\t" \
    "v_cvt_pk_bf16_f32 v189, v174, v175\n\t" \
    "v_exp_f32 v178, v178\n\t" \
    "v_exp_f32 v179, v179\n\t" \
    "v_cvt_pk_bf16_f32 v190, v176, v177\n\t" \
    "v_exp_f32 v148, v148\n\t" \
    "v_cvt_pk_bf16_f32 v191, v178, v179\n\t" \
    "v_exp_f32 v149, v149\n\t" \
    "v_exp_f32 v150, v150\n\t" \
    "v_mfma_f32_32x32x16_bf16 v[116:131], a[108:111], v[188:191], v[116:131]\n\t" \
    "s_add_u32 s68, s68, 14336\n\t" \
    "v_exp_f32 v151, v151\n\t" \
    "v_cvt_pk_bf16_f32 v184, v148, v149\n\t" \
    "v_exp_f32 v152, v152\n\t" \
    "v_exp_f32 v153, v153\n\t" \
    "v_cvt_pk_bf16_f32 v185, v150, v151\n\t" \
    "v_exp_f32 v154, v154\n\t" \
    "v_mfma_f32_32x32x16_bf16 v[164:179], a[28:31], v[180:183], v[100:115]\n\t" \
    "s_mov_b32 m0, s69\n\t" \
    "v_exp_f32 v155, v155\n\t" \
    "v_cvt_pk_bf16_f32 v186, v152, v153\n\t" \
    "v_exp_f32 v156, v156\n\t" \
    "v_cvt_pk_bf16_f32 v187, v154, v155\n\t" \
    "v_exp_f32 v157, v157\n\t" \
    "v_exp_f32 v158, v158\n\t" \
    "v_mfma_f32_32x32x16_bf16 v[116:131], a[112:115], v[184:187], v[116:131]\n\t" \
    "buffer_load_dwordx4 v251, s[44:47], s68 offen lds\n\t" \
    "v_exp_f32 v159, v159\n\t" \
    "v_cvt_pk_bf16_f32 v188, v156, v157\n\t" \
    "v_exp_f32 v160, v160\n\t" \
    "v_exp_f32 v161, v161\n\t" \
    "v_cvt_pk_bf16_f32 v189, v158, v159\n\t" \
    "v_exp_f32 v162, v162\n\t" \
    "v_exp_f32 v163, v163\n\t" \
    "v_cvt_pk_bf16_f32 v190, v160, v161\n\t" \
    "v_exp_f32 v164, v164\n\t" \
    "v_cvt_pk_bf16_f32 v191, v162, v163\n\t" \
    "v_exp_f32 v165, v165\n\t" \
    "v_exp_f32 v166, v166\n\t" \
    "v_mfma_f32_32x32x16_bf16 v[116:131], a[116:119], v[188:191], v[116:131]\n\t" \
    "ds_read_b32 v80, v78 offset:16\n\t" \
    "ds_read_b128 v[208:211], v247 offset:0\n\t" \
    "ds_read_b128 v[212:215], v247 offset:64\n\t" \
    "ds_read_b128 v[216:219], v248 offset:0\n\t" \
    "ds_read_b128 v[220:223], v248 offset:64\n\t" \
    "v_exp_f32 v167, v167\n\t" \
    "v_cvt_pk_bf16_f32 v184, v164, v165\n\t" \
    "v_exp_f32 v168, v168\n\t" \
    "v_exp_f32 v169, v169\n\t" \
    "v_cvt_pk_bf16_f32 v185, v166, v167\n\t" \
    "v_exp_f32 v170, v170\n\t" \
    "v_mfma_f32_32x32x16_bf16 v[148:163], a[32:35], v[180:183], v[100:115]\n\t" \
    "s_add_u32 s69, s69, 0x400\n\t" \
    "s_mov_b32 s72, s67\n\t" \
    "v_exp_f32 v171, v171\n\t" \
    "v_cvt_pk_bf16_f32 v186, v168, v169\n\t" \
    "v_exp_f32 v172, v172\n\t" \
    "v_cvt_pk_bf16_f32 v187, v170, v171\n\t" \
    "v_exp_f32 v173, v173\n\t" \
    "v_exp_f32 v174, v174\n\t" \
    "v_mfma_f32_32x32x16_bf16 v[116:131], a[120:123], v[184:187], v[116:131]\n\t" \
    "s_waitcnt vmcnt(5) lgkmcnt(0)\n\t" \
    "v_exp_f32 v175, v175\n\t" \
    "v_cvt_pk_bf16_f32 v188, v172, v173\n\t" \
    "v_exp_f32 v176, v176\n\t" \
    "v_exp_f32 v177, v177\n\t" \
    "v_cvt_pk_bf16_f32 v189, v174, v175\n\t" \
    "v_cmp_ne_u32 vcc, s71, v80\n\t" \
    "v_exp_f32 v178, v178\n\t" \
    "v_exp_f32 v179, v179\n\t" \
    "v_cvt_pk_bf16_f32 v190, v176, v177\n\t" \
    "v_exp_f32 v148, v148\n\t" \
    "v_cvt_pk_bf16_f32 v191, v178, v179\n\t" \
    "v_exp_f32 v149, v149\n\t" \
    "v_exp_f32 v150, v150\n\t" \
    "v_mfma_f32_32x32x16_bf16 v[116:131], a[124:127], v[188:191], v[116:131]\n\t" \
    "s_cbranch_vccnz .Lr21_%=\n\t" \
    ".Lr2r1_%=:\n\t" \
    "v_exp_f32 v151, v151\n\t" \
    "v_cvt_pk_bf16_f32 v184, v148, v149\n\t" \
    "v_exp_f32 v152, v152\n\t" \
    "v_exp_f32 v153, v153\n\t" \
    "v_cvt_pk_bf16_f32 v185, v150, v151\n\t" \
    "v_exp_f32 v154, v154\n\t" \
    "v_mfma_f32_32x32x16_bf16 v[164:179], a[36:39], v[180:183], v[100:115]\n\t" \
    "s_add_u32 s73, s67, 0x1000\n\t" \
    "v_exp_f32 v155, v155\n\t" \
    "v_cvt_pk_bf16_f32 v186, v152, v153\n\t" \
    "v_exp_f32 v156, v156\n\t" \
    "v_cvt_pk_bf16_f32 v187, v154, v155\n\t" \
    "v_exp_f32 v157, v157\n\t" \
    "v_exp_f32 v158, v158\n\t" \
    "v_mfma_f32_32x32x16_bf16 v[116:131], a[128:131], v[184:187], v[116:131]\n\t" \
    "s_add_u32 s74, s67, 0x2000\n\t" \
    "v_exp_f32 v159, v159\n\t" \
    "v_cvt_pk_bf16_f32 v188, v156, v157\n\t" \
    "v_exp_f32 v160, v160\n\t" \
    "v_exp_f32 v161, v161\n\t" \
    "v_mfma_f32_16x16x32_bf16 v[192:195], a[192:195], v[208:211], v[192:195]\n\t" \
    "buffer_load_dwordx4 a[192:195], v2, s[8:11], s72 offen offset:0 sc1\n\t" \
    "v_cvt_pk_bf16_f32 v189, v158, v159\n\t" \
    "v_exp_f32 v162, v162\n\t" \
    "v_exp_f32 v163, v163\n\t" \
    "v_mfma_f32_16x16x32_bf16 v[196:199], a[196:199], v[208:211], v[196:199]\n\t" \
    "buffer_load_dwordx4 a[196:199], v2, s[8:11], s72 offen offset:1024 sc1\n\t" \
    "v_cvt_pk_bf16_f32 v190, v160, v161\n\t" \
    "v_exp_f32 v164, v164\n\t" \
    "v_cvt_pk_bf16_f32 v191, v162, v163\n\t" \
    "v_exp_f32 v165, v165\n\t" \
    "v_exp_f32 v166, v166\n\t" \
    "v_mfma_f32_32x32x16_bf16 v[116:131], a[132:135], v[188:191], v[116:131]\n\t" \
    "s_add_u32 s75, s67, 0x3000\n\t" \
    "v_exp_f32 v167, v167\n\t" \
    "v_cvt_pk_bf16_f32 v184, v164, v165\n\t" \
    "v_exp_f32 v168, v168\n\t" \
    "v_exp_f32 v169, v169\n\t" \
    "v_cvt_pk_bf16_f32 v185, v166, v167\n\t" \
    "v_exp_f32 v170, v170\n\t" \
    "v_mfma_f32_32x32x16_bf16 v[148:163], a[40:43], v[180:183], v[100:115]\n\t" \
    "s_add_u32 s67, s67, 0x10000\n\t" \
    "v_exp_f32 v171, v171\n\t" \
    "v_cvt_pk_bf16_f32 v186, v168, v169\n\t" \
    "v_exp_f32 v172, v172\n\t" \
    "v_cvt_pk_bf16_f32 v187, v170, v171\n\t" \
    "v_exp_f32 v173, v173\n\t" \
    "v_exp_f32 v174, v174\n\t" \
    "v_mfma_f32_32x32x16_bf16 v[116:131], a[136:139], v[184:187], v[116:131]\n\t" \
    "s_add_u32 s68, s68, 14336\n\t" \
    "v_exp_f32 v175, v175\n\t" \
    "v_cvt_pk_bf16_f32 v188, v172, v173\n\t" \
    "v_exp_f32 v176, v176\n\t" \
    "v_exp_f32 v177, v177\n\t" \
    "v_mfma_f32_16x16x32_bf16 v[200:203], a[200:203], v[208:211], v[200:203]\n\t" \
    "buffer_load_dwordx4 a[200:203], v2, s[8:11], s72 offen offset:2048 sc1\n\t" \
    "v_cvt_pk_bf16_f32 v189, v174, v175\n\t" \
    "v_exp_f32 v178, v178\n\t" \
    "v_exp_f32 v179, v179\n\t" \
    "v_mfma_f32_16x16x32_bf16 v[204:207], a[204:207], v[208:211], v[204:207]\n\t" \
    "buffer_load_dwordx4 a[204:207], v2, s[8:11], s72 offen offset:3072 sc1\n\t" \
    "v_cvt_pk_bf16_f32 v190, v176, v177\n\t" \
    "v_exp_f32 v148, v148\n\t" \
    "v_cvt_pk_bf16_f32 v191, v178, v179\n\t" \
    "v_exp_f32 v149, v149\n\t" \
    "v_exp_f32 v150, v150\n\t" \
    "v_mfma_f32_32x32x16_bf16 v[116:131], a[140:143], v[188:191], v[116:131]\n\t" \
    "v_exp_f32 v151, v151\n\t" \
    "v_cvt_pk_bf16_f32 v184, v148, v149\n\t" \
    "v_exp_f32 v152, v152\n\t" \
    "v_exp_f32 v153, v153\n\t" \
    "v_cvt_pk_bf16_f32 v185, v150, v151\n\t" \
    "v_exp_f32 v154, v154\n\t" \
    "v_mfma_f32_32x32x16_bf16 v[164:179], a[44:47], v[180:183], v[100:115]\n\t" \
    "v_exp_f32 v155, v155\n\t" \
    "v_cvt_pk_bf16_f32 v186, v152, v153\n\t" \
    "v_exp_f32 v156, v156\n\t" \
    "v_cvt_pk_bf16_f32 v187, v154, v155\n\t" \
    "v_exp_f32 v157, v157\n\t" \
    "v_exp_f32 v158, v158\n\t" \
    "v_mfma_f32_32x32x16_bf16 v[116:131], a[144:147], v[184:187], v[116:131]\n\t" \
    "v_exp_f32 v159, v159\n\t" \
    "v_cvt_pk_bf16_f32 v188, v156, v157\n\t" \
    "v_exp_f32 v160, v160\n\t" \
    "v_exp_f32 v161, v161\n\t" \
    "v_mfma_f32_16x16x32_bf16 v[192:195], a[208:211], v[216:219], v[192:195]\n\t" \
    "buffer_load_dwordx4 a[208:211], v2, s[8:11], s73 offen offset:0 sc1\n\t" \
    "v_cvt_pk_bf16_f32 v189, v158, v159\n\t" \
    "v_exp_f32 v162, v162\n\t" \
    "v_exp_f32 v163, v163\n\t" \
    "v_mfma_f32_16x16x32_bf16 v[196:199], a[212:215], v[216:219], v[196:199]\n\t" \
    "buffer_load_dwordx4 a[212:215], v2, s[8:11], s73 offen offset:1024 sc1\n\t" \
    "v_cvt_pk_bf16_f32 v190, v160, v161\n\t" \
    "v_exp_f32 v164, v164\n\t" \
    "v_cvt_pk_bf16_f32 v191, v162, v163\n\t" \
    "v_exp_f32 v165, v165\n\t" \
    "v_exp_f32 v166, v166\n\t" \
    "v_mfma_f32_32x32x16_bf16 v[116:131], a[148:151], v[188:191], v[116:131]\n\t" \
    "s_add_u32 s94, s94, 0xe00\n\t" \
    "v_exp_f32 v167, v167\n\t" \
    "v_cvt_pk_bf16_f32 v184, v164, v165\n\t" \
    "v_exp_f32 v168, v168\n\t" \
    "v_exp_f32 v169, v169\n\t" \
    "v_cvt_pk_bf16_f32 v185, v166, v167\n\t" \
    "v_exp_f32 v170, v170\n\t" \
    "v_mfma_f32_32x32x16_bf16 v[148:163], a[48:51], v[180:183], v[100:115]\n\t" \
    "v_exp_f32 v171, v171\n\t" \
    "v_cvt_pk_bf16_f32 v186, v168, v169\n\t" \
    "v_exp_f32 v172, v172\n\t" \
    "v_cvt_pk_bf16_f32 v187, v170, v171\n\t" \
    "v_exp_f32 v173, v173\n\t" \
    "v_exp_f32 v174, v174\n\t" \
    "v_mfma_f32_32x32x16_bf16 v[116:131], a[152:155], v[184:187], v[116:131]\n\t" \
    "v_exp_f32 v175, v175\n\t" \
    "v_cvt_pk_bf16_f32 v188, v172, v173\n\t" \
    "v_exp_f32 v176, v176\n\t" \
    "v_exp_f32 v177, v177\n\t" \
    "v_mfma_f32_16x16x32_bf16 v[200:203], a[216:219], v[216:219], v[200:203]\n\t" \
    "buffer_load_dwordx4 a[216:219], v2, s[8:11], s73 offen offset:2048 sc1\n\t" \
    "v_cvt_pk_bf16_f32 v189, v174, v175\n\t" \
    "v_exp_f32 v178, v178\n\t" \
    "v_exp_f32 v179, v179\n\t" \
    "v_mfma_f32_16x16x32_bf16 v[204:207], a[220:223], v[216:219], v[204:207]\n\t" \
    "buffer_load_dwordx4 a[220:223], v2, s[8:11], s73 offen offset:3072 sc1\n\t" \
    "v_cvt_pk_bf16_f32 v190, v176, v177\n\t" \
    "v_exp_f32 v148, v148\n\t" \
    "v_cvt_pk_bf16_f32 v191, v178, v179\n\t" \
    "v_exp_f32 v149, v149\n\t" \
    "v_exp_f32 v150, v150\n\t" \
    "v_mfma_f32_32x32x16_bf16 v[116:131], a[156:159], v[188:191], v[116:131]\n\t" \
    "v_exp_f32 v151, v151\n\t" \
    "v_cvt_pk_bf16_f32 v184, v148, v149\n\t" \
    "v_exp_f32 v152, v152\n\t" \
    "v_exp_f32 v153, v153\n\t" \
    "v_cvt_pk_bf16_f32 v185, v150, v151\n\t" \
    "v_exp_f32 v154, v154\n\t" \
    "v_mfma_f32_32x32x16_bf16 v[164:179], a[52:55], v[180:183], v[100:115]\n\t" \
    "v_exp_f32 v155, v155\n\t" \
    "v_cvt_pk_bf16_f32 v186, v152, v153\n\t" \
    "v_exp_f32 v156, v156\n\t" \
    "v_cvt_pk_bf16_f32 v187, v154, v155\n\t" \
    "v_exp_f32 v157, v157\n\t" \
    "v_exp_f32 v158, v158\n\t" \
    "v_mfma_f32_32x32x16_bf16 v[116:131], a[160:163], v[184:187], v[116:131]\n\t" \
    "v_exp_f32 v159, v159\n\t" \
    "v_cvt_pk_bf16_f32 v188, v156, v157\n\t" \
    "v_exp_f32 v160, v160\n\t" \
    "v_exp_f32 v161, v161\n\t" \
    "v_mfma_f32_16x16x32_bf16 v[192:195], a[224:227], v[212:215], v[192:195]\n\t" \
    "buffer_load_dwordx4 a[224:227], v2, s[8:11], s74 offen offset:0 sc1\n\t" \
    "v_cvt_pk_bf16_f32 v189, v158, v159\n\t" \
    "v_exp_f32 v162, v162\n\t" \
    "v_exp_f32 v163, v163\n\t" \
    "v_mfma_f32_16x16x32_bf16 v[196:199], a[228:231], v[212:215], v[196:199]\n\t" \
    "buffer_load_dwordx4 a[228:231], v2, s[8:11], s74 offen offset:1024 sc1\n\t" \
    "v_cvt_pk_bf16_f32 v190, v160, v161\n\t" \
    "v_exp_f32 v164, v164\n\t" \
    "v_cvt_pk_bf16_f32 v191, v162, v163\n\t" \
    "v_exp_f32 v165, v165\n\t" \
    "v_exp_f32 v166, v166\n\t" \
    "v_mfma_f32_32x32x16_bf16 v[116:131], a[164:167], v[188:191], v[116:131]\n\t" \
    "v_exp_f32 v167, v167\n\t" \
    "v_cvt_pk_bf16_f32 v184, v164, v165\n\t" \
    "v_exp_f32 v168, v168\n\t" \
    "v_exp_f32 v169, v169\n\t" \
    "v_cvt_pk_bf16_f32 v185, v166, v167\n\t" \
    "v_exp_f32 v170, v170\n\t" \
    "v_mfma_f32_32x32x16_bf16 v[148:163], a[56:59], v[180:183], v[100:115]\n\t" \
    "v_exp_f32 v171, v171\n\t" \
    "v_cvt_pk_bf16_f32 v186, v168, v169\n\t" \
    "v_exp_f32 v172, v172\n\t" \
    "v_cvt_pk_bf16_f32 v187, v170, v171\n\t" \
    "v_exp_f32 v173, v173\n\t" \
    "v_exp_f32 v174, v174\n\t" \
    "v_mfma_f32_32x32x16_bf16 v[116:131], a[168:171], v[184:187], v[116:131]\n\t" \
    "s_add_u32 s71, s71, 1\n\t" \
    "v_exp_f32 v175, v175\n\t" \
    "v_cvt_pk_bf16_f32 v188, v172, v173\n\t" \
    "v_exp_f32 v176, v176\n\t" \
    "v_exp_f32 v177, v177\n\t" \
    "v_mfma_f32_16x16x32_bf16 v[200:203], a[232:235], v[212:215], v[200:203]\n\t" \
    "buffer_load_dwordx4 a[232:235], v2, s[8:11], s74 offen offset:2048 sc1\n\t" \
    "v_cvt_pk_bf16_f32 v189, v174, v175\n\t" \
    "v_exp_f32 v178, v178\n\t" \
    "v_exp_f32 v179, v179\n\t" \
    "v_mfma_f32_16x16x32_bf16 v[204:207], a[236:239], v[212:215], v[204:207]\n\t" \
    "buffer_load_dwordx4 a[236:239], v2, s[8:11], s74 offen offset:3072 sc1\n\t" \
    "v_cvt_pk_bf16_f32 v190, v176, v177\n\t" \
    "v_exp_f32 v148, v148\n\t" \
    "v_cvt_pk_bf16_f32 v191, v178, v179\n\t" \
    "v_exp_f32 v149, v149\n\t" \
    "v_exp_f32 v150, v150\n\t" \
    "v_mfma_f32_32x32x16_bf16 v[116:131], a[172:175], v[188:191], v[116:131]\n\t" \
    "v_exp_f32 v151, v151\n\t" \
    "v_cvt_pk_bf16_f32 v184, v148, v149\n\t" \
    "v_exp_f32 v152, v152\n\t" \
    "v_exp_f32 v153, v153\n\t" \
    "v_cvt_pk_bf16_f32 v185, v150, v151\n\t" \
    "v_exp_f32 v154, v154\n\t" \
    "v_mfma_f32_32x32x16_bf16 v[164:179], a[60:63], v[180:183], v[100:115]\n\t" \
    "ds_read_b128 v[180:183], v249\n\t" \
    "v_exp_f32 v155, v155\n\t" \
    "v_cvt_pk_bf16_f32 v186, v152, v153\n\t" \
    "v_exp_f32 v156, v156\n\t" \
    "v_cvt_pk_bf16_f32 v187, v154, v155\n\t" \
    "v_exp_f32 v157, v157\n\t" \
    "v_exp_f32 v158, v158\n\t" \
    "v_mfma_f32_32x32x16_bf16 v[116:131], a[176:179], v[184:187], v[116:131]\n\t" \
    "s_cmp_lg_u32 s70, 0\n\t" \
    "v_exp_f32 v159, v159\n\t" \
    "v_cvt_pk_bf16_f32 v188, v156, v157\n\t" \
    "v_mov_b32 v79, s71\n\t" \
    "v_exp_f32 v160, v160\n\t" \
    "v_exp_f32 v161, v161\n\t" \
    "v_mfma_f32_16x16x32_bf16 v[192:195], a[240:243], v[220:223], v[192:195]\n\t" \
    "buffer_load_dwordx4 a[240:243], v2, s[8:11], s75 offen offset:0 sc1\n\t" \
    "v_cvt_pk_bf16_f32 v189, v158, v159\n\t" \
    "v_exp_f32 v162, v162\n\t" \
    "v_exp_f32 v163, v163\n\t" \
    "v_mfma_f32_16x16x32_bf16 v[196:199], a[244:247], v[220:223], v[196:199]\n\t" \
    "buffer_load_dwordx4 a[244:247], v2, s[8:11], s75 offen offset:1024 sc1\n\t" \
    "v_cvt_pk_bf16_f32 v190, v160, v161\n\t" \
    "v_exp_f32 v164, v164\n\t" \
    "v_cvt_pk_bf16_f32 v191, v162, v163\n\t" \
    "v_exp_f32 v165, v165\n\t" \
    "v_exp_f32 v166, v166\n\t" \
    "v_mfma_f32_32x32x16_bf16 v[116:131], a[180:183], v[188:191], v[116:131]\n\t" \
    "s_waitcnt lgkmcnt(0)\n\t" \
    "s_cbranch_scc1 .Lsl1_%=\n\t" \
    ".Lslr1_%=:\n\t" \
    "v_exp_f32 v167, v167\n\t" \
    "v_cvt_pk_bf16_f32 v184, v164, v165\n\t" \
    "v_exp_f32 v168, v168\n\t" \
    "v_exp_f32 v169, v169\n\t" \
    "v_cvt_pk_bf16_f32 v185, v166, v167\n\t" \
    "v_exp_f32 v170, v170\n\t" \
    "v_mfma_f32_32x32x16_bf16 v[148:163], a[0:3], v[180:183], v[100:115]\n\t" \
    "v_exp_f32 v171, v171\n\t" \
    "v_cvt_pk_bf16_f32 v186, v168, v169\n\t" \
    "v_exp_f32 v172, v172\n\t" \
    "v_cvt_pk_bf16_f32 v187, v170, v171\n\t" \
    "v_exp_f32 v173, v173\n\t" \
    "v_exp_f32 v174, v174\n\t" \
    "v_mfma_f32_32x32x16_bf16 v[116:131], a[184:187], v[184:187], v[116:131]\n\t" \
    "s_cmp_lt_u32 s71, 7\n\t" \
    "v_exp_f32 v175, v175\n\t" \
    "v_cvt_pk_bf16_f32 v188, v172, v173\n\t" \
    "v_add_u32 v249, 0x400, v249\n\t" \
    "v_exp_f32 v176, v176\n\t" \
    "v_exp_f32 v177, v177\n\t" \
    "v_mfma_f32_16x16x32_bf16 v[200:203], a[248:251], v[220:223], v[200:203]\n\t" \
    "buffer_load_dwordx4 a[248:251], v2, s[8:11], s75 offen offset:2048 sc1\n\t" \
    "v_cvt_pk_bf16_f32 v189, v174, v175\n\t" \
    "v_exp_f32 v178, v178\n\t" \
    "v_exp_f32 v179, v179\n\t" \
    "v_mfma_f32_16x16x32_bf16 v[204:207], a[252:255], v[220:223], v[204:207]\n\t" \
    "buffer_load_dwordx4 a[252:255], v2, s[8:11], s75 offen offset:3072 sc1\n\t" \
    "v_cvt_pk_bf16_f32 v190, v176, v177\n\t" \
    "s_cbranch_scc1 .Lit_%=\n\t" \
    "v_exp_f32 v148, v148\n\t" \
    "v_cvt_pk_bf16_f32 v191, v178, v179\n\t" \
    "v_exp_f32 v149, v149\n\t" \
    "v_exp_f32 v150, v150\n\t" \
    "v_mfma_f32_32x32x16_bf16 v[116:131], a[188:191], v[188:191], v[116:131]\n\t" \
    "buffer_load_dwordx2 v[84:85], v24, s[8:11], 0 offen sc1\n\t" \
    "v_exp_f32 v151, v151\n\t" \
    "v_cvt_pk_bf16_f32 v184, v148, v149\n\t" \
    "v_exp_f32 v152, v152\n\t" \
    "v_exp_f32 v153, v153\n\t" \
    "v_cvt_pk_bf16_f32 v185, v150, v151\n\t" \
    "v_exp_f32 v154, v154\n\t" \
    "v_mfma_f32_32x32x16_bf16 v[164:179], a[4:7], v[180:183], v[100:115]\n\t" \
    "s_mov_b32 m0, s69\n\t" \
    "s_add_u32 s69, s69, 0x400\n\t" \
    "buffer_load_dwordx4 v250, s[44:47], s68 offen lds\n\t" \
    "s_add_u32 s68, s68, 14336\n\t" \
    "s_mov_b32 m0, s69\n\t" \
    "s_add_u32 s69, s69, 0x400\n\t" \
    "buffer_load_dwordx4 v250, s[44:47], s68 offen lds\n\t" \
    "s_add_u32 s68, s68, 14336\n\t" \
    "s_mov_b32 m0, s69\n\t" \
    "s_add_u32 s69, s69, 0x400\n\t" \
    "buffer_load_dwordx4 v251, s[44:47], s68 offen lds\n\t" \
    "s_add_u32 s68, s68, 14336\n\t" \
    "s_mov_b32 m0, s69\n\t" \
    "s_add_u32 s69, s69, 0x400\n\t" \
    "buffer_load_dwordx4 v251, s[44:47], s68 offen lds\n\t" \
    "s_add_u32 s68, s68, 14336\n\t" \
    "v_exp_f32 v155, v155\n\t" \
    "v_cvt_pk_bf16_f32 v186, v152, v153\n\t" \
    "v_exp_f32 v156, v156\n\t" \
    "v_cvt_pk_bf16_f32 v187, v154, v155\n\t" \
    "v_exp_f32 v157, v157\n\t" \
    "v_exp_f32 v158, v158\n\t" \
    "v_mfma_f32_32x32x16_bf16 v[132:147], a[64:67], v[184:187], 0\n\t" \
    "ds_write_b128 v242, v[116:119]\n\t" \
    "ds_write_b128 v242, v[120:123] offset:1024\n\t" \
    "ds_write_b32 v243, v124\n\t" \
    "ds_write_b32 v76, v79\n\t" \
    "v_exp_f32 v159, v159\n\t" \
    "v_cvt_pk_bf16_f32 v188, v156, v157\n\t" \
    "v_exp_f32 v160, v160\n\t" \
    "v_exp_f32 v161, v161\n\t" \
    "v_cvt_pk_bf16_f32 v189, v158, v159\n\t" \
    "v_exp_f32 v162, v162\n\t" \
    "v_exp_f32 v163, v163\n\t" \
    "v_cvt_pk_bf16_f32 v190, v160, v161\n\t" \
    "v_exp_f32 v164, v164\n\t" \
    "v_cvt_pk_bf16_f32 v191, v162, v163\n\t" \
    "v_exp_f32 v165, v165\n\t" \
    "v_exp_f32 v166, v166\n\t" \
    "v_mfma_f32_32x32x16_bf16 v[116:131], a[68:71], v[188:191], v[132:147]\n\t" \
    "s_mov_b32 m0, s69\n\t" \
    "v_exp_f32 v167, v167\n\t" \
    "v_cvt_pk_bf16_f32 v184, v164, v165\n\t" \
    "v_exp_f32 v168, v168\n\t" \
    "v_exp_f32 v169, v169\n\t" \
    "v_cvt_pk_bf16_f32 v185, v166, v167\n\t" \
    "v_exp_f32 v170, v170\n\t" \
    "v_mfma_f32_32x32x16_bf16 v[148:163], a[8:11], v[180:183], v[100:115]\n\t" \
    "buffer_load_dwordx4 v250, s[44:47], s68 offen lds\n\t" \
    "v_exp_f32 v171, v171\n\t" \
    "v_cvt_pk_bf16_f32 v186, v168, v169\n\t" \
    "v_exp_f32 v172, v172\n\t" \
    "v_cvt_pk_bf16_f32 v187, v170, v171\n\t" \
    "v_exp_f32 v173, v173\n\t" \
    "v_exp_f32 v174, v174\n\t" \
    "v_mfma_f32_32x32x16_bf16 v[116:131], a[72:75], v[184:187], v[116:131]\n\t" \
    "s_add_u32 s69, s69, 0x400\n\t" \
    "v_exp_f32 v175, v175\n\t" \
    "v_cvt_pk_bf16_f32 v188, v172, v173\n\t" \
    "v_exp_f32 v176, v176\n\t" \
    "v_exp_f32 v177, v177\n\t" \
    "v_cvt_pk_bf16_f32 v189, v174, v175\n\t" \
    "v_exp_f32 v178, v178\n\t" \
    "v_exp_f32 v179, v179\n\t" \
    "v_cvt_pk_bf16_f32 v190, v176, v177\n\t" \
    "v_exp_f32 v148, v148\n\t" \
    "v_cvt_pk_bf16_f32 v191, v178, v179\n\t" \
    "v_exp_f32 v149, v149\n\t" \
    "v_exp_f32 v150, v150\n\t" \
    "v_mfma_f32_32x32x16_bf16 v[116:131], a[76:79], v[188:191], v[116:131]\n\t" \
    "s_add_u32 s68, s68, 14336\n\t" \
    "v_exp_f32 v151, v151\n\t" \
    "v_cvt_pk_bf16_f32 v184, v148, v149\n\t" \
    "v_exp_f32 v152, v152\n\t" \
    "v_exp_f32 v153, v153\n\t" \
    "v_cvt_pk_bf16_f32 v185, v150, v151\n\t" \
    "v_exp_f32 v154, v154\n\t" \
    "v_mfma_f32_32x32x16_bf16 v[164:179], a[12:15], v[180:183], v[100:115]\n\t" \
    "s_mov_b32 m0, s69\n\t" \
    "v_exp_f32 v155, v155\n\t" \
    "v_cvt_pk_bf16_f32 v186, v152, v153\n\t" \
    "v_exp_f32 v156, v156\n\t" \
    "v_cvt_pk_bf16_f32 v187, v154, v155\n\t" \
    "v_exp_f32 v157, v157\n\t" \
    "v_exp_f32 v158, v158\n\t" \
    "v_mfma_f32_32x32x16_bf16 v[116:131], a[80:83], v[184:187], v[116:131]\n\t" \
    "ds_read_b32 v80, v78\n\t" \
    "ds_read_b64 v[224:225], v244 offset:0\n\t" \
    "ds_read_b64 v[226:227], v244 offset:2048\n\t" \
    "ds_read_b64 v[228:229], v244 offset:4096\n\t" \
    "ds_read_b64 v[230:231], v244 offset:6144\n\t" \
    "ds_read_b128 v[232:235], v245\n\t" \
    "v_exp_f32 v159, v159\n\t" \
    "v_cvt_pk_bf16_f32 v188, v156, v157\n\t" \
    "v_exp_f32 v160, v160\n\t" \
    "v_exp_f32 v161, v161\n\t" \
    "v_cvt_pk_bf16_f32 v189, v158, v159\n\t" \
    "v_exp_f32 v162, v162\n\t" \
    "v_exp_f32 v163, v163\n\t" \
    "v_cvt_pk_bf16_f32 v190, v160, v161\n\t" \
    "v_exp_f32 v164, v164\n\t" \
    "v_cvt_pk_bf16_f32 v191, v162, v163\n\t" \
    "v_exp_f32 v165, v165\n\t" \
    "v_exp_f32 v166, v166\n\t" \
    "v_mfma_f32_32x32x16_bf16 v[116:131], a[84:87], v[188:191], v[116:131]\n\t" \
    "buffer_load_dwordx4 v250, s[44:47], s68 offen lds\n\t" \
    "v_exp_f32 v167, v167\n\t" \
    "v_cvt_pk_bf16_f32 v184, v164, v165\n\t" \
    "v_exp_f32 v168, v168\n\t" \
    "v_exp_f32 v169, v169\n\t" \
    "v_cvt_pk_bf16_f32 v185, v166, v167\n\t" \
    "v_exp_f32 v170, v170\n\t" \
    "v_mfma_f32_32x32x16_bf16 v[148:163], a[16:19], v[180:183], v[100:115]\n\t" \
    "s_add_u32 s69, s69, 0x400\n\t" \
    "v_exp_f32 v171, v171\n\t" \
    "v_cvt_pk_bf16_f32 v186, v168, v169\n\t" \
    "v_exp_f32 v172, v172\n\t" \
    "v_cvt_pk_bf16_f32 v187, v170, v171\n\t" \
    "v_exp_f32 v173, v173\n\t" \
    "v_exp_f32 v174, v174\n\t" \
    "v_mfma_f32_32x32x16_bf16 v[116:131], a[88:91], v[184:187], v[116:131]\n\t" \
    "s_waitcnt lgkmcnt(0)\n\t" \
    "v_exp_f32 v175, v175\n\t" \
    "v_cvt_pk_bf16_f32 v188, v172, v173\n\t" \
    "v_cmp_ne_u32 vcc, s71, v80\n\t" \
    "v_mul_f32 v232, v232, v236\n\t" \
    "v_mul_f32 v224, v224, v236\n\t" \
    "v_mul_f32 v225, v225, v236\n\t" \
    "v_fmac_f32 v232, v233, v237\n\t" \
    "v_fmac_f32 v224, v226, v237\n\t" \
    "v_fmac_f32 v225, v227, v237\n\t" \
    "v_exp_f32 v176, v176\n\t" \
    "v_exp_f32 v177, v177\n\t" \
    "v_cvt_pk_bf16_f32 v189, v174, v175\n\t" \
    "v_fmac_f32 v232, v234, v238\n\t" \
    "v_fmac_f32 v224, v228, v238\n\t" \
    "v_fmac_f32 v225, v229, v238\n\t" \
    "v_fmac_f32 v232, v235, v239\n\t" \
    "v_fmac_f32 v224, v230, v239\n\t" \
    "v_rcp_f32 v232, v232\n\t" \
    "v_fmac_f32 v225, v231, v239\n\t" \
    "v_exp_f32 v178, v178\n\t" \
    "v_exp_f32 v179, v179\n\t" \
    "v_cvt_pk_bf16_f32 v190, v176, v177\n\t" \
    "v_exp_f32 v148, v148\n\t" \
    "v_cvt_pk_bf16_f32 v191, v178, v179\n\t" \
    "v_exp_f32 v149, v149\n\t" \
    "v_exp_f32 v150, v150\n\t" \
    "v_mfma_f32_32x32x16_bf16 v[116:131], a[92:95], v[188:191], v[116:131]\n\t" \
    "s_cbranch_vccnz .Lr13_%=\n\t" \
    ".Lr1r3_%=:\n\t" \
    "v_exp_f32 v151, v151\n\t" \
    "v_cvt_pk_bf16_f32 v184, v148, v149\n\t" \
    "v_exp_f32 v152, v152\n\t" \
    "v_exp_f32 v153, v153\n\t" \
    "v_cvt_pk_bf16_f32 v185, v150, v151\n\t" \
    "v_exp_f32 v154, v154\n\t" \
    "v_mfma_f32_32x32x16_bf16 v[164:179], a[20:23], v[180:183], v[100:115]\n\t" \
    "s_add_u32 s68, s68, 14336\n\t" \
    "v_exp_f32 v155, v155\n\t" \
    "v_cvt_pk_bf16_f32 v186, v152, v153\n\t" \
    "v_exp_f32 v156, v156\n\t" \
    "v_cvt_pk_bf16_f32 v187, v154, v155\n\t" \
    "v_exp_f32 v157, v157\n\t" \
    "v_exp_f32 v158, v158\n\t" \
    "v_mfma_f32_32x32x16_bf16 v[116:131], a[96:99], v[184:187], v[116:131]\n\t" \
    "s_mov_b32 m0, s69\n\t" \
    "v_exp_f32 v159, v159\n\t" \
    "v_cvt_pk_bf16_f32 v188, v156, v157\n\t" \
    "v_mul_f32 v224, v224, v232\n\t" \
    "v_mul_f32 v225, v225, v232\n\t" \
    "v_cvt_pk_bf16_f32 v224, v224, v225\n\t" \
    "v_exp_f32 v160, v160\n\t" \
    "v_exp_f32 v161, v161\n\t" \
    "v_cvt_pk_bf16_f32 v189, v158, v159\n\t" \
    "v_exp_f32 v162, v162\n\t" \
    "v_exp_f32 v163, v163\n\t" \
    "v_cvt_pk_bf16_f32 v190, v160, v161\n\t" \
    "v_exp_f32 v164, v164\n\t" \
    "v_cvt_pk_bf16_f32 v191, v162, v163\n\t" \
    "v_exp_f32 v165, v165\n\t" \
    "v_exp_f32 v166, v166\n\t" \
    "v_mfma_f32_32x32x16_bf16 v[116:131], a[100:103], v[188:191], v[116:131]\n\t" \
    "ds_write_b32 v246, v224\n\t" \
    "ds_write_b32 v77, v79\n\t" \
    "v_exp_f32 v167, v167\n\t" \
    "v_cvt_pk_bf16_f32 v184, v164, v165\n\t" \
    "v_exp_f32 v168, v168\n\t" \
    "v_exp_f32 v169, v169\n\t" \
    "v_cvt_pk_bf16_f32 v185, v166, v167\n\t" \
    "v_exp_f32 v170, v170\n\t" \
    "v_mfma_f32_32x32x16_bf16 v[148:163], a[24:27], v[180:183], v[100:115]\n\t" \
    "buffer_load_dwordx4 v251, s[44:47], s68 offen lds\n\t" \
    "v_exp_f32 v171, v171\n\t" \
    "v_cvt_pk_bf16_f32 v186, v168, v169\n\t" \
    "v_exp_f32 v172, v172\n\t" \
    "v_cvt_pk_bf16_f32 v187, v170, v171\n\t" \
    "v_exp_f32 v173, v173\n\t" \
    "v_exp_f32 v174, v174\n\t" \
    "v_mfma_f32_32x32x16_bf16 v[116:131], a[104:107], v[184:187], v[116:131]\n\t" \
    "s_add_u32 s69, s69, 0x400\n\t" \
    "s_waitcnt vmcnt(7)\n\t" \
    "v_exp_f32 v175, v175\n\t" \
    "v_cvt_pk_bf16_f32 v188, v172, v173\n\t" \
    "v_exp_f32 v176, v176\n\t" \
    "v_exp_f32 v177, v177\n\t" \
    "v_cvt_pk_bf16_f32 v189, v174, v175\n\t" \
    "v_cmp_ne_u64 vcc, s[54:55], v[84:85]\n\t" \
    "v_exp_f32 v178, v178\n\t" \
    "v_exp_f32 v179, v179\n\t" \
    "v_cvt_pk_bf16_f32 v190, v176, v177\n\t" \
    "v_exp_f32 v148, v148\n\t" \
    "v_cvt_pk_bf16_f32 v191, v178, v179\n\t" \
    "v_exp_f32 v149, v149\n\t" \
    "v_exp_f32 v150, v150\n\t" \
    "v_mfma_f32_32x32x16_bf16 v[116:131], a[108:111], v[188:191], v[116:131]\n\t" \
    "s_cbranch_vccnz .Lw_%=\n\t" \
    ".Lwr_%=:\n\t" \
    "s_add_u32 s68, s68, 14336\n\t" \
    "buffer_load_dwordx4 v[36:39], v27, s[8:11], s88 offen offset:128 sc1\n\t" \
    "v_exp_f32 v151, v151\n\t" \
    "v_cvt_pk_bf16_f32 v184, v148, v149\n\t" \
    "v_exp_f32 v152, v152\n\t" \
    "v_exp_f32 v153, v153\n\t" \
    "v_cvt_pk_bf16_f32 v185, v150, v151\n\t" \
    "v_exp_f32 v154, v154\n\t" \
    "v_mfma_f32_32x32x16_bf16 v[164:179], a[28:31], v[180:183], v[100:115]\n\t" \
    "s_mov_b32 m0, s69\n\t" \
    "buffer_load_dwordx4 v[28:31], v27, s[8:11], s88 offen offset:0 sc1\n\t" \
    "v_exp_f32 v155, v155\n\t" \
    "v_cvt_pk_bf16_f32 v186, v152, v153\n\t" \
    "v_exp_f32 v156, v156\n\t" \
    "v_cvt_pk_bf16_f32 v187, v154, v155\n\t" \
    "v_exp_f32 v157, v157\n\t" \
    "v_exp_f32 v158, v158\n\t" \
    "v_mfma_f32_32x32x16_bf16 v[116:131], a[112:115], v[184:187], v[116:131]\n\t" \
    "buffer_load_dwordx4 v251, s[44:47], s68 offen lds\n\t" \
    "buffer_load_dwordx4 v[32:35], v27, s[8:11], s88 offen offset:16 sc1\n\t" \
    "v_exp_f32 v159, v159\n\t" \
    "v_cvt_pk_bf16_f32 v188, v156, v157\n\t" \
    "v_exp_f32 v160, v160\n\t" \
    "v_exp_f32 v161, v161\n\t" \
    "v_cvt_pk_bf16_f32 v189, v158, v159\n\t" \
    "v_exp_f32 v162, v162\n\t" \
    "v_exp_f32 v163, v163\n\t" \
    "v_cvt_pk_bf16_f32 v190, v160, v161\n\t" \
    "v_exp_f32 v164, v164\n\t" \
    "v_cvt_pk_bf16_f32 v191, v162, v163\n\t" \
    "v_exp_f32 v165, v165\n\t" \
    "v_exp_f32 v166, v166\n\t" \
    "v_mfma_f32_32x32x16_bf16 v[116:131], a[116:119], v[188:191], v[116:131]\n\t" \
    "ds_read_b32 v80, v78 offset:16\n\t" \
    "ds_read_b128 v[208:211], v247 offset:0\n\t" \
    "ds_read_b128 v[212:215], v247 offset:64\n\t" \
    "ds_read_b128 v[216:219], v248 offset:0\n\t" \
    "ds_read_b128 v[220:223], v248 offset:64\n\t" \
    "buffer_load_dwordx4 v[48:51], v27, s[8:11], s89 offen offset:16 sc1\n\t" \
    "v_exp_f32 v167, v167\n\t" \
    "v_cvt_pk_bf16_f32 v184, v164, v165\n\t" \
    "v_exp_f32 v168, v168\n\t" \
    "v_exp_f32 v169, v169\n\t" \
    "v_cvt_pk_bf16_f32 v185, v166, v167\n\t" \
    "v_exp_f32 v170, v170\n\t" \
    "v_mfma_f32_32x32x16_bf16 v[148:163], a[32:35], v[180:183], v[100:115]\n\t" \
    "s_add_u32 s69, s69, 0x400\n\t" \
    "s_mov_b32 s72, s67\n\t" \
    "buffer_load_dwordx4 v[40:43], v27, s[8:11], s88 offen offset:144 sc1\n\t" \
    "v_exp_f32 v171, v171\n\t" \
    "v_cvt_pk_bf16_f32 v186, v168, v169\n\t" \
    "v_exp_f32 v172, v172\n\t" \
    "v_cvt_pk_bf16_f32 v187, v170, v171\n\t" \
    "v_exp_f32 v173, v173\n\t" \
    "v_exp_f32 v174, v174\n\t" \
    "v_mfma_f32_32x32x16_bf16 v[116:131], a[120:123], v[184:187], v[116:131]\n\t" \
    "s_waitcnt vmcnt(14) lgkmcnt(0)\n\t" \
    "buffer_load_dwordx4 v[44:47], v27, s[8:11], s89 offen offset:0 sc1\n\t" \
    "v_exp_f32 v175, v175\n\t" \
    "v_cvt_pk_bf16_f32 v188, v172, v173\n\t" \
    "v_exp_f32 v176, v176\n\t" \
    "v_exp_f32 v177, v177\n\t" \
    "v_cvt_pk_bf16_f32 v189, v174, v175\n\t" \
    "v_cmp_ne_u32 vcc, s71, v80\n\t" \
    "v_exp_f32 v178, v178\n\t" \
    "v_exp_f32 v179, v179\n\t" \
    "v_cvt_pk_bf16_f32 v190, v176, v177\n\t" \
    "v_exp_f32 v148, v148\n\t" \
    "v_cvt_pk_bf16_f32 v191, v178, v179\n\t" \
    "v_exp_f32 v149, v149\n\t" \
    "v_exp_f32 v150, v150\n\t" \
    "v_mfma_f32_32x32x16_bf16 v[116:131], a[124:127], v[188:191], v[116:131]\n\t" \
    "s_cbranch_vccnz .Lr23_%=\n\t" \
    ".Lr2r3_%=:\n\t" \
    "v_exp_f32 v151, v151\n\t" \
    "v_cvt_pk_bf16_f32 v184, v148, v149\n\t" \
    "v_exp_f32 v152, v152\n\t" \
    "v_exp_f32 v153, v153\n\t" \
    "v_cvt_pk_bf16_f32 v185, v150, v151\n\t" \
    "v_exp_f32 v154, v154\n\t" \
    "v_mfma_f32_32x32x16_bf16 v[164:179], a[36:39], v[180:183], v[100:115]\n\t" \
    "s_add_u32 s73, s67, 0x1000\n\t" \
    "buffer_load_dwordx4 v[52:55], v27, s[8:11], s89 offen offset:128 sc1\n\t" \
    "v_exp_f32 v155, v155\n\t" \
    "v_cvt_pk_bf16_f32 v186, v152, v153\n\t" \
    "v_exp_f32 v156, v156\n\t" \
    "v_cvt_pk_bf16_f32 v187, v154, v155\n\t" \
    "v_exp_f32 v157, v157\n\t" \
    "v_exp_f32 v158, v158\n\t" \
    "v_mfma_f32_32x32x16_bf16 v[116:131], a[128:131], v[184:187], v[116:131]\n\t" \
    "s_add_u32 s74, s67, 0x2000\n\t" \
    "buffer_load_dwordx4 v[56:59], v27, s[8:11], s89 offen offset:144 sc1\n\t" \
    "v_exp_f32 v159, v159\n\t" \
    "v_cvt_pk_bf16_f32 v188, v156, v157\n\t" \
    "v_exp_f32 v160, v160\n\t" \
    "v_exp_f32 v161, v161\n\t" \
    "v_mfma_f32_16x16x32_bf16 v[192:195], a[192:195], v[208:211], v[192:195]\n\t" \
    "buffer_load_dwordx4 a[192:195], v2, s[8:11], s72 offen offset:0 sc1\n\t" \
    "v_cvt_pk_bf16_f32 v189, v158, v159\n\t" \
    "v_exp_f32 v162, v162\n\t" \
    "v_exp_f32 v163, v163\n\t" \
    "v_mfma_f32_16x16x32_bf16 v[196:199], a[196:199], v[208:211], v[196:199]\n\t" \
    "buffer_load_dwordx4 a[196:199], v2, s[8:11], s72 offen offset:1024 sc1\n\t" \
    "v_cvt_pk_bf16_f32 v190, v160, v161\n\t" \
    "v_exp_f32 v164, v164\n\t" \
    "v_cvt_pk_bf16_f32 v191, v162, v163\n\t" \
    "v_exp_f32 v165, v165\n\t" \
    "v_exp_f32 v166, v166\n\t" \
    "v_mfma_f32_32x32x16_bf16 v[116:131], a[132:135], v[188:191], v[116:131]\n\t" \
    "s_add_u32 s75, s67, 0x3000\n\t" \
    "v_exp_f32 v167, v167\n\t" \
    "v_cvt_pk_bf16_f32 v184, v164, v165\n\t" \
    "v_exp_f32 v168, v168\n\t" \
    "v_exp_f32 v169, v169\n\t" \
    "v_cvt_pk_bf16_f32 v185, v166, v167\n\t" \
    "v_exp_f32 v170, v170\n\t" \
    "v_mfma_f32_32x32x16_bf16 v[148:163], a[40:43], v[180:183], v[100:115]\n\t" \
    "s_add_u32 s67, s67, 0x10000\n\t" \
    "buffer_load_dwordx4 v[60:63], v27, s[8:11], s90 offen offset:0 sc1\n\t" \
    "v_exp_f32 v171, v171\n\t" \
    "v_cvt_pk_bf16_f32 v186, v168, v169\n\t" \
    "v_exp_f32 v172, v172\n\t" \
    "v_cvt_pk_bf16_f32 v187, v170, v171\n\t" \
    "v_exp_f32 v173, v173\n\t" \
    "v_exp_f32 v174, v174\n\t" \
    "v_mfma_f32_32x32x16_bf16 v[116:131], a[136:139], v[184:187], v[116:131]\n\t" \
    "s_add_u32 s68, s68, 14336\n\t" \
    "buffer_load_dwordx4 v[64:67], v27, s[8:11], s90 offen offset:16 sc1\n\t" \
    "v_exp_f32 v175, v175\n\t" \
    "v_cvt_pk_bf16_f32 v188, v172, v173\n\t" \
    "v_exp_f32 v176, v176\n\t" \
    "v_exp_f32 v177, v177\n\t" \
    "v_mfma_f32_16x16x32_bf16 v[200:203], a[200:203], v[208:211], v[200:203]\n\t" \
    "buffer_load_dwordx4 a[200:203], v2, s[8:11], s72 offen offset:2048 sc1\n\t" \
    "v_cvt_pk_bf16_f32 v189, v174, v175\n\t" \
    "v_exp_f32 v178, v178\n\t" \
    "v_exp_f32 v179, v179\n\t" \
    "v_mfma_f32_16x16x32_bf16 v[204:207], a[204:207], v[208:211], v[204:207]\n\t" \
    "buffer_load_dwordx4 a[204:207], v2, s[8:11], s72 offen offset:3072 sc1\n\t" \
    "v_cvt_pk_bf16_f32 v190, v176, v177\n\t" \
    "v_exp_f32 v148, v148\n\t" \
    "v_cvt_pk_bf16_f32 v191, v178, v179\n\t" \
    "v_exp_f32 v149, v149\n\t" \
    "v_exp_f32 v150, v150\n\t" \
    "v_mfma_f32_32x32x16_bf16 v[116:131], a[140:143], v[188:191], v[116:131]\n\t" \
    "v_exp_f32 v151, v151\n\t" \
    "v_cvt_pk_bf16_f32 v184, v148, v149\n\t" \
    "v_exp_f32 v152, v152\n\t" \
    "v_exp_f32 v153, v153\n\t" \
    "v_cvt_pk_bf16_f32 v185, v150, v151\n\t" \
    "v_exp_f32 v154, v154\n\t" \
    "v_mfma_f32_32x32x16_bf16 v[164:179], a[44:47], v[180:183], v[100:115]\n\t" \
    "buffer_load_dwordx4 v[68:71], v27, s[8:11], s90 offen offset:128 sc1\n\t" \
    "v_exp_f32 v155, v155\n\t" \
    "v_cvt_pk_bf16_f32 v186, v152, v153\n\t" \
    "v_exp_f32 v156, v156\n\t" \
    "v_cvt_pk_bf16_f32 v187, v154, v155\n\t" \
    "v_exp_f32 v157, v157\n\t" \
    "v_exp_f32 v158, v158\n\t" \
    "v_mfma_f32_32x32x16_bf16 v[116:131], a[144:147], v[184:187], v[116:131]\n\t" \
    "buffer_load_dwordx4 v[72:75], v27, s[8:11], s90 offen offset:144 sc1\n\t" \
    "v_exp_f32 v159, v159\n\t" \
    "v_cvt_pk_bf16_f32 v188, v156, v157\n\t" \
    "v_exp_f32 v160, v160\n\t" \
    "v_exp_f32 v161, v161\n\t" \
    "v_mfma_f32_16x16x32_bf16 v[192:195], a[208:211], v[216:219], v[192:195]\n\t" \
    "buffer_load_dwordx4 a[208:211], v2, s[8:11], s73 offen offset:0 sc1\n\t" \
    "v_cvt_pk_bf16_f32 v189, v158, v159\n\t" \
    "v_exp_f32 v162, v162\n\t" \
    "v_exp_f32 v163, v163\n\t" \
    "v_mfma_f32_16x16x32_bf16 v[196:199], a[212:215], v[216:219], v[196:199]\n\t" \
    "buffer_load_dwordx4 a[212:215], v2, s[8:11], s73 offen offset:1024 sc1\n\t" \
    "v_cvt_pk_bf16_f32 v190, v160, v161\n\t" \
    "v_exp_f32 v164, v164\n\t" \
    "v_cvt_pk_bf16_f32 v191, v162, v163\n\t" \
    "v_exp_f32 v165, v165\n\t" \
    "v_exp_f32 v166, v166\n\t" \
    "v_mfma_f32_32x32x16_bf16 v[116:131], a[148:151], v[188:191], v[116:131]\n\t" \
    "v_exp_f32 v167, v167\n\t" \
    "v_cvt_pk_bf16_f32 v184, v164, v165\n\t" \
    "v_exp_f32 v168, v168\n\t" \
    "v_exp_f32 v169, v169\n\t" \
    "v_cvt_pk_bf16_f32 v185, v166, v167\n\t" \
    "v_exp_f32 v170, v170\n\t" \
    "v_mfma_f32_32x32x16_bf16 v[148:163], a[48:51], v[180:183], v[100:115]\n\t" \
    "buffer_load_dwordx4 a[0:3], v2, s[8:11], s16 offen offset:0 sc1\n\t" \
    "v_exp_f32 v171, v171\n\t" \
    "v_cvt_pk_bf16_f32 v186, v168, v169\n\t" \
    "v_exp_f32 v172, v172\n\t" \
    "v_cvt_pk_bf16_f32 v187, v170, v171\n\t" \
    "v_exp_f32 v173, v173\n\t" \
    "v_exp_f32 v174, v174\n\t" \
    "v_mfma_f32_32x32x16_bf16 v[116:131], a[152:155], v[184:187], v[116:131]\n\t" \
    "buffer_load_dwordx4 a[4:7], v2, s[8:11], s16 offen offset:1024 sc1\n\t" \
    "v_exp_f32 v175, v175\n\t" \
    "v_cvt_pk_bf16_f32 v188, v172, v173\n\t" \
    "v_exp_f32 v176, v176\n\t" \
    "v_exp_f32 v177, v177\n\t" \
    "v_mfma_f32_16x16x32_bf16 v[200:203], a[216:219], v[216:219], v[200:203]\n\t" \
    "buffer_load_dwordx4 a[216:219], v2, s[8:11], s73 offen offset:2048 sc1\n\t" \
    "v_cvt_pk_bf16_f32 v189, v174, v175\n\t" \
    "v_exp_f32 v178, v178\n\t" \
    "v_exp_f32 v179, v179\n\t" \
    "v_mfma_f32_16x16x32_bf16 v[204:207], a[220:223], v[216:219], v[204:207]\n\t" \
    "buffer_load_dwordx4 a[220:223], v2, s[8:11], s73 offen offset:3072 sc1\n\t" \
    "v_cvt_pk_bf16_f32 v190, v176, v177\n\t" \
    "v_exp_f32 v148, v148\n\t" \
    "v_cvt_pk_bf16_f32 v191, v178, v179\n\t" \
    "v_exp_f32 v149, v149\n\t" \
    "v_exp_f32 v150, v150\n\t" \
    "v_mfma_f32_32x32x16_bf16 v[116:131], a[156:159], v[188:191], v[116:131]\n\t" \
    "v_exp_f32 v151, v151\n\t" \
    "v_cvt_pk_bf16_f32 v184, v148, v149\n\t" \
    "v_exp_f32 v152, v152\n\t" \
    "v_exp_f32 v153, v153\n\t" \
    "v_cvt_pk_bf16_f32 v185, v150, v151\n\t" \
    "v_exp_f32 v154, v154\n\t" \
    "v_mfma_f32_32x32x16_bf16 v[164:179], a[52:55], v[180:183], v[100:115]\n\t" \
    "buffer_load_dwordx4 a[8:11], v2, s[8:11], s16 offen offset:2048 sc1\n\t" \
    "v_exp_f32 v155, v155\n\t" \
    "v_cvt_pk_bf16_f32 v186, v152, v153\n\t" \
    "v_exp_f32 v156, v156\n\t" \
    "v_cvt_pk_bf16_f32 v187, v154, v155\n\t" \
    "v_exp_f32 v157, v157\n\t" \
    "v_exp_f32 v158, v158\n\t" \
    "v_mfma_f32_32x32x16_bf16 v[116:131], a[160:163], v[184:187], v[116:131]\n\t" \
    "buffer_load_dwordx4 a[12:15], v2, s[8:11], s16 offen offset:3072 sc1\n\t" \
    "v_exp_f32 v159, v159\n\t" \
    "v_cvt_pk_bf16_f32 v188, v156, v157\n\t" \
    "v_exp_f32 v160, v160\n\t" \
    "v_exp_f32 v161, v161\n\t" \
    "v_mfma_f32_16x16x32_bf16 v[192:195], a[224:227], v[212:215], v[192:195]\n\t" \
    "buffer_load_dwordx4 a[224:227], v2, s[8:11], s74 offen offset:0 sc1\n\t" \
    "v_cvt_pk_bf16_f32 v189, v158, v159\n\t" \
    "v_exp_f32 v162, v162\n\t" \
    "v_exp_f32 v163, v163\n\t" \
    "v_mfma_f32_16x16x32_bf16 v[196:199], a[228:231], v[212:215], v[196:199]\n\t" \
    "buffer_load_dwordx4 a[228:231], v2, s[8:11], s74 offen offset:1024 sc1\n\t" \
    "v_cvt_pk_bf16_f32 v190, v160, v161\n\t" \
    "v_exp_f32 v164, v164\n\t" \
    "v_cvt_pk_bf16_f32 v191, v162, v163\n\t" \
    "v_exp_f32 v165, v165\n\t" \
    "v_exp_f32 v166, v166\n\t" \
    "v_mfma_f32_32x32x16_bf16 v[116:131], a[164:167], v[188:191], v[116:131]\n\t" \
    "v_exp_f32 v167, v167\n\t" \
    "v_cvt_pk_bf16_f32 v184, v164, v165\n\t" \
    "v_exp_f32 v168, v168\n\t" \
    "v_exp_f32 v169, v169\n\t" \
    "v_cvt_pk_bf16_f32 v185, v166, v167\n\t" \
    "v_exp_f32 v170, v170\n\t" \
    "v_mfma_f32_32x32x16_bf16 v[148:163], a[56:59], v[180:183], v[100:115]\n\t" \
    "buffer_load_dwordx4 a[16:19], v2, s[8:11], s17 offen offset:0 sc1\n\t" \
    "v_exp_f32 v171, v171\n\t" \
    "v_cvt_pk_bf16_f32 v186, v168, v169\n\t" \
    "v_exp_f32 v172, v172\n\t" \
    "v_cvt_pk_bf16_f32 v187, v170, v171\n\t" \
    "v_exp_f32 v173, v173\n\t" \
    "v_exp_f32 v174, v174\n\t" \
    "v_mfma_f32_32x32x16_bf16 v[116:131], a[168:171], v[184:187], v[116:131]\n\t" \
    "buffer_load_dwordx4 a[20:23], v2, s[8:11], s17 offen offset:1024 sc1\n\t" \
    "v_exp_f32 v175, v175\n\t" \
    "v_cvt_pk_bf16_f32 v188, v172, v173\n\t" \
    "v_exp_f32 v176, v176\n\t" \
    "v_exp_f32 v177, v177\n\t" \
    "v_mfma_f32_16x16x32_bf16 v[200:203], a[232:235], v[212:215], v[200:203]\n\t" \
    "buffer_load_dwordx4 a[232:235], v2, s[8:11], s74 offen offset:2048 sc1\n\t" \
    "v_cvt_pk_bf16_f32 v189, v174, v175\n\t" \
    "v_exp_f32 v178, v178\n\t" \
    "v_exp_f32 v179, v179\n\t" \
    "v_mfma_f32_16x16x32_bf16 v[204:207], a[236:239], v[212:215], v[204:207]\n\t" \
    "buffer_load_dwordx4 a[236:239], v2, s[8:11], s74 offen offset:3072 sc1\n\t" \
    "v_cvt_pk_bf16_f32 v190, v176, v177\n\t" \
    "v_exp_f32 v148, v148\n\t" \
    "v_cvt_pk_bf16_f32 v191, v178, v179\n\t" \
    "v_exp_f32 v149, v149\n\t" \
    "v_exp_f32 v150, v150\n\t" \
    "v_mfma_f32_32x32x16_bf16 v[116:131], a[172:175], v[188:191], v[116:131]\n\t" \
    "v_exp_f32 v151, v151\n\t" \
    "v_cvt_pk_bf16_f32 v184, v148, v149\n\t" \
    "v_exp_f32 v152, v152\n\t" \
    "v_exp_f32 v153, v153\n\t" \
    "v_cvt_pk_bf16_f32 v185, v150, v151\n\t" \
    "v_exp_f32 v154, v154\n\t" \
    "v_mfma_f32_32x32x16_bf16 v[164:179], a[60:63], v[180:183], v[100:115]\n\t" \
    "buffer_load_dwordx4 a[24:27], v2, s[8:11], s17 offen offset:2048 sc1\n\t" \
    "v_exp_f32 v155, v155\n\t" \
    "v_cvt_pk_bf16_f32 v186, v152, v153\n\t" \
    "v_exp_f32 v156, v156\n\t" \
    "v_cvt_pk_bf16_f32 v187, v154, v155\n\t" \
    "v_exp_f32 v157, v157\n\t" \
    "v_exp_f32 v158, v158\n\t" \
    "v_mfma_f32_32x32x16_bf16 v[116:131], a[176:179], v[184:187], v[116:131]\n\t" \
    "buffer_load_dwordx4 a[28:31], v2, s[8:11], s17 offen offset:3072 sc1\n\t" \
    "v_exp_f32 v159, v159\n\t" \
    "v_cvt_pk_bf16_f32 v188, v156, v157\n\t" \
    "v_exp_f32 v160, v160\n\t" \
    "v_exp_f32 v161, v161\n\t" \
    "v_mfma_f32_16x16x32_bf16 v[192:195], a[240:243], v[220:223], v[192:195]\n\t" \
    "buffer_load_dwordx4 a[240:243], v2, s[8:11], s75 offen offset:0 sc1\n\t" \
    "v_cvt_pk_bf16_f32 v189, v158, v159\n\t" \
    "v_exp_f32 v162, v162\n\t" \
    "v_exp_f32 v163, v163\n\t" \
    "v_mfma_f32_16x16x32_bf16 v[196:199], a[244:247], v[220:223], v[196:199]\n\t" \
    "buffer_load_dwordx4 a[244:247], v2, s[8:11], s75 offen offset:1024 sc1\n\t" \
    "v_cvt_pk_bf16_f32 v190, v160, v161\n\t" \
    "v_exp_f32 v164, v164\n\t" \
    "v_cvt_pk_bf16_f32 v191, v162, v163\n\t" \
    "v_exp_f32 v165, v165\n\t" \
    "v_exp_f32 v166, v166\n\t" \
    "v_mfma_f32_32x32x16_bf16 v[116:131], a[180:183], v[188:191], v[116:131]\n\t" \
    "v_exp_f32 v167, v167\n\t" \
    "v_cvt_pk_bf16_f32 v184, v164, v165\n\t" \
    "v_exp_f32 v168, v168\n\t" \
    "v_exp_f32 v169, v169\n\t" \
    "v_cvt_pk_bf16_f32 v185, v166, v167\n\t" \
    "v_exp_f32 v170, v170\n\t" \
    "buffer_load_dwordx4 a[32:35], v2, s[8:11], s18 offen offset:0 sc1\n\t" \
    "v_exp_f32 v171, v171\n\t" \
    "v_cvt_pk_bf16_f32 v186, v168, v169\n\t" \
    "v_exp_f32 v172, v172\n\t" \
    "v_cvt_pk_bf16_f32 v187, v170, v171\n\t" \
    "v_exp_f32 v173, v173\n\t" \
    "v_exp_f32 v174, v174\n\t" \
    "v_mfma_f32_32x32x16_bf16 v[116:131], a[184:187], v[184:187], v[116:131]\n\t" \
    "buffer_load_dwordx4 a[36:39], v2, s[8:11], s18 offen offset:1024 sc1\n\t" \
    "v_exp_f32 v175, v175\n\t" \
    "v_cvt_pk_bf16_f32 v188, v172, v173\n\t" \
    "v_exp_f32 v176, v176\n\t" \
    "v_exp_f32 v177, v177\n\t" \
    "v_mfma_f32_16x16x32_bf16 v[200:203], a[248:251], v[220:223], v[200:203]\n\t" \
    "buffer_load_dwordx4 a[248:251], v2, s[8:11], s75 offen offset:2048 sc1\n\t" \
    "v_cvt_pk_bf16_f32 v189, v174, v175\n\t" \
    "v_exp_f32 v178, v178\n\t" \
    "v_exp_f32 v179, v179\n\t" \
    "v_mfma_f32_16x16x32_bf16 v[204:207], a[252:255], v[220:223], v[204:207]\n\t" \
    "buffer_load_dwordx4 a[252:255], v2, s[8:11], s75 offen offset:3072 sc1\n\t" \
    "v_cvt_pk_bf16_f32 v190, v176, v177\n\t" \
    "v_cvt_pk_bf16_f32 v191, v178, v179\n\t" \
    "s_nop 1\n\t" \
    "v_mfma_f32_32x32x16_bf16 v[116:131], a[188:191], v[188:191], v[116:131]\n\t" \
    "buffer_load_dwordx4 a[40:43], v2, s[8:11], s18 offen offset:2048 sc1\n\t" \
    "buffer_load_dwordx4 a[44:47], v2, s[8:11], s18 offen offset:3072 sc1\n\t" \
    "v_mov_b32 v79, 8\n\t" \
    "s_nop 8\n\t" \
    "ds_write_b128 v242, v[116:119]\n\t" \
    "ds_write_b128 v242, v[120:123] offset:1024\n\t" \
    "ds_write_b32 v243, v124\n\t" \
    "ds_write_b32 v76, v79\n\t" \
    "v_lshlrev_b32 v100, 16, v16\n\t" \
    "v_and_b32 v101, 0xffff0000, v16\n\t" \
    "v_lshlrev_b32 v102, 16, v17\n\t" \
    "v_and_b32 v103, 0xffff0000, v17\n\t" \
    "v_lshlrev_b32 v104, 16, v18\n\t" \
    "buffer_load_dwordx4 a[48:51], v2, s[8:11], s19 offen offset:0 sc1\n\t" \
    "v_and_b32 v105, 0xffff0000, v18\n\t" \
    "v_lshlrev_b32 v106, 16, v19\n\t" \
    "v_and_b32 v107, 0xffff0000, v19\n\t" \
    "v_lshlrev_b32 v108, 16, v20\n\t" \
    "v_and_b32 v109, 0xffff0000, v20\n\t" \
    "buffer_load_dwordx4 a[52:55], v2, s[8:11], s19 offen offset:1024 sc1\n\t" \
    "v_lshlrev_b32 v110, 16, v21\n\t" \
    "v_and_b32 v111, 0xffff0000, v21\n\t" \
    "v_lshlrev_b32 v112, 16, v22\n\t" \
    "v_and_b32 v113, 0xffff0000, v22\n\t" \
    "v_lshlrev_b32 v114, 16, v23\n\t" \
    "buffer_load_dwordx4 a[56:59], v2, s[8:11], s19 offen offset:2048 sc1\n\t" \
    "v_and_b32 v115, 0xffff0000, v23\n\t" \
    ".Lt1_%=:\n\t" \
    "ds_read_b32 v80, v78\n\t" \
    "ds_read_b64 v[224:225], v244 offset:0\n\t" \
    "ds_read_b64 v[226:227], v244 offset:2048\n\t" \
    "ds_read_b64 v[228:229], v244 offset:4096\n\t" \
    "ds_read_b64 v[230:231], v244 offset:6144\n\t" \
    "ds_read_b128 v[232:235], v245\n\t" \
    "s_waitcnt lgkmcnt(0)\n\t" \
    "v_cmp_ne_u32 vcc, 8, v80\n\t" \
    "v_mul_f32 v232, v232, v236\n\t" \
    "v_mul_f32 v224, v224, v236\n\t" \
    "v_mul_f32 v225, v225, v236\n\t" \
    "v_fmac_f32 v232, v233, v237\n\t" \
    "v_fmac_f32 v224, v226, v237\n\t" \
    "v_fmac_f32 v225, v227, v237\n\t" \
    "s_cbranch_vccz .Lt1_%=_ok\n\t" \
    "s_sub_u32 s62, s62, 1\n\t" \
    "s_cmp_lt_i32 s62, 0\n\t" \
    "s_cbranch_scc0 .Lt1_%=\n\t" \
    ".Lt1_%=_ok:\n\t" \
    "v_fmac_f32 v232, v234, v238\n\t" \
    "v_fmac_f32 v224, v228, v238\n\t" \
    "v_fmac_f32 v225, v229, v238\n\t" \
    "v_fmac_f32 v232, v235, v239\n\t" \
    "v_fmac_f32 v224, v230, v239\n\t" \
    "v_rcp_f32 v232, v232\n\t" \
    "v_fmac_f32 v225, v231, v239\n\t" \
    "v_mul_f32 v224, v224, v232\n\t" \
    "v_mul_f32 v225, v225, v232\n\t" \
    "v_cvt_pk_bf16_f32 v224, v224, v225\n\t" \
    "ds_write_b32 v246, v224\n\t" \
    "ds_write_b32 v77, v79\n\t" \
    "v_and_b32 v120, 7, v1\n\t" \
    "v_lshlrev_b32 v120, 4, v120\n\t" \
    "v_lshrrev_b32 v121, 4, v1\n\t" \
    "v_lshl_add_u32 v120, v121, 8, v120\n\t" \
    "v_bfe_u32 v121, v1, 3, 1\n\t" \
    "v_lshl_add_u32 v122, v121, 12, v120\n\t" \
    "v_lshl_add_u32 v120, v121, 11, v120\n\t" \
    "buffer_load_dwordx4 a[60:63], v2, s[8:11], s19 offen offset:3072 sc1\n\t" \
    "s_lshr_b32 s88, s53, 1\n\t" \
    "s_lshl_b32 s88, s88, 14\n\t" \
    "s_lshl_b32 s89, s50, 12\n\t" \
    "s_add_u32 s88, s88, s89\n\t" \
    "s_and_b32 s90, s53, 1\n\t" \
    "s_lshl_b32 s90, s90, 7\n\t" \
    "s_add_u32 s88, s88, s90\n\t" \
    "buffer_load_dwordx4 a[64:67], v2, s[8:11], s20 offen offset:0 sc1\n\t" \
    "s_add_u32 s88, s88, 4849664\n\t" \
    "s_lshr_b32 s91, s53, 2\n\t" \
    "s_lshl_b32 s91, s91, 15\n\t" \
    "s_lshl_b32 s89, s50, 13\n\t" \
    "s_add_u32 s91, s91, s89\n\t" \
    "s_bfe_u32 s89, s53, 0x10001\n\t" \
    "s_lshl_b32 s89, s89, 10\n\t" \
    "buffer_load_dwordx4 a[68:71], v2, s[8:11], s20 offen offset:1024 sc1\n\t" \
    "s_add_u32 s91, s91, s89\n\t" \
    "s_add_u32 s91, s91, s90\n\t" \
    "s_add_u32 s91, s91, 6946816\n\t" \
    "v_and_b32 v146, 7, v1\n\t" \
    "v_lshlrev_b32 v146, 5, v146\n\t" \
    "v_add_u32 v146, 158976, v146\n\t" \
    "v_lshrrev_b32 v147, 5, v1\n\t" \
    "buffer_load_dwordx4 a[72:75], v2, s[8:11], s20 offen offset:2048 sc1\n\t" \
    "v_lshl_add_u32 v147, v147, 4, v146\n\t" \
    "s_lshl_b32 s89, s50, 2\n\t" \
    "v_add_u32 v147, s89, v147\n\t" \
    ".Lt2_%=:\n\t" \
    "ds_read_b32 v80, v78 offset:16\n\t" \
    "ds_read_b128 v[208:211], v247 offset:0\n\t" \
    "ds_read_b128 v[212:215], v247 offset:64\n\t" \
    "ds_read_b128 v[216:219], v248 offset:0\n\t" \
    "ds_read_b128 v[220:223], v248 offset:64\n\t" \
    "s_waitcnt vmcnt(9) lgkmcnt(0)\n\t" \
    "v_cmp_ne_u32 vcc, 8, v80\n\t" \
    "s_nop 0\n\t" \
    "s_cbranch_vccz .Lt2_%=_ok\n\t" \
    "s_sub_u32 s62, s62, 1\n\t" \
    "s_cmp_lt_i32 s62, 0\n\t" \
    "s_cbranch_scc0 .Lt2_%=\n\t" \
    ".Lt2_%=_ok:\n\t" \
    "v_mfma_f32_16x16x32_bf16 v[192:195], a[192:195], v[208:211], v[192:195]\n\t" \
    "buffer_load_dwordx4 a[76:79], v2, s[8:11], s20 offen offset:3072 sc1\n\t" \
    "v_mfma_f32_16x16x32_bf16 v[192:195], a[208:211], v[216:219], v[192:195]\n\t" \
    "v_mfma_f32_16x16x32_bf16 v[192:195], a[224:227], v[212:215], v[192:195]\n\t" \
    "v_mfma_f32_16x16x32_bf16 v[192:195], a[240:243], v[220:223], v[192:195]\n\t" \
    "v_mfma_f32_16x16x32_bf16 v[196:199], a[196:199], v[208:211], v[196:199]\n\t" \
    "buffer_load_dwordx4 a[80:83], v2, s[8:11], s21 offen offset:0 sc1\n\t" \
    "v_mfma_f32_16x16x32_bf16 v[196:199], a[212:215], v[216:219], v[196:199]\n\t" \
    "v_mfma_f32_16x16x32_bf16 v[196:199], a[228:231], v[212:215], v[196:199]\n\t" \
    "v_mfma_f32_16x16x32_bf16 v[196:199], a[244:247], v[220:223], v[196:199]\n\t" \
    "v_mfma_f32_16x16x32_bf16 v[200:203], a[200:203], v[208:211], v[200:203]\n\t" \
    "buffer_load_dwordx4 a[84:87], v2, s[8:11], s21 offen offset:1024 sc1\n\t" \
    "v_mfma_f32_16x16x32_bf16 v[200:203], a[216:219], v[216:219], v[200:203]\n\t" \
    "v_mfma_f32_16x16x32_bf16 v[200:203], a[232:235], v[212:215], v[200:203]\n\t" \
    "v_mfma_f32_16x16x32_bf16 v[200:203], a[248:251], v[220:223], v[200:203]\n\t" \
    "v_mfma_f32_16x16x32_bf16 v[204:207], a[204:207], v[208:211], v[204:207]\n\t" \
    "buffer_load_dwordx4 a[88:91], v2, s[8:11], s21 offen offset:2048 sc1\n\t" \
    "v_mfma_f32_16x16x32_bf16 v[204:207], a[220:223], v[216:219], v[204:207]\n\t" \
    "v_mfma_f32_16x16x32_bf16 v[204:207], a[236:239], v[212:215], v[204:207]\n\t" \
    "v_mfma_f32_16x16x32_bf16 v[204:207], a[252:255], v[220:223], v[204:207]\n\t" \
    "v_fmac_f32 v100, v28, v192\n\t" \
    "v_fmac_f32 v101, v29, v193\n\t" \
    "v_fmac_f32 v102, v30, v194\n\t" \
    "v_fmac_f32 v103, v31, v195\n\t" \
    "v_fmac_f32 v104, v32, v196\n\t" \
    "v_fmac_f32 v105, v33, v197\n\t" \
    "v_fmac_f32 v106, v34, v198\n\t" \
    "v_fmac_f32 v107, v35, v199\n\t" \
    "v_fmac_f32 v108, v36, v200\n\t" \
    "v_fmac_f32 v109, v37, v201\n\t" \
    "v_fmac_f32 v110, v38, v202\n\t" \
    "v_fmac_f32 v111, v39, v203\n\t" \
    "v_fmac_f32 v112, v40, v204\n\t" \
    "v_fmac_f32 v113, v41, v205\n\t" \
    "v_fmac_f32 v114, v42, v206\n\t" \
    "v_fmac_f32 v115, v43, v207\n\t" \
    "v_pk_add_f32 v[132:133], v[100:101], v[102:103]\n\t" \
    "v_pk_mul_f32 v[134:135], v[100:101], v[100:101]\n\t" \
    "v_pk_add_f32 v[132:133], v[132:133], v[104:105]\n\t" \
    "v_pk_fma_f32 v[134:135], v[102:103], v[102:103], v[134:135]\n\t" \
    "v_pk_add_f32 v[132:133], v[132:133], v[106:107]\n\t" \
    "v_pk_fma_f32 v[134:135], v[104:105], v[104:105], v[134:135]\n\t" \
    "v_pk_add_f32 v[132:133], v[132:133], v[108:109]\n\t" \
    "v_pk_fma_f32 v[134:135], v[106:107], v[106:107], v[134:135]\n\t" \
    "v_pk_add_f32 v[132:133], v[132:133], v[110:111]\n\t" \
    "v_pk_fma_f32 v[134:135], v[108:109], v[108:109], v[134:135]\n\t" \
    "v_pk_add_f32 v[132:133], v[132:133], v[112:113]\n\t" \
    "v_pk_fma_f32 v[134:135], v[110:111], v[110:111], v[134:135]\n\t" \
    "v_pk_add_f32 v[132:133], v[132:133], v[114:115]\n\t" \
    "v_pk_fma_f32 v[134:135], v[112:113], v[112:113], v[134:135]\n\t" \
    "v_pk_fma_f32 v[134:135], v[114:115], v[114:115], v[134:135]\n\t" \
    "v_add_f32 v132, v132, v133\n\t" \
    "v_add_f32 v133, v134, v135\n\t" \
    "s_nop 1\n\t" \
    "v_permlane32_swap_b32 v132, v133\n\t" \
    "v_add_f32 v132, v132, v133\n\t" \
    "v_mov_b32 v133, v132\n\t" \
    "s_nop 1\n\t" \
    "v_permlane16_swap_b32 v132, v133\n\t" \
    "v_add_f32 v132, v132, v133\n\t" \
    "s_nop 1\n\t" \
    "v_add_f32_dpp v132, v132, v132 row_ror:8 row_mask:0xf bank_mask:0xf\n\t" \
    "ds_write_b32 v147, v132\n\t" \
    "v_cvt_pk_bf16_f32 v124, v100, v101\n\t" \
    "v_cvt_pk_bf16_f32 v125, v102, v103\n\t" \
    "v_cvt_pk_bf16_f32 v126, v104, v105\n\t" \
    "v_cvt_pk_bf16_f32 v127, v106, v107\n\t" \
    "v_cvt_pk_bf16_f32 v128, v108, v109\n\t" \
    "v_cvt_pk_bf16_f32 v129, v110, v111\n\t" \
    "v_cvt_pk_bf16_f32 v130, v112, v113\n\t" \
    "v_cvt_pk_bf16_f32 v131, v114, v115\n\t" \
    "s_waitcnt lgkmcnt(0)\n\t" \
    "s_barrier\n\t" \
    "ds_read_b128 v[136:139], v146\n\t" \
    "ds_read_b128 v[140:143], v146 offset:16\n\t" \
    "buffer_store_dwordx4 v[124:127], v120, s[8:11], s88 offen offset:0 sc1\n\t" \
    "buffer_store_dwordx4 v[128:131], v120, s[8:11], s88 offen offset:1024 sc1\n\t" \
    "s_waitcnt lgkmcnt(0)\n\t" \
    "v_pk_add_f32 v[136:137], v[136:137], v[138:139]\n\t" \
    "v_pk_add_f32 v[140:141], v[140:141], v[142:143]\n\t" \
    "v_add_f32 v136, v136, v137\n\t" \
    "v_add_f32 v140, v140, v141\n\t" \
    "v_mul_f32 v136, 0x3b000000, v136\n\t" \
    "v_mul_f32 v140, 0x3b000000, v140\n\t" \
    "v_fma_f32 v140, -v136, v136, v140\n\t" \
    "v_max_f32 v140, 0, v140\n\t" \
    "v_add_f32 v140, 0x3727c5ac, v140\n\t" \
    "v_rsq_f32 v140, v140\n\t" \
    "s_nop 0\n\t" \
    "v_mul_f32 v140, 0x3fb8aa3b, v140\n\t" \
    "v_mul_f32 v141, -v136, v140\n\t" \
    "v_pk_fma_f32 v[100:101], v[100:101], v[140:141], v[140:141] op_sel:[0,0,1] op_sel_hi:[1,0,1]\n\t" \
    "v_pk_fma_f32 v[102:103], v[102:103], v[140:141], v[140:141] op_sel:[0,0,1] op_sel_hi:[1,0,1]\n\t" \
    "v_pk_fma_f32 v[104:105], v[104:105], v[140:141], v[140:141] op_sel:[0,0,1] op_sel_hi:[1,0,1]\n\t" \
    "v_pk_fma_f32 v[106:107], v[106:107], v[140:141], v[140:141] op_sel:[0,0,1] op_sel_hi:[1,0,1]\n\t" \
    "v_pk_fma_f32 v[108:109], v[108:109], v[140:141], v[140:141] op_sel:[0,0,1] op_sel_hi:[1,0,1]\n\t" \
    "v_pk_fma_f32 v[110:111], v[110:111], v[140:141], v[140:141] op_sel:[0,0,1] op_sel_hi:[1,0,1]\n\t" \
    "v_pk_fma_f32 v[112:113], v[112:113], v[140:141], v[140:141] op_sel:[0,0,1] op_sel_hi:[1,0,1]\n\t" \
    "v_pk_fma_f32 v[114:115], v[114:115], v[140:141], v[140:141] op_sel:[0,0,1] op_sel_hi:[1,0,1]\n\t" \
    "v_pk_fma_f32 v[100:101], v[100:101], v[60:61], v[44:45]\n\t" \
    "v_pk_fma_f32 v[102:103], v[102:103], v[62:63], v[46:47]\n\t" \
    "v_pk_fma_f32 v[104:105], v[104:105], v[64:65], v[48:49]\n\t" \
    "v_pk_fma_f32 v[106:107], v[106:107], v[66:67], v[50:51]\n\t" \
    "v_pk_fma_f32 v[108:109], v[108:109], v[68:69], v[52:53]\n\t" \
    "v_pk_fma_f32 v[110:111], v[110:111], v[70:71], v[54:55]\n\t" \
    "v_pk_fma_f32 v[112:113], v[112:113], v[72:73], v[56:57]\n\t" \
    "v_pk_fma_f32 v[114:115], v[114:115], v[74:75], v[58:59]\n\t" \
    "v_cvt_pk_bf16_f32 v148, v100, v101\n\t" \
    "v_cvt_pk_bf16_f32 v149, v102, v103\n\t" \
    "v_cvt_pk_bf16_f32 v150, v104, v105\n\t" \
    "v_cvt_pk_bf16_f32 v151, v106, v107\n\t" \
    "buffer_store_dwordx4 v[148:151], v122, s[8:11], s91 offen offset:0 sc1\n\t" \
    "v_cvt_pk_bf16_f32 v152, v108, v109\n\t" \
    "v_cvt_pk_bf16_f32 v153, v110, v111\n\t" \
    "v_cvt_pk_bf16_f32 v154, v112, v113\n\t" \
    "v_cvt_pk_bf16_f32 v155, v114, v115\n\t" \
    "buffer_store_dwordx4 v[152:155], v122, s[8:11], s91 offen offset:2048 sc1\n\t" \
    "v_accvgpr_write_b32 a252, v10\n\t" \
    "v_accvgpr_write_b32 a253, v11\n\t" \
    "v_accvgpr_write_b32 a254, v10\n\t" \
    "v_accvgpr_write_b32 a255, v11\n\t" \
    "v_lshrrev_b32 v12, 4, v1\n\t" \
    "v_bfe_u32 v13, v1, 2, 2\n\t" \
    "v_and_b32 v14, 3, v1\n\t" \
    "v_lshl_add_u32 v15, v12, 3, v13\n\t" \
    "v_lshlrev_b32 v15, 8, v15\n\t" \
    "v_lshl_add_u32 v15, v14, 3, v15\n\t" \
    "buffer_load_dwordx4 a[92:95], v2, s[8:11], s21 offen offset:3072 sc1\n\t" \
    "v_and_b32 v12, 1, v12\n\t" \
    "v_lshl_add_u32 v12, v12, 2, v13\n\t" \
    "s_and_b32 s90, s59, 1\n\t" \
    "s_add_u32 s88, s90, 0\n\t" \
    "v_xor_b32 v13, s88, v12\n\t" \
    "v_lshl_add_u32 v58, v13, 5, v15\n\t" \
    "buffer_load_dwordx4 a[96:99], v2, s[8:11], s22 offen offset:0 sc1\n\t" \
    "v_add_u32 v65, 0x10000, v58\n\t" \
    "s_add_u32 s88, s90, 1\n\t" \
    "v_xor_b32 v13, s88, v12\n\t" \
    "v_lshl_add_u32 v59, v13, 5, v15\n\t" \
    "v_add_u32 v66, 0x10000, v59\n\t" \
    "s_add_u32 s88, s90, 2\n\t" \
    "buffer_load_dwordx4 a[100:103], v2, s[8:11], s22 offen offset:1024 sc1\n\t" \
    "v_xor_b32 v13, s88, v12\n\t" \
    "v_lshl_add_u32 v60, v13, 5, v15\n\t" \
    "v_add_u32 v67, 0x10000, v60\n\t" \
    "s_add_u32 s88, s90, 3\n\t" \
    "v_xor_b32 v13, s88, v12\n\t" \
    "v_lshl_add_u32 v61, v13, 5, v15\n\t" \
    "buffer_load_dwordx4 a[104:107], v2, s[8:11], s22 offen offset:2048 sc1\n\t" \
    "v_add_u32 v68, 0x10000, v61\n\t" \
    "s_add_u32 s88, s90, 4\n\t" \
    "v_xor_b32 v13, s88, v12\n\t" \
    "v_lshl_add_u32 v62, v13, 5, v15\n\t" \
    "v_add_u32 v69, 0x10000, v62\n\t" \
    "s_add_u32 s88, s90, 5\n\t" \
    "buffer_load_dwordx4 a[108:111], v2, s[8:11], s22 offen offset:3072 sc1\n\t" \
    "v_xor_b32 v13, s88, v12\n\t" \
    "v_lshl_add_u32 v63, v13, 5, v15\n\t" \
    "v_add_u32 v70, 0x10000, v63\n\t" \
    "s_cmp_eq_u32 s90, 0\n\t" \
    "s_cselect_b32 s88, 6, 0\n\t" \
    "v_xor_b32 v13, s88, v12\n\t" \
    "buffer_load_dwordx4 a[112:115], v2, s[8:11], s23 offen offset:0 sc1\n\t" \
    "v_lshl_add_u32 v64, v13, 5, v15\n\t" \
    "v_add_u32 v71, 0x10000, v64\n\t" \
    "v_lshlrev_b32 v14, 7, v14\n\t" \
    "s_lshl_b32 s88, s58, 11\n\t" \
    "s_lshl_b32 s89, s50, 9\n\t" \
    "s_add_u32 s88, s88, s89\n\t" \
    "buffer_load_dwordx4 a[116:119], v2, s[8:11], s23 offen offset:1024 sc1\n\t" \
    "s_add_u32 s91, s88, 131072\n\t" \
    "s_add_u32 s88, s88, 98304\n\t" \
    "s_lshl_b32 s80, s58, 17\n\t" \
    "s_lshl_b32 s89, s50, 15\n\t" \
    "s_add_u32 s80, s80, s89\n\t" \
    "s_add_u32 s80, s80, 6946816\n\t" \
    "buffer_load_dwordx4 a[120:123], v2, s[8:11], s23 offen offset:2048 sc1\n\t" \
    "s_add_u32 s81, s80, 4096\n\t" \
    "s_add_u32 s82, s80, 8192\n\t" \
    "s_add_u32 s83, s80, 12288\n\t" \
    "s_add_u32 s84, s80, 16384\n\t" \
    "s_add_u32 s85, s80, 20480\n\t" \
    "s_add_u32 s86, s80, 24576\n\t" \
    "buffer_load_dwordx4 a[124:127], v2, s[8:11], s23 offen offset:3072 sc1\n\t" \
    "s_add_u32 s87, s80, 28672\n\t" \
    "s_lshl_b32 s79, s58, 1\n\t" \
    "s_lshr_b32 s89, s50, 1\n\t" \
    "s_add_u32 s79, s79, s89\n\t" \
    "s_mul_i32 s79, s79, 56\n\t" \
    "s_mul_i32 s89, s59, 7\n\t" \
    "buffer_load_dwordx4 a[128:131], v2, s[8:11], s24 offen offset:0 sc1\n\t" \
    "s_add_u32 s89, s89, s90\n\t" \
    "s_lshr_b32 s89, s89, 1\n\t" \
    "s_add_u32 s94, s79, s89\n\t" \
    "s_lshr_b32 s89, s59, 1\n\t" \
    "s_mul_i32 s89, s89, 7\n\t" \
    "s_add_u32 s89, s89, 3\n\t" \
    "buffer_load_dwordx4 a[132:135], v2, s[8:11], s24 offen offset:1024 sc1\n\t" \
    "s_add_u32 s95, s79, s89\n\t" \
    "s_and_b32 s89, s50, 1\n\t" \
    "s_lshl_b32 s89, s89, 11\n\t" \
    "s_add_u32 s89, s89, 9043968\n\t" \
    "s_lshl_b32 s94, s94, 12\n\t" \
    "s_add_u32 s94, s94, s89\n\t" \
    "buffer_load_dwordx4 a[136:139], v2, s[8:11], s24 offen offset:2048 sc1\n\t" \
    "s_lshl_b32 s95, s95, 12\n\t" \
    "s_add_u32 s95, s95, s89\n\t" \
    "s_lshl_b32 s89, s90, 10\n\t" \
    "s_add_u32 s95, s95, s89\n\t" \
    "buffer_load_dwordx4 a[140:143], v2, s[8:11], s24 offen offset:3072 sc1\n\t" \
    "buffer_load_dwordx4 a[144:147], v2, s[8:11], s25 offen offset:0 sc1\n\t" \
    "s_lshl_b32 s89, s53, 7\n\t" \
    "s_add_u32 s89, s89, 98304\n\t" \
    "s_waitcnt vmcnt(14)\n\t" \
    "s_barrier\n\t" \
    "s_mov_b64 exec, s[92:93]\n\t" \
    "buffer_store_dwordx4 a[252:255], v2, s[8:11], s89 offen sc1\n\t" \
    "s_cmp_eq_u32 s6, 0\n\t" \
    "s_cselect_b32 exec_lo, exec_lo, 0\n\t" \
    "buffer_store_dwordx4 a[252:255], v2, s[8:11], 0 offen sc1\n\t" \
    "s_mov_b64 exec, -1\n\t" \
      \
    "buffer_load_dwordx4 a[148:151], v2, s[8:11], s25 offen offset:1024 sc1\n\t" \
    "buffer_load_dwordx4 a[152:155], v2, s[8:11], s25 offen offset:2048 sc1\n\t" \
    "buffer_load_dwordx4 a[156:159], v2, s[8:11], s25 offen offset:3072 sc1\n\t" \
    "buffer_load_dwordx4 a[160:163], v2, s[8:11], s26 offen offset:0 sc1\n\t" \
    "buffer_load_dwordx4 a[164:167], v2, s[8:11], s26 offen offset:1024 sc1\n\t" \
    "buffer_load_dwordx4 a[168:171], v2, s[8:11], s26 offen offset:2048 sc1\n\t" \
    "buffer_load_dwordx4 a[172:175], v2, s[8:11], s26 offen offset:3072 sc1\n\t" \
    "buffer_load_dwordx4 a[176:179], v2, s[8:11], s27 offen offset:0 sc1\n\t" \
    "buffer_load_dwordx4 a[180:183], v2, s[8:11], s27 offen offset:1024 sc1\n\t" \
    "buffer_load_dwordx4 a[184:187], v2, s[8:11], s27 offen offset:2048 sc1\n\t" \
    "buffer_load_dwordx4 a[188:191], v2, s[8:11], s27 offen offset:3072 sc1\n\t" \
    "buffer_load_dwordx4 a[192:195], v2, s[8:11], s28 offen offset:0 sc1\n\t" \
    "ds_read_b64_tr_b16 v[16:17], v58 offset:0\n\t" \
    "ds_read_b64_tr_b16 v[18:19], v58 offset:1024\n\t" \
    "ds_read_b64_tr_b16 v[20:21], v59 offset:0\n\t" \
    "ds_read_b64_tr_b16 v[22:23], v59 offset:1024\n\t" \
    "ds_read_b64_tr_b16 v[24:25], v60 offset:0\n\t" \
    "ds_read_b64_tr_b16 v[26:27], v60 offset:1024\n\t" \
    "ds_read_b64_tr_b16 v[28:29], v61 offset:0\n\t" \
    "ds_read_b64_tr_b16 v[30:31], v61 offset:1024\n\t" \
    "ds_read_b64_tr_b16 v[32:33], v58 offset:8192\n\t" \
    "ds_read_b64_tr_b16 v[34:35], v58 offset:9216\n\t" \
    "ds_read_b64_tr_b16 v[36:37], v59 offset:8192\n\t" \
    "ds_read_b64_tr_b16 v[38:39], v59 offset:9216\n\t" \
    "ds_read_b64_tr_b16 v[40:41], v60 offset:8192\n\t" \
    "ds_read_b64_tr_b16 v[42:43], v60 offset:9216\n\t" \
    "v_mov_b32 v3, 160112\n\t" \
    "v_mov_b32 v57, 1\n\t" \
    "s_lshl_b32 s78, s58, 4\n\t" \
    "s_add_u32 s78, s78, s59\n\t" \
    "s_lshl_b32 s78, s78, 7\n\t" \
    "s_add_u32 s78, s78, 131072\n\t" \
    ".Lpc3_%=:\n\t" \
    "buffer_load_dwordx2 v[12:13], v14, s[8:11], s88 offen sc1\n\t" \
    "s_waitcnt vmcnt(0)\n\t" \
    "v_cmp_ne_u64 vcc, s[54:55], v[12:13]\n\t" \
    "s_nop 0\n\t" \
    "s_cbranch_vccz .Lpc3_%=_ok\n\t" \
    "s_sub_u32 s62, s62, 1\n\t" \
    "s_cmp_lt_i32 s62, 0\n\t" \
    "s_cbranch_scc1 .Lpc3_%=_ok\n\t" \
    "s_sleep 2\n\t" \
    "s_branch .Lpc3_%=\n\t" \
    ".Lpc3_%=_ok:\n\t" \
    "buffer_load_dwordx4 v[128:131], v2, s[8:11], s80 offen offset:0 sc1\n\t" \
    "buffer_load_dwordx4 v[132:135], v2, s[8:11], s80 offen offset:1024 sc1\n\t" \
    "buffer_load_dwordx4 v[136:139], v2, s[8:11], s80 offen offset:2048 sc1\n\t" \
    "buffer_load_dwordx4 v[140:143], v2, s[8:11], s80 offen offset:3072 sc1\n\t" \
    "buffer_load_dwordx4 v[144:147], v2, s[8:11], s81 offen offset:0 sc1\n\t" \
    "buffer_load_dwordx4 v[148:151], v2, s[8:11], s81 offen offset:1024 sc1\n\t" \
    "buffer_load_dwordx4 v[152:155], v2, s[8:11], s81 offen offset:2048 sc1\n\t" \
    "buffer_load_dwordx4 v[156:159], v2, s[8:11], s81 offen offset:3072 sc1\n\t" \
    "buffer_load_dwordx4 v[160:163], v2, s[8:11], s82 offen offset:0 sc1\n\t" \
    "buffer_load_dwordx4 v[164:167], v2, s[8:11], s82 offen offset:1024 sc1\n\t" \
    "buffer_load_dwordx4 v[168:171], v2, s[8:11], s82 offen offset:2048 sc1\n\t" \
    "buffer_load_dwordx4 v[172:175], v2, s[8:11], s82 offen offset:3072 sc1\n\t" \
    "s_mov_b32 s64, s94\n\t" \
    "s_add_u32 s65, s94, 0x1000\n\t" \
    "s_add_u32 s66, s94, 0x2000\n\t" \
    "s_waitcnt lgkmcnt(10)\n\t" \
    "s_waitcnt vmcnt(10)\n\t" \
    "v_mfma_f32_16x16x32_bf16 v[72:75], v[16:19], v[128:131], 0\n\t" \
    "ds_read_b64_tr_b16 v[44:45], v61 offset:8192\n\t" \
    "v_mfma_f32_16x16x32_bf16 v[76:79], v[16:19], v[132:135], 0\n\t" \
    "ds_read_b64_tr_b16 v[46:47], v61 offset:9216\n\t" \
    "buffer_load_dwordx4 v[176:179], v2, s[8:11], s83 offen offset:0 sc1\n\t" \
    "v_mfma_f32_16x16x32_bf16 v[80:83], v[20:23], v[128:131], 0\n\t" \
    "ds_read_b64_tr_b16 v[48:49], v58 offset:16384\n\t" \
    "v_mfma_f32_16x16x32_bf16 v[84:87], v[20:23], v[132:135], 0\n\t" \
    "ds_read_b64_tr_b16 v[50:51], v58 offset:17408\n\t" \
    "s_waitcnt lgkmcnt(10)\n\t" \
    "v_mfma_f32_16x16x32_bf16 v[88:91], v[24:27], v[128:131], 0\n\t" \
    "ds_read_b64_tr_b16 v[52:53], v59 offset:16384\n\t" \
    "v_mfma_f32_16x16x32_bf16 v[92:95], v[24:27], v[132:135], 0\n\t" \
    "ds_read_b64_tr_b16 v[54:55], v59 offset:17408\n\t" \
    "buffer_load_dwordx4 v[180:183], v2, s[8:11], s83 offen offset:1024 sc1\n\t" \
    "v_mfma_f32_16x16x32_bf16 v[96:99], v[28:31], v[128:131], 0\n\t" \
    "ds_read_b64_tr_b16 v[16:17], v60 offset:16384\n\t" \
    "v_mfma_f32_16x16x32_bf16 v[100:103], v[28:31], v[132:135], 0\n\t" \
    "ds_read_b64_tr_b16 v[18:19], v60 offset:17408\n\t" \
    "s_waitcnt lgkmcnt(10)\n\t" \
    "s_waitcnt vmcnt(10)\n\t" \
    "v_mfma_f32_16x16x32_bf16 v[72:75], v[32:35], v[136:139], v[72:75]\n\t" \
    "ds_read_b64_tr_b16 v[20:21], v61 offset:16384\n\t" \
    "v_mfma_f32_16x16x32_bf16 v[76:79], v[32:35], v[140:143], v[76:79]\n\t" \
    "ds_read_b64_tr_b16 v[22:23], v61 offset:17408\n\t" \
    "buffer_load_dwordx4 v[184:187], v2, s[8:11], s83 offen offset:2048 sc1\n\t" \
    "v_mfma_f32_16x16x32_bf16 v[80:83], v[36:39], v[136:139], v[80:83]\n\t" \
    "ds_read_b64_tr_b16 v[24:25], v58 offset:24576\n\t" \
    "v_mfma_f32_16x16x32_bf16 v[84:87], v[36:39], v[140:143], v[84:87]\n\t" \
    "ds_read_b64_tr_b16 v[26:27], v58 offset:25600\n\t" \
    "s_waitcnt lgkmcnt(10)\n\t" \
    "v_mfma_f32_16x16x32_bf16 v[88:91], v[40:43], v[136:139], v[88:91]\n\t" \
    "ds_read_b64_tr_b16 v[28:29], v59 offset:24576\n\t" \
    "v_mfma_f32_16x16x32_bf16 v[92:95], v[40:43], v[140:143], v[92:95]\n\t" \
    "ds_read_b64_tr_b16 v[30:31], v59 offset:25600\n\t" \
    "buffer_load_dwordx4 v[188:191], v2, s[8:11], s83 offen offset:3072 sc1\n\t" \
    "v_mfma_f32_16x16x32_bf16 v[96:99], v[44:47], v[136:139], v[96:99]\n\t" \
    "ds_read_b64_tr_b16 v[32:33], v60 offset:24576\n\t" \
    "v_mfma_f32_16x16x32_bf16 v[100:103], v[44:47], v[140:143], v[100:103]\n\t" \
    "ds_read_b64_tr_b16 v[34:35], v60 offset:25600\n\t" \
    "s_waitcnt lgkmcnt(10)\n\t" \
    "s_waitcnt vmcnt(10)\n\t" \
    "v_mfma_f32_16x16x32_bf16 v[72:75], v[48:51], v[144:147], v[72:75]\n\t" \
    "ds_read_b64_tr_b16 v[36:37], v61 offset:24576\n\t" \
    "v_mfma_f32_16x16x32_bf16 v[76:79], v[48:51], v[148:151], v[76:79]\n\t" \
    "ds_read_b64_tr_b16 v[38:39], v61 offset:25600\n\t" \
    "buffer_load_dwordx4 v[192:195], v2, s[8:11], s84 offen offset:0 sc1\n\t" \
    "v_mfma_f32_16x16x32_bf16 v[80:83], v[52:55], v[144:147], v[80:83]\n\t" \
    "ds_read_b64_tr_b16 v[40:41], v58 offset:32768\n\t" \
    "v_mfma_f32_16x16x32_bf16 v[84:87], v[52:55], v[148:151], v[84:87]\n\t" \
    "ds_read_b64_tr_b16 v[42:43], v58 offset:33792\n\t" \
    "s_waitcnt lgkmcnt(10)\n\t" \
    "v_mfma_f32_16x16x32_bf16 v[88:91], v[16:19], v[144:147], v[88:91]\n\t" \
    "ds_read_b64_tr_b16 v[44:45], v59 offset:32768\n\t" \
    "v_mfma_f32_16x16x32_bf16 v[92:95], v[16:19], v[148:151], v[92:95]\n\t" \
    "ds_read_b64_tr_b16 v[46:47], v59 offset:33792\n\t" \
    "buffer_load_dwordx4 v[196:199], v2, s[8:11], s84 offen offset:1024 sc1\n\t" \
    "v_mfma_f32_16x16x32_bf16 v[96:99], v[20:23], v[144:147], v[96:99]\n\t" \
    "ds_read_b64_tr_b16 v[48:49], v60 offset:32768\n\t" \
    "v_mfma_f32_16x16x32_bf16 v[100:103], v[20:23], v[148:151], v[100:103]\n\t" \
    "ds_read_b64_tr_b16 v[50:51], v60 offset:33792\n\t" \
    "s_waitcnt lgkmcnt(10)\n\t" \
    "s_waitcnt vmcnt(10)\n\t" \
    "v_mfma_f32_16x16x32_bf16 v[72:75], v[24:27], v[152:155], v[72:75]\n\t" \
    "ds_read_b64_tr_b16 v[52:53], v61 offset:32768\n\t" \
    "v_mfma_f32_16x16x32_bf16 v[76:79], v[24:27], v[156:159], v[76:79]\n\t" \
    "ds_read_b64_tr_b16 v[54:55], v61 offset:33792\n\t" \
    "buffer_load_dwordx4 v[200:203], v2, s[8:11], s84 offen offset:2048 sc1\n\t" \
    "v_mfma_f32_16x16x32_bf16 v[80:83], v[28:31], v[152:155], v[80:83]\n\t" \
    "ds_read_b64_tr_b16 v[16:17], v58 offset:40960\n\t" \
    "v_mfma_f32_16x16x32_bf16 v[84:87], v[28:31], v[156:159], v[84:87]\n\t" \
    "ds_read_b64_tr_b16 v[18:19], v58 offset:41984\n\t" \
    "s_waitcnt lgkmcnt(10)\n\t" \
    "v_mfma_f32_16x16x32_bf16 v[88:91], v[32:35], v[152:155], v[88:91]\n\t" \
    "ds_read_b64_tr_b16 v[20:21], v59 offset:40960\n\t" \
    "v_mfma_f32_16x16x32_bf16 v[92:95], v[32:35], v[156:159], v[92:95]\n\t" \
    "ds_read_b64_tr_b16 v[22:23], v59 offset:41984\n\t" \
    "buffer_load_dwordx4 v[204:207], v2, s[8:11], s84 offen offset:3072 sc1\n\t" \
    "v_mfma_f32_16x16x32_bf16 v[96:99], v[36:39], v[152:155], v[96:99]\n\t" \
    "ds_read_b64_tr_b16 v[24:25], v60 offset:40960\n\t" \
    "v_mfma_f32_16x16x32_bf16 v[100:103], v[36:39], v[156:159], v[100:103]\n\t" \
    "ds_read_b64_tr_b16 v[26:27], v60 offset:41984\n\t" \
    "s_waitcnt lgkmcnt(10)\n\t" \
    "s_waitcnt vmcnt(10)\n\t" \
    "v_mfma_f32_16x16x32_bf16 v[72:75], v[40:43], v[160:163], v[72:75]\n\t" \
    "ds_read_b64_tr_b16 v[28:29], v61 offset:40960\n\t" \
    "v_mfma_f32_16x16x32_bf16 v[76:79], v[40:43], v[164:167], v[76:79]\n\t" \
    "ds_read_b64_tr_b16 v[30:31], v61 offset:41984\n\t" \
    "buffer_load_dwordx4 v[208:211], v2, s[8:11], s85 offen offset:0 sc1\n\t" \
    "v_mfma_f32_16x16x32_bf16 v[80:83], v[44:47], v[160:163], v[80:83]\n\t" \
    "ds_read_b64_tr_b16 v[32:33], v58 offset:49152\n\t" \
    "v_mfma_f32_16x16x32_bf16 v[84:87], v[44:47], v[164:167], v[84:87]\n\t" \
    "ds_read_b64_tr_b16 v[34:35], v58 offset:50176\n\t" \
    "s_waitcnt lgkmcnt(10)\n\t" \
    "v_mfma_f32_16x16x32_bf16 v[88:91], v[48:51], v[160:163], v[88:91]\n\t" \
    "ds_read_b64_tr_b16 v[36:37], v59 offset:49152\n\t" \
    "v_mfma_f32_16x16x32_bf16 v[92:95], v[48:51], v[164:167], v[92:95]\n\t" \
    "ds_read_b64_tr_b16 v[38:39], v59 offset:50176\n\t" \
    "buffer_load_dwordx4 v[212:215], v2, s[8:11], s85 offen offset:1024 sc1\n\t" \
    "v_mfma_f32_16x16x32_bf16 v[96:99], v[52:55], v[160:163], v[96:99]\n\t" \
    "ds_read_b64_tr_b16 v[40:41], v60 offset:49152\n\t" \
    "v_mfma_f32_16x16x32_bf16 v[100:103], v[52:55], v[164:167], v[100:103]\n\t" \
    "ds_read_b64_tr_b16 v[42:43], v60 offset:50176\n\t" \
    "s_waitcnt lgkmcnt(10)\n\t" \
    "s_waitcnt vmcnt(10)\n\t" \
    "v_mfma_f32_16x16x32_bf16 v[72:75], v[16:19], v[168:171], v[72:75]\n\t" \
    "ds_read_b64_tr_b16 v[44:45], v61 offset:49152\n\t" \
    "v_mfma_f32_16x16x32_bf16 v[76:79], v[16:19], v[172:175], v[76:79]\n\t" \
    "ds_read_b64_tr_b16 v[46:47], v61 offset:50176\n\t" \
    "buffer_load_dwordx4 v[216:219], v2, s[8:11], s85 offen offset:2048 sc1\n\t" \
    "v_mfma_f32_16x16x32_bf16 v[80:83], v[20:23], v[168:171], v[80:83]\n\t" \
    "ds_read_b64_tr_b16 v[48:49], v58 offset:57344\n\t" \
    "v_mfma_f32_16x16x32_bf16 v[84:87], v[20:23], v[172:175], v[84:87]\n\t" \
    "ds_read_b64_tr_b16 v[50:51], v58 offset:58368\n\t" \
    "s_waitcnt lgkmcnt(10)\n\t" \
    "v_mfma_f32_16x16x32_bf16 v[88:91], v[24:27], v[168:171], v[88:91]\n\t" \
    "ds_read_b64_tr_b16 v[52:53], v59 offset:57344\n\t" \
    "v_mfma_f32_16x16x32_bf16 v[92:95], v[24:27], v[172:175], v[92:95]\n\t" \
    "ds_read_b64_tr_b16 v[54:55], v59 offset:58368\n\t" \
    "buffer_load_dwordx4 v[220:223], v2, s[8:11], s85 offen offset:3072 sc1\n\t" \
    "v_mfma_f32_16x16x32_bf16 v[96:99], v[28:31], v[168:171], v[96:99]\n\t" \
    "ds_read_b64_tr_b16 v[16:17], v60 offset:57344\n\t" \
    "v_mfma_f32_16x16x32_bf16 v[100:103], v[28:31], v[172:175], v[100:103]\n\t" \
    "ds_read_b64_tr_b16 v[18:19], v60 offset:58368\n\t" \
    "s_waitcnt lgkmcnt(10)\n\t" \
    "s_waitcnt vmcnt(10)\n\t" \
    "v_mfma_f32_16x16x32_bf16 v[72:75], v[32:35], v[176:179], v[72:75]\n\t" \
    "ds_read_b64_tr_b16 v[20:21], v61 offset:57344\n\t" \
    "v_mfma_f32_16x16x32_bf16 v[76:79], v[32:35], v[180:183], v[76:79]\n\t" \
    "ds_read_b64_tr_b16 v[22:23], v61 offset:58368\n\t" \
    "buffer_load_dwordx4 v[224:227], v2, s[8:11], s86 offen offset:0 sc1\n\t" \
    "v_mfma_f32_16x16x32_bf16 v[80:83], v[36:39], v[176:179], v[80:83]\n\t" \
    "ds_read_b64_tr_b16 v[24:25], v65 offset:0\n\t" \
    "v_mfma_f32_16x16x32_bf16 v[84:87], v[36:39], v[180:183], v[84:87]\n\t" \
    "ds_read_b64_tr_b16 v[26:27], v65 offset:1024\n\t" \
    "s_waitcnt lgkmcnt(10)\n\t" \
    "v_mfma_f32_16x16x32_bf16 v[88:91], v[40:43], v[176:179], v[88:91]\n\t" \
    "ds_read_b64_tr_b16 v[28:29], v66 offset:0\n\t" \
    "v_mfma_f32_16x16x32_bf16 v[92:95], v[40:43], v[180:183], v[92:95]\n\t" \
    "ds_read_b64_tr_b16 v[30:31], v66 offset:1024\n\t" \
    "buffer_load_dwordx4 v[228:231], v2, s[8:11], s86 offen offset:1024 sc1\n\t" \
    "v_mfma_f32_16x16x32_bf16 v[96:99], v[44:47], v[176:179], v[96:99]\n\t" \
    "ds_read_b64_tr_b16 v[32:33], v67 offset:0\n\t" \
    "v_mfma_f32_16x16x32_bf16 v[100:103], v[44:47], v[180:183], v[100:103]\n\t" \
    "ds_read_b64_tr_b16 v[34:35], v67 offset:1024\n\t" \
    "s_waitcnt lgkmcnt(10)\n\t" \
    "s_waitcnt vmcnt(10)\n\t" \
    "v_mfma_f32_16x16x32_bf16 v[72:75], v[48:51], v[184:187], v[72:75]\n\t" \
    "ds_read_b64_tr_b16 v[36:37], v68 offset:0\n\t" \
    "v_mfma_f32_16x16x32_bf16 v[76:79], v[48:51], v[188:191], v[76:79]\n\t" \
    "ds_read_b64_tr_b16 v[38:39], v68 offset:1024\n\t" \
    "buffer_load_dwordx4 v[232:235], v2, s[8:11], s86 offen offset:2048 sc1\n\t" \
    "v_mfma_f32_16x16x32_bf16 v[80:83], v[52:55], v[184:187], v[80:83]\n\t" \
    "ds_read_b64_tr_b16 v[40:41], v65 offset:8192\n\t" \
    "v_mfma_f32_16x16x32_bf16 v[84:87], v[52:55], v[188:191], v[84:87]\n\t" \
    "ds_read_b64_tr_b16 v[42:43], v65 offset:9216\n\t" \
    "s_waitcnt lgkmcnt(10)\n\t" \
    "v_mfma_f32_16x16x32_bf16 v[88:91], v[16:19], v[184:187], v[88:91]\n\t" \
    "ds_read_b64_tr_b16 v[44:45], v66 offset:8192\n\t" \
    "v_mfma_f32_16x16x32_bf16 v[92:95], v[16:19], v[188:191], v[92:95]\n\t" \
    "ds_read_b64_tr_b16 v[46:47], v66 offset:9216\n\t" \
    "buffer_load_dwordx4 v[236:239], v2, s[8:11], s86 offen offset:3072 sc1\n\t" \
    "v_mfma_f32_16x16x32_bf16 v[96:99], v[20:23], v[184:187], v[96:99]\n\t" \
    "ds_read_b64_tr_b16 v[48:49], v67 offset:8192\n\t" \
    "v_mfma_f32_16x16x32_bf16 v[100:103], v[20:23], v[188:191], v[100:103]\n\t" \
    "ds_read_b64_tr_b16 v[50:51], v67 offset:9216\n\t" \
    "s_waitcnt lgkmcnt(10)\n\t" \
    "s_waitcnt vmcnt(10)\n\t" \
    "v_mfma_f32_16x16x32_bf16 v[72:75], v[24:27], v[192:195], v[72:75]\n\t" \
    "ds_read_b64_tr_b16 v[52:53], v68 offset:8192\n\t" \
    "v_mfma_f32_16x16x32_bf16 v[76:79], v[24:27], v[196:199], v[76:79]\n\t" \
    "ds_read_b64_tr_b16 v[54:55], v68 offset:9216\n\t" \
    "buffer_load_dwordx4 v[240:243], v2, s[8:11], s87 offen offset:0 sc1\n\t" \
    "v_mfma_f32_16x16x32_bf16 v[80:83], v[28:31], v[192:195], v[80:83]\n\t" \
    "ds_read_b64_tr_b16 v[16:17], v65 offset:16384\n\t" \
    "v_mfma_f32_16x16x32_bf16 v[84:87], v[28:31], v[196:199], v[84:87]\n\t" \
    "ds_read_b64_tr_b16 v[18:19], v65 offset:17408\n\t" \
    "s_waitcnt lgkmcnt(10)\n\t" \
    "v_mfma_f32_16x16x32_bf16 v[88:91], v[32:35], v[192:195], v[88:91]\n\t" \
    "ds_read_b64_tr_b16 v[20:21], v66 offset:16384\n\t" \
    "v_mfma_f32_16x16x32_bf16 v[92:95], v[32:35], v[196:199], v[92:95]\n\t" \
    "ds_read_b64_tr_b16 v[22:23], v66 offset:17408\n\t" \
    "buffer_load_dwordx4 v[244:247], v2, s[8:11], s87 offen offset:1024 sc1\n\t" \
    "v_mfma_f32_16x16x32_bf16 v[96:99], v[36:39], v[192:195], v[96:99]\n\t" \
    "ds_read_b64_tr_b16 v[24:25], v67 offset:16384\n\t" \
    "v_mfma_f32_16x16x32_bf16 v[100:103], v[36:39], v[196:199], v[100:103]\n\t" \
    "ds_read_b64_tr_b16 v[26:27], v67 offset:17408\n\t" \
    "s_waitcnt lgkmcnt(10)\n\t" \
    "s_waitcnt vmcnt(10)\n\t" \
    "v_mfma_f32_16x16x32_bf16 v[72:75], v[40:43], v[200:203], v[72:75]\n\t" \
    "ds_read_b64_tr_b16 v[28:29], v68 offset:16384\n\t" \
    "v_mfma_f32_16x16x32_bf16 v[76:79], v[40:43], v[204:207], v[76:79]\n\t" \
    "ds_read_b64_tr_b16 v[30:31], v68 offset:17408\n\t" \
    "buffer_load_dwordx4 v[248:251], v2, s[8:11], s87 offen offset:2048 sc1\n\t" \
    "v_mfma_f32_16x16x32_bf16 v[80:83], v[44:47], v[200:203], v[80:83]\n\t" \
    "ds_read_b64_tr_b16 v[32:33], v65 offset:24576\n\t" \
    "v_mfma_f32_16x16x32_bf16 v[84:87], v[44:47], v[204:207], v[84:87]\n\t" \
    "ds_read_b64_tr_b16 v[34:35], v65 offset:25600\n\t" \
    "s_waitcnt lgkmcnt(10)\n\t" \
    "v_mfma_f32_16x16x32_bf16 v[88:91], v[48:51], v[200:203], v[88:91]\n\t" \
    "ds_read_b64_tr_b16 v[36:37], v66 offset:24576\n\t" \
    "v_mfma_f32_16x16x32_bf16 v[92:95], v[48:51], v[204:207], v[92:95]\n\t" \
    "ds_read_b64_tr_b16 v[38:39], v66 offset:25600\n\t" \
    "buffer_load_dwordx4 v[252:255], v2, s[8:11], s87 offen offset:3072 sc1\n\t" \
    "v_mfma_f32_16x16x32_bf16 v[96:99], v[52:55], v[200:203], v[96:99]\n\t" \
    "ds_read_b64_tr_b16 v[40:41], v67 offset:24576\n\t" \
    "v_mfma_f32_16x16x32_bf16 v[100:103], v[52:55], v[204:207], v[100:103]\n\t" \
    "ds_read_b64_tr_b16 v[42:43], v67 offset:25600\n\t" \
    "s_waitcnt lgkmcnt(10)\n\t" \
    "s_waitcnt vmcnt(10)\n\t" \
    "v_mfma_f32_16x16x32_bf16 v[72:75], v[16:19], v[208:211], v[72:75]\n\t" \
    "ds_read_b64_tr_b16 v[44:45], v68 offset:24576\n\t" \
    "v_mfma_f32_16x16x32_bf16 v[76:79], v[16:19], v[212:215], v[76:79]\n\t" \
    "ds_read_b64_tr_b16 v[46:47], v68 offset:25600\n\t" \
    "buffer_load_dwordx4 a[196:199], v2, s[8:11], s28 offen offset:1024 sc1\n\t" \
    "v_mfma_f32_16x16x32_bf16 v[80:83], v[20:23], v[208:211], v[80:83]\n\t" \
    "ds_read_b64_tr_b16 v[48:49], v65 offset:32768\n\t" \
    "v_mfma_f32_16x16x32_bf16 v[84:87], v[20:23], v[212:215], v[84:87]\n\t" \
    "ds_read_b64_tr_b16 v[50:51], v65 offset:33792\n\t" \
    "s_waitcnt lgkmcnt(10)\n\t" \
    "v_mfma_f32_16x16x32_bf16 v[88:91], v[24:27], v[208:211], v[88:91]\n\t" \
    "ds_read_b64_tr_b16 v[52:53], v66 offset:32768\n\t" \
    "v_mfma_f32_16x16x32_bf16 v[92:95], v[24:27], v[212:215], v[92:95]\n\t" \
    "ds_read_b64_tr_b16 v[54:55], v66 offset:33792\n\t" \
    "buffer_load_dwordx4 a[200:203], v2, s[8:11], s28 offen offset:2048 sc1\n\t" \
    "v_mfma_f32_16x16x32_bf16 v[96:99], v[28:31], v[208:211], v[96:99]\n\t" \
    "ds_read_b64_tr_b16 v[16:17], v67 offset:32768\n\t" \
    "v_mfma_f32_16x16x32_bf16 v[100:103], v[28:31], v[212:215], v[100:103]\n\t" \
    "ds_read_b64_tr_b16 v[18:19], v67 offset:33792\n\t" \
    "s_waitcnt lgkmcnt(10)\n\t" \
    "s_waitcnt vmcnt(10)\n\t" \
    "v_mfma_f32_16x16x32_bf16 v[72:75], v[32:35], v[216:219], v[72:75]\n\t" \
    "ds_read_b64_tr_b16 v[20:21], v68 offset:32768\n\t" \
    "v_mfma_f32_16x16x32_bf16 v[76:79], v[32:35], v[220:223], v[76:79]\n\t" \
    "ds_read_b64_tr_b16 v[22:23], v68 offset:33792\n\t" \
    "buffer_load_dwordx4 a[204:207], v2, s[8:11], s28 offen offset:3072 sc1\n\t" \
    "v_mfma_f32_16x16x32_bf16 v[80:83], v[36:39], v[216:219], v[80:83]\n\t" \
    "ds_read_b64_tr_b16 v[24:25], v65 offset:40960\n\t" \
    "v_mfma_f32_16x16x32_bf16 v[84:87], v[36:39], v[220:223], v[84:87]\n\t" \
    "ds_read_b64_tr_b16 v[26:27], v65 offset:41984\n\t" \
    "s_waitcnt lgkmcnt(10)\n\t" \
    "v_mfma_f32_16x16x32_bf16 v[88:91], v[40:43], v[216:219], v[88:91]\n\t" \
    "ds_read_b64_tr_b16 v[28:29], v66 offset:40960\n\t" \
    "v_mfma_f32_16x16x32_bf16 v[92:95], v[40:43], v[220:223], v[92:95]\n\t" \
    "ds_read_b64_tr_b16 v[30:31], v66 offset:41984\n\t" \
    "buffer_load_dwordx4 a[208:211], v2, s[8:11], s29 offen offset:0 sc1\n\t" \
    "v_mfma_f32_16x16x32_bf16 v[96:99], v[44:47], v[216:219], v[96:99]\n\t" \
    "ds_read_b64_tr_b16 v[32:33], v67 offset:40960\n\t" \
    "v_mfma_f32_16x16x32_bf16 v[100:103], v[44:47], v[220:223], v[100:103]\n\t" \
    "ds_read_b64_tr_b16 v[34:35], v67 offset:41984\n\t" \
    "s_waitcnt lgkmcnt(10)\n\t" \
    "s_waitcnt vmcnt(10)\n\t" \
    "v_mfma_f32_16x16x32_bf16 v[72:75], v[48:51], v[224:227], v[72:75]\n\t" \
    "ds_read_b64_tr_b16 v[36:37], v68 offset:40960\n\t" \
    "v_mfma_f32_16x16x32_bf16 v[76:79], v[48:51], v[228:231], v[76:79]\n\t" \
    "ds_read_b64_tr_b16 v[38:39], v68 offset:41984\n\t" \
    "buffer_load_dwordx4 a[212:215], v2, s[8:11], s29 offen offset:1024 sc1\n\t" \
    "v_mfma_f32_16x16x32_bf16 v[80:83], v[52:55], v[224:227], v[80:83]\n\t" \
    "ds_read_b64_tr_b16 v[40:41], v65 offset:49152\n\t" \
    "v_mfma_f32_16x16x32_bf16 v[84:87], v[52:55], v[228:231], v[84:87]\n\t" \
    "ds_read_b64_tr_b16 v[42:43], v65 offset:50176\n\t" \
    "s_waitcnt lgkmcnt(10)\n\t" \
    "v_mfma_f32_16x16x32_bf16 v[88:91], v[16:19], v[224:227], v[88:91]\n\t" \
    "ds_read_b64_tr_b16 v[44:45], v66 offset:49152\n\t" \
    "v_mfma_f32_16x16x32_bf16 v[92:95], v[16:19], v[228:231], v[92:95]\n\t" \
    "ds_read_b64_tr_b16 v[46:47], v66 offset:50176\n\t" \
    "buffer_load_dwordx4 a[216:219], v2, s[8:11], s29 offen offset:2048 sc1\n\t" \
    "v_mfma_f32_16x16x32_bf16 v[96:99], v[20:23], v[224:227], v[96:99]\n\t" \
    "ds_read_b64_tr_b16 v[48:49], v67 offset:49152\n\t" \
    "v_mfma_f32_16x16x32_bf16 v[100:103], v[20:23], v[228:231], v[100:103]\n\t" \
    "ds_read_b64_tr_b16 v[50:51], v67 offset:50176\n\t" \
    "s_waitcnt lgkmcnt(10)\n\t" \
    "s_waitcnt vmcnt(10)\n\t" \
    "v_mfma_f32_16x16x32_bf16 v[72:75], v[24:27], v[232:235], v[72:75]\n\t" \
    "ds_read_b64_tr_b16 v[52:53], v68 offset:49152\n\t" \
    "v_mfma_f32_16x16x32_bf16 v[76:79], v[24:27], v[236:239], v[76:79]\n\t" \
    "ds_read_b64_tr_b16 v[54:55], v68 offset:50176\n\t" \
    "buffer_load_dwordx4 a[220:223], v2, s[8:11], s29 offen offset:3072 sc1\n\t" \
    "v_mfma_f32_16x16x32_bf16 v[80:83], v[28:31], v[232:235], v[80:83]\n\t" \
    "ds_read_b64_tr_b16 v[16:17], v65 offset:57344\n\t" \
    "v_mfma_f32_16x16x32_bf16 v[84:87], v[28:31], v[236:239], v[84:87]\n\t" \
    "ds_read_b64_tr_b16 v[18:19], v65 offset:58368\n\t" \
    "s_waitcnt lgkmcnt(10)\n\t" \
    "v_mfma_f32_16x16x32_bf16 v[88:91], v[32:35], v[232:235], v[88:91]\n\t" \
    "ds_read_b64_tr_b16 v[20:21], v66 offset:57344\n\t" \
    "v_mfma_f32_16x16x32_bf16 v[92:95], v[32:35], v[236:239], v[92:95]\n\t" \
    "ds_read_b64_tr_b16 v[22:23], v66 offset:58368\n\t" \
    "v_mfma_f32_16x16x32_bf16 v[96:99], v[36:39], v[232:235], v[96:99]\n\t" \
    "ds_read_b64_tr_b16 v[24:25], v67 offset:57344\n\t" \
    "v_mfma_f32_16x16x32_bf16 v[100:103], v[36:39], v[236:239], v[100:103]\n\t" \
    "ds_read_b64_tr_b16 v[26:27], v67 offset:58368\n\t" \
    "s_waitcnt lgkmcnt(10)\n\t" \
    "s_waitcnt vmcnt(9)\n\t" \
    "v_mfma_f32_16x16x32_bf16 v[72:75], v[40:43], v[240:243], v[72:75]\n\t" \
    "ds_read_b64_tr_b16 v[28:29], v68 offset:57344\n\t" \
    "v_mfma_f32_16x16x32_bf16 v[76:79], v[40:43], v[244:247], v[76:79]\n\t" \
    "ds_read_b64_tr_b16 v[30:31], v68 offset:58368\n\t" \
    "v_mfma_f32_16x16x32_bf16 v[80:83], v[44:47], v[240:243], v[80:83]\n\t" \
    "ds_read_b64_tr_b16 v[32:33], v62 offset:0\n\t" \
    "v_mfma_f32_16x16x32_bf16 v[84:87], v[44:47], v[244:247], v[84:87]\n\t" \
    "ds_read_b64_tr_b16 v[34:35], v62 offset:1024\n\t" \
    "s_waitcnt lgkmcnt(10)\n\t" \
    "v_mfma_f32_16x16x32_bf16 v[88:91], v[48:51], v[240:243], v[88:91]\n\t" \
    "ds_read_b64_tr_b16 v[36:37], v62 offset:8192\n\t" \
    "v_mfma_f32_16x16x32_bf16 v[92:95], v[48:51], v[244:247], v[92:95]\n\t" \
    "ds_read_b64_tr_b16 v[38:39], v62 offset:9216\n\t" \
    "v_mfma_f32_16x16x32_bf16 v[96:99], v[52:55], v[240:243], v[96:99]\n\t" \
    "ds_read_b64_tr_b16 v[40:41], v62 offset:16384\n\t" \
    "v_mfma_f32_16x16x32_bf16 v[100:103], v[52:55], v[244:247], v[100:103]\n\t" \
    "ds_read_b64_tr_b16 v[42:43], v62 offset:17408\n\t" \
    "s_waitcnt lgkmcnt(10)\n\t" \
    "s_waitcnt vmcnt(7)\n\t" \
    "v_mfma_f32_16x16x32_bf16 v[72:75], v[16:19], v[248:251], v[72:75]\n\t" \
    "ds_read_b64_tr_b16 v[44:45], v62 offset:24576\n\t" \
    "v_mfma_f32_16x16x32_bf16 v[76:79], v[16:19], v[252:255], v[76:79]\n\t" \
    "ds_read_b64_tr_b16 v[46:47], v62 offset:25600\n\t" \
    "v_mfma_f32_16x16x32_bf16 v[80:83], v[20:23], v[248:251], v[80:83]\n\t" \
    "ds_read_b64_tr_b16 v[48:49], v62 offset:32768\n\t" \
    "v_mfma_f32_16x16x32_bf16 v[84:87], v[20:23], v[252:255], v[84:87]\n\t" \
    "ds_read_b64_tr_b16 v[50:51], v62 offset:33792\n\t" \
    "s_waitcnt lgkmcnt(10)\n\t" \
    "v_mfma_f32_16x16x32_bf16 v[88:91], v[24:27], v[248:251], v[88:91]\n\t" \
    "ds_read_b64_tr_b16 v[52:53], v62 offset:40960\n\t" \
    "v_mfma_f32_16x16x32_bf16 v[92:95], v[24:27], v[252:255], v[92:95]\n\t" \
    "ds_read_b64_tr_b16 v[54:55], v62 offset:41984\n\t" \
    "v_exp_f32 v12, -v72\n\t" \
    "v_exp_f32 v13, -v73\n\t" \
    "v_exp_f32 v14, -v74\n\t" \
    "v_exp_f32 v15, -v75\n\t" \
    "v_add_f32 v12, 1.0, v12\n\t" \
    "v_add_f32 v13, 1.0, v13\n\t" \
    "v_add_f32 v14, 1.0, v14\n\t" \
    "v_add_f32 v15, 1.0, v15\n\t" \
    "v_mfma_f32_16x16x32_bf16 v[96:99], v[28:31], v[248:251], v[96:99]\n\t" \
    "ds_read_b64_tr_b16 v[16:17], v62 offset:49152\n\t" \
    "v_rcp_f32 v12, v12\n\t" \
    "v_rcp_f32 v13, v13\n\t" \
    "v_rcp_f32 v14, v14\n\t" \
    "v_rcp_f32 v15, v15\n\t" \
    "v_mul_f32 v72, v72, v12\n\t" \
    "v_mul_f32 v73, v73, v13\n\t" \
    "v_mul_f32 v74, v74, v14\n\t" \
    "v_mul_f32 v75, v75, v15\n\t" \
    "v_mfma_f32_16x16x32_bf16 v[100:103], v[28:31], v[252:255], v[100:103]\n\t" \
    "ds_read_b64_tr_b16 v[18:19], v62 offset:50176\n\t" \
    "v_cvt_pk_bf16_f32 v72, v72, v73\n\t" \
    "v_cvt_pk_bf16_f32 v73, v74, v75\n\t" \
    "v_exp_f32 v12, -v76\n\t" \
    "v_exp_f32 v13, -v77\n\t" \
    "v_exp_f32 v14, -v78\n\t" \
    "v_exp_f32 v15, -v79\n\t" \
    "v_add_f32 v12, 1.0, v12\n\t" \
    "v_add_f32 v13, 1.0, v13\n\t" \
    "s_waitcnt lgkmcnt(10)\n\t" \
    "v_mfma_f32_16x16x32_bf16 v[104:107], v[32:35], v[128:131], 0\n\t" \
    "ds_read_b64_tr_b16 v[20:21], v62 offset:57344\n\t" \
    "v_add_f32 v14, 1.0, v14\n\t" \
    "v_add_f32 v15, 1.0, v15\n\t" \
    "v_rcp_f32 v12, v12\n\t" \
    "v_rcp_f32 v13, v13\n\t" \
    "v_rcp_f32 v14, v14\n\t" \
    "v_rcp_f32 v15, v15\n\t" \
    "v_mul_f32 v76, v76, v12\n\t" \
    "v_mul_f32 v77, v77, v13\n\t" \
    "v_mfma_f32_16x16x32_bf16 v[108:111], v[32:35], v[132:135], 0\n\t" \
    "ds_read_b64_tr_b16 v[22:23], v62 offset:58368\n\t" \
    "v_mul_f32 v78, v78, v14\n\t" \
    "v_mul_f32 v79, v79, v15\n\t" \
    "v_cvt_pk_bf16_f32 v76, v76, v77\n\t" \
    "v_cvt_pk_bf16_f32 v77, v78, v79\n\t" \
    "v_exp_f32 v12, -v80\n\t" \
    "v_exp_f32 v13, -v81\n\t" \
    "v_exp_f32 v14, -v82\n\t" \
    "v_exp_f32 v15, -v83\n\t" \
    "v_mfma_f32_16x16x32_bf16 v[104:107], v[36:39], v[136:139], v[104:107]\n\t" \
    "ds_read_b64_tr_b16 v[24:25], v69 offset:0\n\t" \
    "v_add_f32 v12, 1.0, v12\n\t" \
    "v_add_f32 v13, 1.0, v13\n\t" \
    "v_add_f32 v14, 1.0, v14\n\t" \
    "v_add_f32 v15, 1.0, v15\n\t" \
    "v_rcp_f32 v12, v12\n\t" \
    "v_rcp_f32 v13, v13\n\t" \
    "v_rcp_f32 v14, v14\n\t" \
    "v_rcp_f32 v15, v15\n\t" \
    "v_mfma_f32_16x16x32_bf16 v[108:111], v[36:39], v[140:143], v[108:111]\n\t" \
    "ds_read_b64_tr_b16 v[26:27], v69 offset:1024\n\t" \
    "v_mul_f32 v80, v80, v12\n\t" \
    "v_mul_f32 v81, v81, v13\n\t" \
    "v_mul_f32 v82, v82, v14\n\t" \
    "v_mul_f32 v83, v83, v15\n\t" \
    "v_cvt_pk_bf16_f32 v74, v80, v81\n\t" \
    "v_cvt_pk_bf16_f32 v75, v82, v83\n\t" \
    "v_exp_f32 v12, -v84\n\t" \
    "v_exp_f32 v13, -v85\n\t" \
    "s_waitcnt lgkmcnt(10)\n\t" \
    "v_mfma_f32_16x16x32_bf16 v[104:107], v[40:43], v[144:147], v[104:107]\n\t" \
    "s_lshl_b32 s80, s61, 2\n\t" \
    "ds_read_b64_tr_b16 v[28:29], v69 offset:8192\n\t" \
    "buffer_store_dwordx4 v[72:75], v2, s[8:11], s64 offen offset:0 sc1\n\t" \
    "v_exp_f32 v14, -v86\n\t" \
    "v_exp_f32 v15, -v87\n\t" \
    "v_add_f32 v12, 1.0, v12\n\t" \
    "v_add_f32 v13, 1.0, v13\n\t" \
    "v_add_f32 v14, 1.0, v14\n\t" \
    "v_add_f32 v15, 1.0, v15\n\t" \
    "v_rcp_f32 v12, v12\n\t" \
    "v_rcp_f32 v13, v13\n\t" \
    "v_mfma_f32_16x16x32_bf16 v[108:111], v[40:43], v[148:151], v[108:111]\n\t" \
    "s_add_u32 s80, s80, s50\n\t" \
    "ds_read_b64_tr_b16 v[30:31], v69 offset:9216\n\t" \
    "v_rcp_f32 v14, v14\n\t" \
    "v_rcp_f32 v15, v15\n\t" \
    "v_mul_f32 v84, v84, v12\n\t" \
    "v_mul_f32 v85, v85, v13\n\t" \
    "v_mul_f32 v86, v86, v14\n\t" \
    "v_mul_f32 v87, v87, v15\n\t" \
    "v_cvt_pk_bf16_f32 v78, v84, v85\n\t" \
    "v_cvt_pk_bf16_f32 v79, v86, v87\n\t" \
    "v_mfma_f32_16x16x32_bf16 v[104:107], v[44:47], v[152:155], v[104:107]\n\t" \
    "s_lshl_b32 s80, s80, 14\n\t" \
    "ds_read_b64_tr_b16 v[32:33], v69 offset:16384\n\t" \
    "v_exp_f32 v12, -v88\n\t" \
    "v_exp_f32 v13, -v89\n\t" \
    "v_exp_f32 v14, -v90\n\t" \
    "v_exp_f32 v15, -v91\n\t" \
    "v_add_f32 v12, 1.0, v12\n\t" \
    "v_add_f32 v13, 1.0, v13\n\t" \
    "v_add_f32 v14, 1.0, v14\n\t" \
    "v_add_f32 v15, 1.0, v15\n\t" \
    "v_mfma_f32_16x16x32_bf16 v[108:111], v[44:47], v[156:159], v[108:111]\n\t" \
    "s_lshl_b32 s89, s60, 11\n\t" \
    "ds_read_b64_tr_b16 v[34:35], v69 offset:17408\n\t" \
    "buffer_store_dwordx4 v[76:79], v2, s[8:11], s64 offen offset:1024 sc1\n\t" \
    "v_rcp_f32 v12, v12\n\t" \
    "v_rcp_f32 v13, v13\n\t" \
    "v_rcp_f32 v14, v14\n\t" \
    "v_rcp_f32 v15, v15\n\t" \
    "v_mul_f32 v88, v88, v12\n\t" \
    "v_mul_f32 v89, v89, v13\n\t" \
    "v_mul_f32 v90, v90, v14\n\t" \
    "v_mul_f32 v91, v91, v15\n\t" \
    "s_waitcnt lgkmcnt(10)\n\t" \
    "v_mfma_f32_16x16x32_bf16 v[104:107], v[48:51], v[160:163], v[104:107]\n\t" \
    "s_add_u32 s80, s80, s89\n\t" \
    "ds_read_b64_tr_b16 v[36:37], v69 offset:24576\n\t" \
    "v_cvt_pk_bf16_f32 v88, v88, v89\n\t" \
    "v_cvt_pk_bf16_f32 v89, v90, v91\n\t" \
    "v_exp_f32 v12, -v92\n\t" \
    "v_exp_f32 v13, -v93\n\t" \
    "v_exp_f32 v14, -v94\n\t" \
    "v_exp_f32 v15, -v95\n\t" \
    "v_add_f32 v12, 1.0, v12\n\t" \
    "v_add_f32 v13, 1.0, v13\n\t" \
    "v_mfma_f32_16x16x32_bf16 v[108:111], v[48:51], v[164:167], v[108:111]\n\t" \
    "s_add_u32 s80, s80, 4849664\n\t" \
    "ds_read_b64_tr_b16 v[38:39], v69 offset:25600\n\t" \
    "v_add_f32 v14, 1.0, v14\n\t" \
    "v_add_f32 v15, 1.0, v15\n\t" \
    "v_rcp_f32 v12, v12\n\t" \
    "v_rcp_f32 v13, v13\n\t" \
    "v_rcp_f32 v14, v14\n\t" \
    "v_rcp_f32 v15, v15\n\t" \
    "v_mul_f32 v92, v92, v12\n\t" \
    "v_mul_f32 v93, v93, v13\n\t" \
    "v_mfma_f32_16x16x32_bf16 v[104:107], v[52:55], v[168:171], v[104:107]\n\t" \
    "s_mul_i32 s16, s61, 56\n\t" \
    "ds_read_b64_tr_b16 v[40:41], v69 offset:32768\n\t" \
    "v_mul_f32 v94, v94, v14\n\t" \
    "v_mul_f32 v95, v95, v15\n\t" \
    "v_cvt_pk_bf16_f32 v92, v92, v93\n\t" \
    "v_cvt_pk_bf16_f32 v93, v94, v95\n\t" \
    "v_exp_f32 v12, -v96\n\t" \
    "v_exp_f32 v13, -v97\n\t" \
    "v_exp_f32 v14, -v98\n\t" \
    "v_exp_f32 v15, -v99\n\t" \
    "v_mfma_f32_16x16x32_bf16 v[108:111], v[52:55], v[172:175], v[108:111]\n\t" \
    "s_mul_i32 s88, s50, 14\n\t" \
    "ds_read_b64_tr_b16 v[42:43], v69 offset:33792\n\t" \
    "v_add_f32 v12, 1.0, v12\n\t" \
    "v_add_f32 v13, 1.0, v13\n\t" \
    "v_add_f32 v14, 1.0, v14\n\t" \
    "v_add_f32 v15, 1.0, v15\n\t" \
    "v_rcp_f32 v12, v12\n\t" \
    "v_rcp_f32 v13, v13\n\t" \
    "v_rcp_f32 v14, v14\n\t" \
    "v_rcp_f32 v15, v15\n\t" \
    "s_waitcnt lgkmcnt(10)\n\t" \
    "v_mfma_f32_16x16x32_bf16 v[104:107], v[16:19], v[176:179], v[104:107]\n\t" \
    "s_add_u32 s16, s16, s88\n\t" \
    "ds_read_b64_tr_b16 v[44:45], v69 offset:40960\n\t" \
    "v_mul_f32 v96, v96, v12\n\t" \
    "v_mul_f32 v97, v97, v13\n\t" \
    "v_mul_f32 v98, v98, v14\n\t" \
    "v_mul_f32 v99, v99, v15\n\t" \
    "v_cvt_pk_bf16_f32 v90, v96, v97\n\t" \
    "v_cvt_pk_bf16_f32 v91, v98, v99\n\t" \
    "v_exp_f32 v12, -v100\n\t" \
    "v_exp_f32 v13, -v101\n\t" \
    "v_mfma_f32_16x16x32_bf16 v[108:111], v[16:19], v[180:183], v[108:111]\n\t" \
    "s_lshl_b32 s16, s16, 12\n\t" \
    "ds_read_b64_tr_b16 v[46:47], v69 offset:41984\n\t" \
    "buffer_store_dwordx4 v[88:91], v2, s[8:11], s65 offen offset:0 sc1\n\t" \
    "v_exp_f32 v14, -v102\n\t" \
    "v_exp_f32 v15, -v103\n\t" \
    "v_add_f32 v12, 1.0, v12\n\t" \
    "v_add_f32 v13, 1.0, v13\n\t" \
    "v_add_f32 v14, 1.0, v14\n\t" \
    "v_add_f32 v15, 1.0, v15\n\t" \
    "v_rcp_f32 v12, v12\n\t" \
    "v_rcp_f32 v13, v13\n\t" \
    "v_mfma_f32_16x16x32_bf16 v[104:107], v[20:23], v[184:187], v[104:107]\n\t" \
    "s_add_u32 s16, s16, 9043968\n\t" \
    "ds_read_b64_tr_b16 v[48:49], v69 offset:49152\n\t" \
    "v_rcp_f32 v14, v14\n\t" \
    "v_rcp_f32 v15, v15\n\t" \
    "v_mul_f32 v100, v100, v12\n\t" \
    "v_mul_f32 v101, v101, v13\n\t" \
    "v_mul_f32 v102, v102, v14\n\t" \
    "v_mul_f32 v103, v103, v15\n\t" \
    "v_cvt_pk_bf16_f32 v94, v100, v101\n\t" \
    "v_cvt_pk_bf16_f32 v95, v102, v103\n\t" \
    "v_mfma_f32_16x16x32_bf16 v[108:111], v[20:23], v[188:191], v[108:111]\n\t" \
    "s_add_u32 s17, s16, 4096\n\t" \
    "ds_read_b64_tr_b16 v[50:51], v69 offset:50176\n\t" \
    "s_waitcnt lgkmcnt(10)\n\t" \
    "v_mfma_f32_16x16x32_bf16 v[104:107], v[24:27], v[192:195], v[104:107]\n\t" \
    "s_add_u32 s18, s16, 8192\n\t" \
    "ds_read_b64_tr_b16 v[52:53], v69 offset:57344\n\t" \
    "buffer_store_dwordx4 v[92:95], v2, s[8:11], s65 offen offset:1024 sc1\n\t" \
    "v_mfma_f32_16x16x32_bf16 v[108:111], v[24:27], v[196:199], v[108:111]\n\t" \
    "s_add_u32 s19, s16, 12288\n\t" \
    "ds_read_b64_tr_b16 v[54:55], v69 offset:58368\n\t" \
    "v_mfma_f32_16x16x32_bf16 v[104:107], v[28:31], v[200:203], v[104:107]\n\t" \
    "s_add_u32 s20, s16, 16384\n\t" \
    "ds_read_b64_tr_b16 v[16:17], v63 offset:0\n\t" \
    "v_mfma_f32_16x16x32_bf16 v[108:111], v[28:31], v[204:207], v[108:111]\n\t" \
    "s_add_u32 s21, s16, 20480\n\t" \
    "ds_read_b64_tr_b16 v[18:19], v63 offset:1024\n\t" \
    "s_waitcnt lgkmcnt(10)\n\t" \
    "v_mfma_f32_16x16x32_bf16 v[104:107], v[32:35], v[208:211], v[104:107]\n\t" \
    "s_add_u32 s22, s16, 24576\n\t" \
    "ds_read_b64_tr_b16 v[20:21], v63 offset:8192\n\t" \
    "v_mfma_f32_16x16x32_bf16 v[108:111], v[32:35], v[212:215], v[108:111]\n\t" \
    "s_add_u32 s23, s16, 28672\n\t" \
    "ds_read_b64_tr_b16 v[22:23], v63 offset:9216\n\t" \
    "v_mfma_f32_16x16x32_bf16 v[104:107], v[36:39], v[216:219], v[104:107]\n\t" \
    "s_add_u32 s24, s16, 32768\n\t" \
    "ds_read_b64_tr_b16 v[24:25], v63 offset:16384\n\t" \
    "v_mfma_f32_16x16x32_bf16 v[108:111], v[36:39], v[220:223], v[108:111]\n\t" \
    "s_add_u32 s25, s16, 36864\n\t" \
    "ds_read_b64_tr_b16 v[26:27], v63 offset:17408\n\t" \
    "s_waitcnt lgkmcnt(10)\n\t" \
    "v_mfma_f32_16x16x32_bf16 v[104:107], v[40:43], v[224:227], v[104:107]\n\t" \
    "s_add_u32 s26, s16, 40960\n\t" \
    "ds_read_b64_tr_b16 v[28:29], v63 offset:24576\n\t" \
    "v_mfma_f32_16x16x32_bf16 v[108:111], v[40:43], v[228:231], v[108:111]\n\t" \
    "s_add_u32 s27, s16, 45056\n\t" \
    "ds_read_b64_tr_b16 v[30:31], v63 offset:25600\n\t" \
    "v_mfma_f32_16x16x32_bf16 v[104:107], v[44:47], v[232:235], v[104:107]\n\t" \
    "s_add_u32 s28, s16, 49152\n\t" \
    "ds_read_b64_tr_b16 v[32:33], v63 offset:32768\n\t" \
    "v_mfma_f32_16x16x32_bf16 v[108:111], v[44:47], v[236:239], v[108:111]\n\t" \
    "s_add_u32 s29, s16, 53248\n\t" \
    "ds_read_b64_tr_b16 v[34:35], v63 offset:33792\n\t" \
    "s_waitcnt lgkmcnt(10)\n\t" \
    "v_mfma_f32_16x16x32_bf16 v[104:107], v[48:51], v[240:243], v[104:107]\n\t" \
    "v_and_b32 v58, 3, v1\n\t" \
    "ds_read_b64_tr_b16 v[36:37], v63 offset:40960\n\t" \
    "v_mfma_f32_16x16x32_bf16 v[108:111], v[48:51], v[244:247], v[108:111]\n\t" \
    "v_lshlrev_b32 v58, 7, v58\n\t" \
    "ds_read_b64_tr_b16 v[38:39], v63 offset:41984\n\t" \
    "v_mfma_f32_16x16x32_bf16 v[104:107], v[52:55], v[248:251], v[104:107]\n\t" \
    "s_lshl_b32 s82, s60, 8\n\t" \
    "ds_read_b64_tr_b16 v[40:41], v63 offset:49152\n\t" \
    "v_mfma_f32_16x16x32_bf16 v[108:111], v[52:55], v[252:255], v[108:111]\n\t" \
    "s_add_u32 s82, s82, 202752\n\t" \
    "ds_read_b64_tr_b16 v[42:43], v63 offset:50176\n\t" \
    "s_waitcnt lgkmcnt(10)\n\t" \
    "v_mfma_f32_16x16x32_bf16 v[112:115], v[16:19], v[128:131], 0\n\t" \
    "s_lshl_b32 s94, s61, 16\n\t" \
    "ds_read_b64_tr_b16 v[44:45], v63 offset:57344\n\t" \
    "v_mfma_f32_16x16x32_bf16 v[116:119], v[16:19], v[132:135], 0\n\t" \
    "s_lshl_b32 s89, s50, 14\n\t" \
    "ds_read_b64_tr_b16 v[46:47], v63 offset:58368\n\t" \
    "v_mfma_f32_16x16x32_bf16 v[112:115], v[20:23], v[136:139], v[112:115]\n\t" \
    "s_add_u32 s94, s94, s89\n\t" \
    "ds_read_b64_tr_b16 v[48:49], v70 offset:0\n\t" \
    "v_mfma_f32_16x16x32_bf16 v[116:119], v[20:23], v[140:143], v[116:119]\n\t" \
    "s_lshl_b32 s89, s60, 7\n\t" \
    "ds_read_b64_tr_b16 v[50:51], v70 offset:1024\n\t" \
    "v_exp_f32 v12, -v104\n\t" \
    "v_exp_f32 v13, -v105\n\t" \
    "v_exp_f32 v14, -v106\n\t" \
    "v_exp_f32 v15, -v107\n\t" \
    "v_add_f32 v12, 1.0, v12\n\t" \
    "v_add_f32 v13, 1.0, v13\n\t" \
    "v_add_f32 v14, 1.0, v14\n\t" \
    "v_add_f32 v15, 1.0, v15\n\t" \
    "s_waitcnt lgkmcnt(10)\n\t" \
    "v_mfma_f32_16x16x32_bf16 v[112:115], v[24:27], v[144:147], v[112:115]\n\t" \
    "s_add_u32 s94, s94, s89\n\t" \
    "ds_read_b64_tr_b16 v[52:53], v70 offset:8192\n\t" \
    "v_rcp_f32 v12, v12\n\t" \
    "v_rcp_f32 v13, v13\n\t" \
    "v_rcp_f32 v14, v14\n\t" \
    "v_rcp_f32 v15, v15\n\t" \
    "v_mul_f32 v104, v104, v12\n\t" \
    "v_mul_f32 v105, v105, v13\n\t" \
    "v_mul_f32 v106, v106, v14\n\t" \
    "v_mul_f32 v107, v107, v15\n\t" \
    "v_mfma_f32_16x16x32_bf16 v[116:119], v[24:27], v[148:151], v[116:119]\n\t" \
    "s_add_u32 s81, s91, 0x8000\n\t" \
    "ds_read_b64_tr_b16 v[54:55], v70 offset:9216\n\t" \
    "v_cvt_pk_bf16_f32 v104, v104, v105\n\t" \
    "v_cvt_pk_bf16_f32 v105, v106, v107\n\t" \
    "v_exp_f32 v12, -v108\n\t" \
    "v_exp_f32 v13, -v109\n\t" \
    "v_exp_f32 v14, -v110\n\t" \
    "v_exp_f32 v15, -v111\n\t" \
    "v_add_f32 v12, 1.0, v12\n\t" \
    "v_add_f32 v13, 1.0, v13\n\t" \
    "v_mfma_f32_16x16x32_bf16 v[112:115], v[28:31], v[152:155], v[112:115]\n\t" \
    "s_lshl_b32 s89, s50, 10\n\t" \
    "ds_read_b64_tr_b16 v[16:17], v70 offset:16384\n\t" \
    "v_add_f32 v14, 1.0, v14\n\t" \
    "v_add_f32 v15, 1.0, v15\n\t" \
    "v_rcp_f32 v12, v12\n\t" \
    "v_rcp_f32 v13, v13\n\t" \
    "v_rcp_f32 v14, v14\n\t" \
    "v_rcp_f32 v15, v15\n\t" \
    "v_mul_f32 v108, v108, v12\n\t" \
    "v_mul_f32 v109, v109, v13\n\t" \
    "v_mfma_f32_16x16x32_bf16 v[116:119], v[28:31], v[156:159], v[116:119]\n\t" \
    "v_add_u32 v60, s89, v2\n\t" \
    "ds_read_b64_tr_b16 v[18:19], v70 offset:17408\n\t" \
    "v_mul_f32 v110, v110, v14\n\t" \
    "v_mul_f32 v111, v111, v15\n\t" \
    "v_cvt_pk_bf16_f32 v108, v108, v109\n\t" \
    "v_cvt_pk_bf16_f32 v109, v110, v111\n\t" \
    "s_waitcnt lgkmcnt(10)\n\t" \
    "v_mfma_f32_16x16x32_bf16 v[112:115], v[32:35], v[160:163], v[112:115]\n\t" \
    "ds_read_b64_tr_b16 v[20:21], v70 offset:24576\n\t" \
    "v_mfma_f32_16x16x32_bf16 v[116:119], v[32:35], v[164:167], v[116:119]\n\t" \
    "ds_read_b64_tr_b16 v[22:23], v70 offset:25600\n\t" \
    "v_mfma_f32_16x16x32_bf16 v[112:115], v[36:39], v[168:171], v[112:115]\n\t" \
    "ds_read_b64_tr_b16 v[24:25], v70 offset:32768\n\t" \
    "v_mfma_f32_16x16x32_bf16 v[116:119], v[36:39], v[172:175], v[116:119]\n\t" \
    "ds_read_b64_tr_b16 v[26:27], v70 offset:33792\n\t" \
    "s_waitcnt lgkmcnt(10)\n\t" \
    "v_mfma_f32_16x16x32_bf16 v[112:115], v[40:43], v[176:179], v[112:115]\n\t" \
    "ds_read_b64_tr_b16 v[28:29], v70 offset:40960\n\t" \
    "v_mfma_f32_16x16x32_bf16 v[116:119], v[40:43], v[180:183], v[116:119]\n\t" \
    "ds_read_b64_tr_b16 v[30:31], v70 offset:41984\n\t" \
    "v_mfma_f32_16x16x32_bf16 v[112:115], v[44:47], v[184:187], v[112:115]\n\t" \
    "ds_read_b64_tr_b16 v[32:33], v70 offset:49152\n\t" \
    "v_mfma_f32_16x16x32_bf16 v[116:119], v[44:47], v[188:191], v[116:119]\n\t" \
    "ds_read_b64_tr_b16 v[34:35], v70 offset:50176\n\t" \
    "s_waitcnt lgkmcnt(10)\n\t" \
    "v_mfma_f32_16x16x32_bf16 v[112:115], v[48:51], v[192:195], v[112:115]\n\t" \
    "ds_read_b64_tr_b16 v[36:37], v70 offset:57344\n\t" \
    "v_mfma_f32_16x16x32_bf16 v[116:119], v[48:51], v[196:199], v[116:119]\n\t" \
    "ds_read_b64_tr_b16 v[38:39], v70 offset:58368\n\t" \
    "v_mfma_f32_16x16x32_bf16 v[112:115], v[52:55], v[200:203], v[112:115]\n\t" \
    "ds_read_b64_tr_b16 v[40:41], v64 offset:0\n\t" \
    "v_mfma_f32_16x16x32_bf16 v[116:119], v[52:55], v[204:207], v[116:119]\n\t" \
    "ds_read_b64_tr_b16 v[42:43], v64 offset:1024\n\t" \
    "s_waitcnt lgkmcnt(10)\n\t" \
    "v_mfma_f32_16x16x32_bf16 v[112:115], v[16:19], v[208:211], v[112:115]\n\t" \
    "ds_read_b64_tr_b16 v[44:45], v64 offset:8192\n\t" \
    "v_mfma_f32_16x16x32_bf16 v[116:119], v[16:19], v[212:215], v[116:119]\n\t" \
    "ds_read_b64_tr_b16 v[46:47], v64 offset:9216\n\t" \
    "v_mfma_f32_16x16x32_bf16 v[112:115], v[20:23], v[216:219], v[112:115]\n\t" \
    "ds_read_b64_tr_b16 v[48:49], v64 offset:16384\n\t" \
    "v_mfma_f32_16x16x32_bf16 v[116:119], v[20:23], v[220:223], v[116:119]\n\t" \
    "ds_read_b64_tr_b16 v[50:51], v64 offset:17408\n\t" \
    "s_waitcnt lgkmcnt(10)\n\t" \
    "v_mfma_f32_16x16x32_bf16 v[112:115], v[24:27], v[224:227], v[112:115]\n\t" \
    "ds_read_b64_tr_b16 v[52:53], v64 offset:24576\n\t" \
    "v_mfma_f32_16x16x32_bf16 v[116:119], v[24:27], v[228:231], v[116:119]\n\t" \
    "ds_read_b64_tr_b16 v[54:55], v64 offset:25600\n\t" \
    "v_mfma_f32_16x16x32_bf16 v[112:115], v[28:31], v[232:235], v[112:115]\n\t" \
    "ds_read_b64_tr_b16 v[16:17], v64 offset:32768\n\t" \
    "v_mfma_f32_16x16x32_bf16 v[116:119], v[28:31], v[236:239], v[116:119]\n\t" \
    "ds_read_b64_tr_b16 v[18:19], v64 offset:33792\n\t" \
    "s_waitcnt lgkmcnt(10)\n\t" \
    "v_mfma_f32_16x16x32_bf16 v[112:115], v[32:35], v[240:243], v[112:115]\n\t" \
    "ds_read_b64_tr_b16 v[20:21], v64 offset:40960\n\t" \
    "v_mfma_f32_16x16x32_bf16 v[116:119], v[32:35], v[244:247], v[116:119]\n\t" \
    "ds_read_b64_tr_b16 v[22:23], v64 offset:41984\n\t" \
    "s_waitcnt vmcnt(0)\n\t" \
    "s_mov_b64 exec, 1\n\t" \
    "ds_add_rtn_u32 v56, v3, v57 offset:0\n\t" \
    "s_mov_b64 exec, -1\n\t" \
    "v_mfma_f32_16x16x32_bf16 v[112:115], v[36:39], v[248:251], v[112:115]\n\t" \
    "ds_read_b64_tr_b16 v[24:25], v64 offset:49152\n\t" \
    "v_mfma_f32_16x16x32_bf16 v[116:119], v[36:39], v[252:255], v[116:119]\n\t" \
    "ds_read_b64_tr_b16 v[26:27], v64 offset:50176\n\t" \
    "s_waitcnt lgkmcnt(11)\n\t" \
    "v_mfma_f32_16x16x32_bf16 v[120:123], v[40:43], v[128:131], 0\n\t" \
    "ds_read_b64_tr_b16 v[28:29], v64 offset:57344\n\t" \
    "v_mfma_f32_16x16x32_bf16 v[124:127], v[40:43], v[132:135], 0\n\t" \
    "ds_read_b64_tr_b16 v[30:31], v64 offset:58368\n\t" \
    "v_mfma_f32_16x16x32_bf16 v[120:123], v[44:47], v[136:139], v[120:123]\n\t" \
    "ds_read_b64_tr_b16 v[32:33], v71 offset:0\n\t" \
    "v_mfma_f32_16x16x32_bf16 v[124:127], v[44:47], v[140:143], v[124:127]\n\t" \
    "ds_read_b64_tr_b16 v[34:35], v71 offset:1024\n\t" \
    "v_exp_f32 v12, -v112\n\t" \
    "v_exp_f32 v13, -v113\n\t" \
    "v_exp_f32 v14, -v114\n\t" \
    "v_exp_f32 v15, -v115\n\t" \
    "v_add_f32 v12, 1.0, v12\n\t" \
    "v_add_f32 v13, 1.0, v13\n\t" \
    "v_add_f32 v14, 1.0, v14\n\t" \
    "v_add_f32 v15, 1.0, v15\n\t" \
    "s_waitcnt lgkmcnt(6)\n\t" \
    "v_cmp_eq_u32 vcc, 3, v56\n\t" \
    "s_nop 0\n\t" \
    "s_bitcmp1_b32 vcc_lo, 0\n\t" \
    "s_cselect_b64 exec, 0xff, 0\n\t" \
    "buffer_store_dwordx4 a[252:255], v2, s[8:11], s78 offen sc1\n\t" \
    "s_mov_b64 exec, -1\n\t" \
    "v_mfma_f32_16x16x32_bf16 v[120:123], v[48:51], v[144:147], v[120:123]\n\t" \
    "ds_read_b64_tr_b16 v[36:37], v71 offset:8192\n\t" \
    "v_rcp_f32 v12, v12\n\t" \
    "v_rcp_f32 v13, v13\n\t" \
    "v_rcp_f32 v14, v14\n\t" \
    "v_rcp_f32 v15, v15\n\t" \
    "v_mul_f32 v112, v112, v12\n\t" \
    "v_mul_f32 v113, v113, v13\n\t" \
    "v_mul_f32 v114, v114, v14\n\t" \
    "v_mul_f32 v115, v115, v15\n\t" \
    "v_mfma_f32_16x16x32_bf16 v[124:127], v[48:51], v[148:151], v[124:127]\n\t" \
    "ds_read_b64_tr_b16 v[38:39], v71 offset:9216\n\t" \
    "v_cvt_pk_bf16_f32 v106, v112, v113\n\t" \
    "v_cvt_pk_bf16_f32 v107, v114, v115\n\t" \
    "v_exp_f32 v12, -v116\n\t" \
    "v_exp_f32 v13, -v117\n\t" \
    "v_exp_f32 v14, -v118\n\t" \
    "v_exp_f32 v15, -v119\n\t" \
    "v_add_f32 v12, 1.0, v12\n\t" \
    "v_add_f32 v13, 1.0, v13\n\t" \
    "v_mfma_f32_16x16x32_bf16 v[120:123], v[52:55], v[152:155], v[120:123]\n\t" \
    "ds_read_b64_tr_b16 v[40:41], v71 offset:16384\n\t" \
    "buffer_store_dwordx4 v[104:107], v2, s[8:11], s66 offen offset:0 sc1\n\t" \
    "v_add_f32 v14, 1.0, v14\n\t" \
    "v_add_f32 v15, 1.0, v15\n\t" \
    "v_rcp_f32 v12, v12\n\t" \
    "v_rcp_f32 v13, v13\n\t" \
    "v_rcp_f32 v14, v14\n\t" \
    "v_rcp_f32 v15, v15\n\t" \
    "v_mul_f32 v116, v116, v12\n\t" \
    "v_mul_f32 v117, v117, v13\n\t" \
    "v_mfma_f32_16x16x32_bf16 v[124:127], v[52:55], v[156:159], v[124:127]\n\t" \
    "ds_read_b64_tr_b16 v[42:43], v71 offset:17408\n\t" \
    "v_mul_f32 v118, v118, v14\n\t" \
    "v_mul_f32 v119, v119, v15\n\t" \
    "v_cvt_pk_bf16_f32 v110, v116, v117\n\t" \
    "v_cvt_pk_bf16_f32 v111, v118, v119\n\t" \
    "v_mfma_f32_16x16x32_bf16 v[120:123], v[16:19], v[160:163], v[120:123]\n\t" \
    "ds_read_b64_tr_b16 v[44:45], v71 offset:24576\n\t" \
    "buffer_store_dwordx4 v[108:111], v2, s[8:11], s66 offen offset:1024 sc1\n\t" \
    "v_mfma_f32_16x16x32_bf16 v[124:127], v[16:19], v[164:167], v[124:127]\n\t" \
    "ds_read_b64_tr_b16 v[46:47], v71 offset:25600\n\t" \
    "v_mfma_f32_16x16x32_bf16 v[120:123], v[20:23], v[168:171], v[120:123]\n\t" \
    "ds_read_b64_tr_b16 v[48:49], v71 offset:32768\n\t" \
    "v_mfma_f32_16x16x32_bf16 v[124:127], v[20:23], v[172:175], v[124:127]\n\t" \
    "ds_read_b64_tr_b16 v[50:51], v71 offset:33792\n\t" \
    "s_waitcnt lgkmcnt(10)\n\t" \
    "v_mfma_f32_16x16x32_bf16 v[120:123], v[24:27], v[176:179], v[120:123]\n\t" \
    "ds_read_b64_tr_b16 v[52:53], v71 offset:40960\n\t" \
    "v_mfma_f32_16x16x32_bf16 v[124:127], v[24:27], v[180:183], v[124:127]\n\t" \
    "ds_read_b64_tr_b16 v[54:55], v71 offset:41984\n\t" \
    "v_mfma_f32_16x16x32_bf16 v[120:123], v[28:31], v[184:187], v[120:123]\n\t" \
    "ds_read_b64_tr_b16 v[16:17], v71 offset:49152\n\t" \
    "v_mfma_f32_16x16x32_bf16 v[124:127], v[28:31], v[188:191], v[124:127]\n\t" \
    "ds_read_b64_tr_b16 v[18:19], v71 offset:50176\n\t" \
    "s_waitcnt lgkmcnt(10)\n\t" \
    "v_mfma_f32_16x16x32_bf16 v[120:123], v[32:35], v[192:195], v[120:123]\n\t" \
    "ds_read_b64_tr_b16 v[20:21], v71 offset:57344\n\t" \
    "v_mfma_f32_16x16x32_bf16 v[124:127], v[32:35], v[196:199], v[124:127]\n\t" \
    "ds_read_b64_tr_b16 v[22:23], v71 offset:58368\n\t" \
    "v_mfma_f32_16x16x32_bf16 v[120:123], v[36:39], v[200:203], v[120:123]\n\t" \
    "v_mfma_f32_16x16x32_bf16 v[124:127], v[36:39], v[204:207], v[124:127]\n\t" \
    "s_waitcnt lgkmcnt(8)\n\t" \
    "v_mfma_f32_16x16x32_bf16 v[120:123], v[40:43], v[208:211], v[120:123]\n\t" \
    "v_mfma_f32_16x16x32_bf16 v[124:127], v[40:43], v[212:215], v[124:127]\n\t" \
    "v_mfma_f32_16x16x32_bf16 v[120:123], v[44:47], v[216:219], v[120:123]\n\t" \
    "v_mfma_f32_16x16x32_bf16 v[124:127], v[44:47], v[220:223], v[124:127]\n\t" \
    "s_waitcnt lgkmcnt(4)\n\t" \
    "v_mfma_f32_16x16x32_bf16 v[120:123], v[48:51], v[224:227], v[120:123]\n\t" \
    "v_mfma_f32_16x16x32_bf16 v[124:127], v[48:51], v[228:231], v[124:127]\n\t" \
    "v_mfma_f32_16x16x32_bf16 v[120:123], v[52:55], v[232:235], v[120:123]\n\t" \
    "v_mfma_f32_16x16x32_bf16 v[124:127], v[52:55], v[236:239], v[124:127]\n\t" \
    "s_waitcnt lgkmcnt(0)\n\t" \
    "v_mfma_f32_16x16x32_bf16 v[120:123], v[16:19], v[240:243], v[120:123]\n\t" \
    "v_mfma_f32_16x16x32_bf16 v[124:127], v[16:19], v[244:247], v[124:127]\n\t" \
    "v_mfma_f32_16x16x32_bf16 v[120:123], v[20:23], v[248:251], v[120:123]\n\t" \
    "v_mfma_f32_16x16x32_bf16 v[124:127], v[20:23], v[252:255], v[124:127]\n\t" \
      \
    "s_nop 6\n\t" \
    "v_exp_f32 v12, -v120\n\t" \
    "v_exp_f32 v13, -v121\n\t" \
    "v_exp_f32 v14, -v122\n\t" \
    "v_exp_f32 v15, -v123\n\t" \
    "v_add_f32 v12, 1.0, v12\n\t" \
    "v_add_f32 v13, 1.0, v13\n\t" \
    "v_add_f32 v14, 1.0, v14\n\t" \
    "v_add_f32 v15, 1.0, v15\n\t" \
    "v_rcp_f32 v12, v12\n\t" \
    "v_rcp_f32 v13, v13\n\t" \
    "v_rcp_f32 v14, v14\n\t" \
    "v_rcp_f32 v15, v15\n\t" \
    "v_mul_f32 v120, v120, v12\n\t" \
    "v_mul_f32 v121, v121, v13\n\t" \
    "v_mul_f32 v122, v122, v14\n\t" \
    "v_mul_f32 v123, v123, v15\n\t" \
    "v_cvt_pk_bf16_f32 v120, v120, v121\n\t" \
    "v_cvt_pk_bf16_f32 v121, v122, v123\n\t" \
    "v_exp_f32 v12, -v124\n\t" \
    "v_exp_f32 v13, -v125\n\t" \
    "v_exp_f32 v14, -v126\n\t" \
    "v_exp_f32 v15, -v127\n\t" \
    "v_add_f32 v12, 1.0, v12\n\t" \
    "v_add_f32 v13, 1.0, v13\n\t" \
    "v_add_f32 v14, 1.0, v14\n\t" \
    "v_add_f32 v15, 1.0, v15\n\t" \
    "v_rcp_f32 v12, v12\n\t" \
    "v_rcp_f32 v13, v13\n\t" \
    "v_rcp_f32 v14, v14\n\t" \
    "v_rcp_f32 v15, v15\n\t" \
    "v_mul_f32 v124, v124, v12\n\t" \
    "v_mul_f32 v125, v125, v13\n\t" \
    "v_mul_f32 v126, v126, v14\n\t" \
    "v_mul_f32 v127, v127, v15\n\t" \
    "v_cvt_pk_bf16_f32 v122, v124, v125\n\t" \
    "v_cvt_pk_bf16_f32 v123, v126, v127\n\t" \
    "buffer_load_dwordx2 v[62:63], v58, s[8:11], s91 offen sc1\n\t" \
    "buffer_store_dwordx4 v[120:123], v2, s[8:11], s95 offen sc1\n\t" \
    "s_add_u32 s78, s78, 0x8000\n\t" \
    "v_xor_b32 v92, 0xc00, v60\n\t" \
    "v_xor_b32 v93, 0x800, v60\n\t" \
    "v_xor_b32 v94, 0x400, v60\n\t" \
    "v_xor_b32 v95, 0x0, v60\n\t" \
    "s_waitcnt vmcnt(1)\n\t" \
    "v_cmp_ne_u64 vcc, s[54:55], v[62:63]\n\t" \
    "s_nop 0\n\t" \
    "s_cbranch_vccz .Lpd_%=_ok\n\t" \
    "buffer_load_dwordx2 v[62:63], v58, s[8:11], s91 offen sc1\n\t" \
    "s_waitcnt vmcnt(0)\n\t" \
    "s_mov_b64 exec, 1\n\t" \
    "ds_add_rtn_u32 v56, v3, v57 offset:4\n\t" \
    "s_mov_b64 exec, -1\n\t" \
    "s_waitcnt lgkmcnt(0)\n\t" \
    "v_cmp_eq_u32 vcc, 3, v56\n\t" \
    "s_nop 0\n\t" \
    "s_bitcmp1_b32 vcc_lo, 0\n\t" \
    "s_cselect_b64 exec, 0xff, 0\n\t" \
    "buffer_store_dwordx4 a[252:255], v2, s[8:11], s78 offen sc1\n\t" \
    "s_mov_b64 exec, -1\n\t" \
    "v_mov_b32 v57, 0\n\t" \
    "v_cmp_ne_u64 vcc, s[54:55], v[62:63]\n\t" \
    "s_nop 0\n\t" \
    "s_cbranch_vccz .Lpd_%=_ok\n\t" \
    ".Lpd_%=:\n\t" \
    "buffer_load_dwordx2 v[62:63], v58, s[8:11], s91 offen sc1\n\t" \
    "s_waitcnt vmcnt(0)\n\t" \
    "v_cmp_ne_u64 vcc, s[54:55], v[62:63]\n\t" \
    "s_nop 0\n\t" \
    "s_cbranch_vccz .Lpd_%=_ok\n\t" \
    "s_sub_u32 s62, s62, 1\n\t" \
    "s_cmp_lt_i32 s62, 0\n\t" \
    "s_cbranch_scc1 .Lpd_%=_ok\n\t" \
    "s_sleep 2\n\t" \
    "s_branch .Lpd_%=\n\t" \
    ".Lpd_%=_ok:\n\t" \
    "buffer_load_dwordx4 v[128:131], v92, s[8:11], s16 offen sc1\n\t" \
    "buffer_load_dwordx4 v[132:135], v93, s[8:11], s16 offen sc1\n\t" \
    "buffer_load_dwordx4 v[136:139], v94, s[8:11], s16 offen sc1\n\t" \
    "buffer_load_dwordx4 v[140:143], v95, s[8:11], s16 offen sc1\n\t" \
    "buffer_load_dwordx4 v[144:147], v92, s[8:11], s17 offen sc1\n\t" \
    "buffer_load_dwordx4 v[148:151], v93, s[8:11], s17 offen sc1\n\t" \
    "buffer_load_dwordx4 v[152:155], v94, s[8:11], s17 offen sc1\n\t" \
    "buffer_load_dwordx4 v[156:159], v95, s[8:11], s17 offen sc1\n\t" \
    "s_waitcnt vmcnt(8)\n\t" \
    "s_mov_b64 exec, 1\n\t" \
    "ds_add_rtn_u32 v56, v3, v57 offset:4\n\t" \
    "s_mov_b64 exec, -1\n\t" \
    "buffer_load_dwordx4 v[160:163], v92, s[8:11], s20 offen sc1\n\t" \
    "buffer_load_dwordx4 v[164:167], v93, s[8:11], s20 offen sc1\n\t" \
    "buffer_load_dwordx4 v[168:171], v94, s[8:11], s20 offen sc1\n\t" \
    "buffer_load_dwordx4 v[172:175], v95, s[8:11], s20 offen sc1\n\t" \
    "s_waitcnt lgkmcnt(0)\n\t" \
    "v_mul_u32_u24 v56, v56, v57\n\t" \
    "v_cmp_eq_u32 vcc, 3, v56\n\t" \
    "s_nop 0\n\t" \
    "s_bitcmp1_b32 vcc_lo, 0\n\t" \
    "s_cselect_b64 exec, 0xff, 0\n\t" \
    "buffer_store_dwordx4 a[252:255], v2, s[8:11], s78 offen sc1\n\t" \
    "s_mov_b64 exec, -1\n\t" \
    "v_mov_b32 v88, v58\n\t" \
    "v_lshrrev_b32 v90, 4, v1\n\t" \
    "v_and_b32 v91, 7, v1\n\t" \
    "v_lshlrev_b32 v91, 10, v91\n\t" \
    "v_lshl_add_u32 v91, v90, 4, v91\n\t" \
    "v_and_b32 v89, 8, v1\n\t" \
    "v_lshl_add_u32 v91, v89, 3, v91\n\t" \
    "v_lshlrev_b32 v90, 5, v90\n\t" \
    "s_mul_i32 s88, s50, 0x5000\n\t" \
    "v_add_u32 v89, s88, v2\n\t" \
    "s_bitcmp1_b32 s50, 0\n\t" \
    "s_cselect_b64 s[84:85], -1, 0\n\t" \
    "s_waitcnt vmcnt(11)\n\t" \
    "v_mfma_f32_16x16x32_bf16 v[16:19], a[0:3], v[128:131], 0\n\t" \
    "buffer_load_dwordx4 v[176:179], v92, s[8:11], s21 offen sc1\n\t" \
    "v_mfma_f32_16x16x32_bf16 v[20:23], a[4:7], v[128:131], 0\n\t" \
    "v_mfma_f32_16x16x32_bf16 v[24:27], a[8:11], v[128:131], 0\n\t" \
    "v_mfma_f32_16x16x32_bf16 v[28:31], a[12:15], v[128:131], 0\n\t" \
    "s_waitcnt vmcnt(11)\n\t" \
    "v_mfma_f32_16x16x32_bf16 v[32:35], a[0:3], v[132:135], 0\n\t" \
    "buffer_load_dwordx4 v[180:183], v93, s[8:11], s21 offen sc1\n\t" \
    "v_mfma_f32_16x16x32_bf16 v[36:39], a[4:7], v[132:135], 0\n\t" \
    "v_mfma_f32_16x16x32_bf16 v[40:43], a[8:11], v[132:135], 0\n\t" \
    "v_mfma_f32_16x16x32_bf16 v[44:47], a[12:15], v[132:135], 0\n\t" \
    "s_waitcnt vmcnt(11)\n\t" \
    "v_mfma_f32_16x16x32_bf16 v[48:51], a[0:3], v[136:139], 0\n\t" \
    "buffer_load_dwordx4 v[184:187], v94, s[8:11], s21 offen sc1\n\t" \
    "v_mfma_f32_16x16x32_bf16 v[52:55], a[4:7], v[136:139], 0\n\t" \
    "v_mfma_f32_16x16x32_bf16 v[56:59], a[8:11], v[136:139], 0\n\t" \
    "v_mfma_f32_16x16x32_bf16 v[60:63], a[12:15], v[136:139], 0\n\t" \
    "s_waitcnt vmcnt(11)\n\t" \
    "v_mfma_f32_16x16x32_bf16 v[64:67], a[0:3], v[140:143], 0\n\t" \
    "buffer_load_dwordx4 v[188:191], v95, s[8:11], s21 offen sc1\n\t" \
    "v_mfma_f32_16x16x32_bf16 v[68:71], a[4:7], v[140:143], 0\n\t" \
    "v_mfma_f32_16x16x32_bf16 v[72:75], a[8:11], v[140:143], 0\n\t" \
    "v_mfma_f32_16x16x32_bf16 v[76:79], a[12:15], v[140:143], 0\n\t" \
    "s_waitcnt vmcnt(11)\n\t" \
    "v_mfma_f32_16x16x32_bf16 v[16:19], a[16:19], v[144:147], v[16:19]\n\t" \
    "buffer_load_dwordx4 v[192:195], v92, s[8:11], s23 offen sc1\n\t" \
    "v_mfma_f32_16x16x32_bf16 v[20:23], a[20:23], v[144:147], v[20:23]\n\t" \
    "v_mfma_f32_16x16x32_bf16 v[24:27], a[24:27], v[144:147], v[24:27]\n\t" \
    "v_mfma_f32_16x16x32_bf16 v[28:31], a[28:31], v[144:147], v[28:31]\n\t" \
    "s_waitcnt vmcnt(11)\n\t" \
    "v_mfma_f32_16x16x32_bf16 v[32:35], a[16:19], v[148:151], v[32:35]\n\t" \
    "buffer_load_dwordx4 v[196:199], v93, s[8:11], s23 offen sc1\n\t" \
    "v_mfma_f32_16x16x32_bf16 v[36:39], a[20:23], v[148:151], v[36:39]\n\t" \
    "v_mfma_f32_16x16x32_bf16 v[40:43], a[24:27], v[148:151], v[40:43]\n\t" \
    "v_mfma_f32_16x16x32_bf16 v[44:47], a[28:31], v[148:151], v[44:47]\n\t" \
    "s_waitcnt vmcnt(11)\n\t" \
    "v_mfma_f32_16x16x32_bf16 v[48:51], a[16:19], v[152:155], v[48:51]\n\t" \
    "buffer_load_dwordx4 v[200:203], v94, s[8:11], s23 offen sc1\n\t" \
    "v_mfma_f32_16x16x32_bf16 v[52:55], a[20:23], v[152:155], v[52:55]\n\t" \
    "v_mfma_f32_16x16x32_bf16 v[56:59], a[24:27], v[152:155], v[56:59]\n\t" \
    "v_mfma_f32_16x16x32_bf16 v[60:63], a[28:31], v[152:155], v[60:63]\n\t" \
    "s_waitcnt vmcnt(11)\n\t" \
    "v_mfma_f32_16x16x32_bf16 v[64:67], a[16:19], v[156:159], v[64:67]\n\t" \
    "buffer_load_dwordx4 v[204:207], v95, s[8:11], s23 offen sc1\n\t" \
    "v_mfma_f32_16x16x32_bf16 v[68:71], a[20:23], v[156:159], v[68:71]\n\t" \
    "v_mfma_f32_16x16x32_bf16 v[72:75], a[24:27], v[156:159], v[72:75]\n\t" \
    "v_mfma_f32_16x16x32_bf16 v[76:79], a[28:31], v[156:159], v[76:79]\n\t" \
    "s_waitcnt vmcnt(11)\n\t" \
    "v_mfma_f32_16x16x32_bf16 v[16:19], a[64:67], v[160:163], v[16:19]\n\t" \
    "buffer_load_dwordx4 v[208:211], v92, s[8:11], s24 offen sc1\n\t" \
    "v_mfma_f32_16x16x32_bf16 v[20:23], a[68:71], v[160:163], v[20:23]\n\t" \
    "v_mfma_f32_16x16x32_bf16 v[24:27], a[72:75], v[160:163], v[24:27]\n\t" \
    "v_mfma_f32_16x16x32_bf16 v[28:31], a[76:79], v[160:163], v[28:31]\n\t" \
    "s_waitcnt vmcnt(11)\n\t" \
    "v_mfma_f32_16x16x32_bf16 v[32:35], a[64:67], v[164:167], v[32:35]\n\t" \
    "buffer_load_dwordx4 v[212:215], v93, s[8:11], s24 offen sc1\n\t" \
    "v_mfma_f32_16x16x32_bf16 v[36:39], a[68:71], v[164:167], v[36:39]\n\t" \
    "v_mfma_f32_16x16x32_bf16 v[40:43], a[72:75], v[164:167], v[40:43]\n\t" \
    "v_mfma_f32_16x16x32_bf16 v[44:47], a[76:79], v[164:167], v[44:47]\n\t" \
    "s_waitcnt vmcnt(11)\n\t" \
    "v_mfma_f32_16x16x32_bf16 v[48:51], a[64:67], v[168:171], v[48:51]\n\t" \
    "buffer_load_dwordx4 v[216:219], v94, s[8:11], s24 offen sc1\n\t" \
    "v_mfma_f32_16x16x32_bf16 v[52:55], a[68:71], v[168:171], v[52:55]\n\t" \
    "v_mfma_f32_16x16x32_bf16 v[56:59], a[72:75], v[168:171], v[56:59]\n\t" \
    "v_mfma_f32_16x16x32_bf16 v[60:63], a[76:79], v[168:171], v[60:63]\n\t" \
    "s_waitcnt vmcnt(11)\n\t" \
    "v_mfma_f32_16x16x32_bf16 v[64:67], a[64:67], v[172:175], v[64:67]\n\t" \
    "buffer_load_dwordx4 v[220:223], v95, s[8:11], s24 offen sc1\n\t" \
    "v_mfma_f32_16x16x32_bf16 v[68:71], a[68:71], v[172:175], v[68:71]\n\t" \
    "v_mfma_f32_16x16x32_bf16 v[72:75], a[72:75], v[172:175], v[72:75]\n\t" \
    "v_mfma_f32_16x16x32_bf16 v[76:79], a[76:79], v[172:175], v[76:79]\n\t" \
    "buffer_load_dwordx2 v[12:13], v88, s[8:11], s81 offen sc1\n\t" \
    "s_waitcnt vmcnt(12)\n\t" \
    "v_mfma_f32_16x16x32_bf16 v[16:19], a[80:83], v[176:179], v[16:19]\n\t" \
    "buffer_load_dwordx4 v[224:227], v92, s[8:11], s27 offen sc1\n\t" \
    "v_mfma_f32_16x16x32_bf16 v[20:23], a[84:87], v[176:179], v[20:23]\n\t" \
    "v_mfma_f32_16x16x32_bf16 v[24:27], a[88:91], v[176:179], v[24:27]\n\t" \
    "v_mfma_f32_16x16x32_bf16 v[28:31], a[92:95], v[176:179], v[28:31]\n\t" \
    "s_waitcnt vmcnt(12)\n\t" \
    "v_mfma_f32_16x16x32_bf16 v[32:35], a[80:83], v[180:183], v[32:35]\n\t" \
    "buffer_load_dwordx4 v[228:231], v93, s[8:11], s27 offen sc1\n\t" \
    "v_mfma_f32_16x16x32_bf16 v[36:39], a[84:87], v[180:183], v[36:39]\n\t" \
    "v_mfma_f32_16x16x32_bf16 v[40:43], a[88:91], v[180:183], v[40:43]\n\t" \
    "v_mfma_f32_16x16x32_bf16 v[44:47], a[92:95], v[180:183], v[44:47]\n\t" \
    "s_waitcnt vmcnt(12)\n\t" \
    "v_mfma_f32_16x16x32_bf16 v[48:51], a[80:83], v[184:187], v[48:51]\n\t" \
    "buffer_load_dwordx4 v[232:235], v94, s[8:11], s27 offen sc1\n\t" \
    "v_mfma_f32_16x16x32_bf16 v[52:55], a[84:87], v[184:187], v[52:55]\n\t" \
    "v_mfma_f32_16x16x32_bf16 v[56:59], a[88:91], v[184:187], v[56:59]\n\t" \
    "v_mfma_f32_16x16x32_bf16 v[60:63], a[92:95], v[184:187], v[60:63]\n\t" \
    "s_waitcnt vmcnt(12)\n\t" \
    "v_mfma_f32_16x16x32_bf16 v[64:67], a[80:83], v[188:191], v[64:67]\n\t" \
    "buffer_load_dwordx4 v[236:239], v95, s[8:11], s27 offen sc1\n\t" \
    "v_mfma_f32_16x16x32_bf16 v[68:71], a[84:87], v[188:191], v[68:71]\n\t" \
    "v_mfma_f32_16x16x32_bf16 v[72:75], a[88:91], v[188:191], v[72:75]\n\t" \
    "v_mfma_f32_16x16x32_bf16 v[76:79], a[92:95], v[188:191], v[76:79]\n\t" \
    "s_waitcnt vmcnt(12)\n\t" \
    "v_mfma_f32_16x16x32_bf16 v[16:19], a[112:115], v[192:195], v[16:19]\n\t" \
    "buffer_load_dwordx4 v[240:243], v92, s[8:11], s28 offen sc1\n\t" \
    "v_mfma_f32_16x16x32_bf16 v[20:23], a[116:119], v[192:195], v[20:23]\n\t" \
    "v_mfma_f32_16x16x32_bf16 v[24:27], a[120:123], v[192:195], v[24:27]\n\t" \
    "v_mfma_f32_16x16x32_bf16 v[28:31], a[124:127], v[192:195], v[28:31]\n\t" \
    "s_waitcnt vmcnt(12)\n\t" \
    "v_mfma_f32_16x16x32_bf16 v[32:35], a[112:115], v[196:199], v[32:35]\n\t" \
    "buffer_load_dwordx4 v[244:247], v93, s[8:11], s28 offen sc1\n\t" \
    "v_mfma_f32_16x16x32_bf16 v[36:39], a[116:119], v[196:199], v[36:39]\n\t" \
    "v_mfma_f32_16x16x32_bf16 v[40:43], a[120:123], v[196:199], v[40:43]\n\t" \
    "v_mfma_f32_16x16x32_bf16 v[44:47], a[124:127], v[196:199], v[44:47]\n\t" \
    "s_waitcnt vmcnt(12)\n\t" \
    "v_mfma_f32_16x16x32_bf16 v[48:51], a[112:115], v[200:203], v[48:51]\n\t" \
    "buffer_load_dwordx4 v[248:251], v94, s[8:11], s28 offen sc1\n\t" \
    "v_mfma_f32_16x16x32_bf16 v[52:55], a[116:119], v[200:203], v[52:55]\n\t" \
    "v_mfma_f32_16x16x32_bf16 v[56:59], a[120:123], v[200:203], v[56:59]\n\t" \
    "v_mfma_f32_16x16x32_bf16 v[60:63], a[124:127], v[200:203], v[60:63]\n\t" \
    "s_waitcnt vmcnt(12)\n\t" \
    "v_mfma_f32_16x16x32_bf16 v[64:67], a[112:115], v[204:207], v[64:67]\n\t" \
    "buffer_load_dwordx4 v[252:255], v95, s[8:11], s28 offen sc1\n\t" \
    "v_mfma_f32_16x16x32_bf16 v[68:71], a[116:119], v[204:207], v[68:71]\n\t" \
    "v_mfma_f32_16x16x32_bf16 v[72:75], a[120:123], v[204:207], v[72:75]\n\t" \
    "v_mfma_f32_16x16x32_bf16 v[76:79], a[124:127], v[204:207], v[76:79]\n\t" \
    "s_waitcnt vmcnt(8)\n\t" \
    "v_cmp_ne_u64 vcc, s[54:55], v[12:13]\n\t" \
    "s_nop 0\n\t" \
    "s_cbranch_vccnz .Ld2_%=\n\t" \
    ".Ld2r_%=:\n\t" \
    "v_mfma_f32_16x16x32_bf16 v[16:19], a[128:131], v[208:211], v[16:19]\n\t" \
    "buffer_load_dwordx4 v[96:99], v92, s[8:11], s18 offen sc1\n\t" \
    "v_mfma_f32_16x16x32_bf16 v[20:23], a[132:135], v[208:211], v[20:23]\n\t" \
    "v_mfma_f32_16x16x32_bf16 v[24:27], a[136:139], v[208:211], v[24:27]\n\t" \
    "v_mfma_f32_16x16x32_bf16 v[28:31], a[140:143], v[208:211], v[28:31]\n\t" \
    "v_mfma_f32_16x16x32_bf16 v[32:35], a[128:131], v[212:215], v[32:35]\n\t" \
    "buffer_load_dwordx4 v[100:103], v93, s[8:11], s18 offen sc1\n\t" \
    "v_mfma_f32_16x16x32_bf16 v[36:39], a[132:135], v[212:215], v[36:39]\n\t" \
    "v_mfma_f32_16x16x32_bf16 v[40:43], a[136:139], v[212:215], v[40:43]\n\t" \
    "v_mfma_f32_16x16x32_bf16 v[44:47], a[140:143], v[212:215], v[44:47]\n\t" \
    "v_mfma_f32_16x16x32_bf16 v[48:51], a[128:131], v[216:219], v[48:51]\n\t" \
    "buffer_load_dwordx4 v[104:107], v94, s[8:11], s18 offen sc1\n\t" \
    "v_mfma_f32_16x16x32_bf16 v[52:55], a[132:135], v[216:219], v[52:55]\n\t" \
    "v_mfma_f32_16x16x32_bf16 v[56:59], a[136:139], v[216:219], v[56:59]\n\t" \
    "v_mfma_f32_16x16x32_bf16 v[60:63], a[140:143], v[216:219], v[60:63]\n\t" \
    "v_mfma_f32_16x16x32_bf16 v[64:67], a[128:131], v[220:223], v[64:67]\n\t" \
    "buffer_load_dwordx4 v[108:111], v95, s[8:11], s18 offen sc1\n\t" \
    "v_mfma_f32_16x16x32_bf16 v[68:71], a[132:135], v[220:223], v[68:71]\n\t" \
    "v_mfma_f32_16x16x32_bf16 v[72:75], a[136:139], v[220:223], v[72:75]\n\t" \
    "v_mfma_f32_16x16x32_bf16 v[76:79], a[140:143], v[220:223], v[76:79]\n\t" \
    "s_waitcnt vmcnt(11)\n\t" \
    "v_mfma_f32_16x16x32_bf16 v[16:19], a[176:179], v[224:227], v[16:19]\n\t" \
    "buffer_load_dwordx4 v[112:115], v92, s[8:11], s19 offen sc1\n\t" \
    "v_mfma_f32_16x16x32_bf16 v[20:23], a[180:183], v[224:227], v[20:23]\n\t" \
    "v_mfma_f32_16x16x32_bf16 v[24:27], a[184:187], v[224:227], v[24:27]\n\t" \
    "v_mfma_f32_16x16x32_bf16 v[28:31], a[188:191], v[224:227], v[28:31]\n\t" \
    "s_waitcnt vmcnt(11)\n\t" \
    "v_mfma_f32_16x16x32_bf16 v[32:35], a[176:179], v[228:231], v[32:35]\n\t" \
    "buffer_load_dwordx4 v[116:119], v93, s[8:11], s19 offen sc1\n\t" \
    "v_mfma_f32_16x16x32_bf16 v[36:39], a[180:183], v[228:231], v[36:39]\n\t" \
    "v_mfma_f32_16x16x32_bf16 v[40:43], a[184:187], v[228:231], v[40:43]\n\t" \
    "v_mfma_f32_16x16x32_bf16 v[44:47], a[188:191], v[228:231], v[44:47]\n\t" \
    "s_waitcnt vmcnt(11)\n\t" \
    "v_mfma_f32_16x16x32_bf16 v[48:51], a[176:179], v[232:235], v[48:51]\n\t" \
    "buffer_load_dwordx4 v[120:123], v94, s[8:11], s19 offen sc1\n\t" \
    "v_mfma_f32_16x16x32_bf16 v[52:55], a[180:183], v[232:235], v[52:55]\n\t" \
    "v_mfma_f32_16x16x32_bf16 v[56:59], a[184:187], v[232:235], v[56:59]\n\t" \
    "v_mfma_f32_16x16x32_bf16 v[60:63], a[188:191], v[232:235], v[60:63]\n\t" \
    "s_waitcnt vmcnt(11)\n\t" \
    "v_mfma_f32_16x16x32_bf16 v[64:67], a[176:179], v[236:239], v[64:67]\n\t" \
    "buffer_load_dwordx4 v[124:127], v95, s[8:11], s19 offen sc1\n\t" \
    "v_mfma_f32_16x16x32_bf16 v[68:71], a[180:183], v[236:239], v[68:71]\n\t" \
    "v_mfma_f32_16x16x32_bf16 v[72:75], a[184:187], v[236:239], v[72:75]\n\t" \
    "v_mfma_f32_16x16x32_bf16 v[76:79], a[188:191], v[236:239], v[76:79]\n\t" \
    "s_waitcnt vmcnt(11)\n\t" \
    "v_mfma_f32_16x16x32_bf16 v[16:19], a[192:195], v[240:243], v[16:19]\n\t" \
    "buffer_load_dwordx4 v[128:131], v92, s[8:11], s22 offen sc1\n\t" \
    "v_mfma_f32_16x16x32_bf16 v[20:23], a[196:199], v[240:243], v[20:23]\n\t" \
    "v_mfma_f32_16x16x32_bf16 v[24:27], a[200:203], v[240:243], v[24:27]\n\t" \
    "v_mfma_f32_16x16x32_bf16 v[28:31], a[204:207], v[240:243], v[28:31]\n\t" \
    "s_waitcnt vmcnt(11)\n\t" \
    "v_mfma_f32_16x16x32_bf16 v[32:35], a[192:195], v[244:247], v[32:35]\n\t" \
    "buffer_load_dwordx4 v[132:135], v93, s[8:11], s22 offen sc1\n\t" \
    "v_mfma_f32_16x16x32_bf16 v[36:39], a[196:199], v[244:247], v[36:39]\n\t" \
    "v_mfma_f32_16x16x32_bf16 v[40:43], a[200:203], v[244:247], v[40:43]\n\t" \
    "v_mfma_f32_16x16x32_bf16 v[44:47], a[204:207], v[244:247], v[44:47]\n\t" \
    "s_waitcnt vmcnt(11)\n\t" \
    "v_mfma_f32_16x16x32_bf16 v[48:51], a[192:195], v[248:251], v[48:51]\n\t" \
    "buffer_load_dwordx4 v[136:139], v94, s[8:11], s22 offen sc1\n\t" \
    "v_mfma_f32_16x16x32_bf16 v[52:55], a[196:199], v[248:251], v[52:55]\n\t" \
    "v_mfma_f32_16x16x32_bf16 v[56:59], a[200:203], v[248:251], v[56:59]\n\t" \
    "v_mfma_f32_16x16x32_bf16 v[60:63], a[204:207], v[248:251], v[60:63]\n\t" \
    "s_waitcnt vmcnt(11)\n\t" \
    "v_mfma_f32_16x16x32_bf16 v[64:67], a[192:195], v[252:255], v[64:67]\n\t" \
    "buffer_load_dwordx4 v[140:143], v95, s[8:11], s22 offen sc1\n\t" \
    "v_mfma_f32_16x16x32_bf16 v[68:71], a[196:199], v[252:255], v[68:71]\n\t" \
    "v_mfma_f32_16x16x32_bf16 v[72:75], a[200:203], v[252:255], v[72:75]\n\t" \
    "v_mfma_f32_16x16x32_bf16 v[76:79], a[204:207], v[252:255], v[76:79]\n\t" \
    "s_waitcnt vmcnt(11)\n\t" \
    "v_mfma_f32_16x16x32_bf16 v[16:19], a[32:35], v[96:99], v[16:19]\n\t" \
    "buffer_load_dwordx4 v[144:147], v92, s[8:11], s25 offen sc1\n\t" \
    "v_mfma_f32_16x16x32_bf16 v[20:23], a[36:39], v[96:99], v[20:23]\n\t" \
    "v_mfma_f32_16x16x32_bf16 v[24:27], a[40:43], v[96:99], v[24:27]\n\t" \
    "v_mfma_f32_16x16x32_bf16 v[28:31], a[44:47], v[96:99], v[28:31]\n\t" \
    "s_waitcnt vmcnt(11)\n\t" \
    "v_mfma_f32_16x16x32_bf16 v[32:35], a[32:35], v[100:103], v[32:35]\n\t" \
    "buffer_load_dwordx4 v[148:151], v93, s[8:11], s25 offen sc1\n\t" \
    "v_mfma_f32_16x16x32_bf16 v[36:39], a[36:39], v[100:103], v[36:39]\n\t" \
    "v_mfma_f32_16x16x32_bf16 v[40:43], a[40:43], v[100:103], v[40:43]\n\t" \
    "v_mfma_f32_16x16x32_bf16 v[44:47], a[44:47], v[100:103], v[44:47]\n\t" \
    "s_waitcnt vmcnt(11)\n\t" \
    "v_mfma_f32_16x16x32_bf16 v[48:51], a[32:35], v[104:107], v[48:51]\n\t" \
    "buffer_load_dwordx4 v[152:155], v94, s[8:11], s25 offen sc1\n\t" \
    "v_mfma_f32_16x16x32_bf16 v[52:55], a[36:39], v[104:107], v[52:55]\n\t" \
    "v_mfma_f32_16x16x32_bf16 v[56:59], a[40:43], v[104:107], v[56:59]\n\t" \
    "v_mfma_f32_16x16x32_bf16 v[60:63], a[44:47], v[104:107], v[60:63]\n\t" \
    "s_waitcnt vmcnt(11)\n\t" \
    "v_mfma_f32_16x16x32_bf16 v[64:67], a[32:35], v[108:111], v[64:67]\n\t" \
    "buffer_load_dwordx4 v[156:159], v95, s[8:11], s25 offen sc1\n\t" \
    "v_mfma_f32_16x16x32_bf16 v[68:71], a[36:39], v[108:111], v[68:71]\n\t" \
    "v_mfma_f32_16x16x32_bf16 v[72:75], a[40:43], v[108:111], v[72:75]\n\t" \
    "v_mfma_f32_16x16x32_bf16 v[76:79], a[44:47], v[108:111], v[76:79]\n\t" \
    "s_waitcnt vmcnt(10)\n\t" \
    "s_mov_b64 exec, s[84:85]\n\t" \
    "v_swap_b32 v114, v116\n\t" \
    "v_swap_b32 v115, v117\n\t" \
    "s_not_b64 exec, exec\n\t" \
    "v_swap_b32 v112, v118\n\t" \
    "v_swap_b32 v113, v119\n\t" \
    "s_mov_b64 exec, -1\n\t" \
    "s_nop 1\n\t" \
    "v_mfma_f32_16x16x32_bf16 v[16:19], a[48:51], v[112:115], v[16:19]\n\t" \
    "buffer_load_dwordx4 v[160:163], v92, s[8:11], s26 offen sc1\n\t" \
    "v_mfma_f32_16x16x32_bf16 v[20:23], a[52:55], v[112:115], v[20:23]\n\t" \
    "v_mfma_f32_16x16x32_bf16 v[24:27], a[56:59], v[112:115], v[24:27]\n\t" \
    "v_mfma_f32_16x16x32_bf16 v[28:31], a[60:63], v[112:115], v[28:31]\n\t" \
    "v_mfma_f32_16x16x32_bf16 v[32:35], a[48:51], v[116:119], v[32:35]\n\t" \
    "buffer_load_dwordx4 v[164:167], v93, s[8:11], s26 offen sc1\n\t" \
    "v_mfma_f32_16x16x32_bf16 v[36:39], a[52:55], v[116:119], v[36:39]\n\t" \
    "v_mfma_f32_16x16x32_bf16 v[40:43], a[56:59], v[116:119], v[40:43]\n\t" \
    "v_mfma_f32_16x16x32_bf16 v[44:47], a[60:63], v[116:119], v[44:47]\n\t" \
    "s_waitcnt vmcnt(10)\n\t" \
    "s_mov_b64 exec, s[84:85]\n\t" \
    "v_swap_b32 v122, v124\n\t" \
    "v_swap_b32 v123, v125\n\t" \
    "s_not_b64 exec, exec\n\t" \
    "v_swap_b32 v120, v126\n\t" \
    "v_swap_b32 v121, v127\n\t" \
    "s_mov_b64 exec, -1\n\t" \
    "s_nop 1\n\t" \
    "v_mfma_f32_16x16x32_bf16 v[48:51], a[48:51], v[120:123], v[48:51]\n\t" \
    "buffer_load_dwordx4 v[168:171], v94, s[8:11], s26 offen sc1\n\t" \
    "v_mfma_f32_16x16x32_bf16 v[52:55], a[52:55], v[120:123], v[52:55]\n\t" \
    "v_mfma_f32_16x16x32_bf16 v[56:59], a[56:59], v[120:123], v[56:59]\n\t" \
    "v_mfma_f32_16x16x32_bf16 v[60:63], a[60:63], v[120:123], v[60:63]\n\t" \
    "v_mfma_f32_16x16x32_bf16 v[64:67], a[48:51], v[124:127], v[64:67]\n\t" \
    "buffer_load_dwordx4 v[172:175], v95, s[8:11], s26 offen sc1\n\t" \
    "v_mfma_f32_16x16x32_bf16 v[68:71], a[52:55], v[124:127], v[68:71]\n\t" \
    "v_mfma_f32_16x16x32_bf16 v[72:75], a[56:59], v[124:127], v[72:75]\n\t" \
    "v_mfma_f32_16x16x32_bf16 v[76:79], a[60:63], v[124:127], v[76:79]\n\t" \
    "s_waitcnt vmcnt(11)\n\t" \
    "v_mfma_f32_16x16x32_bf16 v[16:19], a[96:99], v[128:131], v[16:19]\n\t" \
    "buffer_load_dwordx4 v[176:179], v92, s[8:11], s29 offen sc1\n\t" \
    "v_mfma_f32_16x16x32_bf16 v[20:23], a[100:103], v[128:131], v[20:23]\n\t" \
    "v_mfma_f32_16x16x32_bf16 v[24:27], a[104:107], v[128:131], v[24:27]\n\t" \
    "v_mfma_f32_16x16x32_bf16 v[28:31], a[108:111], v[128:131], v[28:31]\n\t" \
    "s_waitcnt vmcnt(11)\n\t" \
    "v_mfma_f32_16x16x32_bf16 v[32:35], a[96:99], v[132:135], v[32:35]\n\t" \
    "buffer_load_dwordx4 v[180:183], v93, s[8:11], s29 offen sc1\n\t" \
    "v_mfma_f32_16x16x32_bf16 v[36:39], a[100:103], v[132:135], v[36:39]\n\t" \
    "v_mfma_f32_16x16x32_bf16 v[40:43], a[104:107], v[132:135], v[40:43]\n\t" \
    "v_mfma_f32_16x16x32_bf16 v[44:47], a[108:111], v[132:135], v[44:47]\n\t" \
    "s_waitcnt vmcnt(11)\n\t" \
    "v_mfma_f32_16x16x32_bf16 v[48:51], a[96:99], v[136:139], v[48:51]\n\t" \
    "buffer_load_dwordx4 v[184:187], v94, s[8:11], s29 offen sc1\n\t" \
    "v_mfma_f32_16x16x32_bf16 v[52:55], a[100:103], v[136:139], v[52:55]\n\t" \
    "v_mfma_f32_16x16x32_bf16 v[56:59], a[104:107], v[136:139], v[56:59]\n\t" \
    "v_mfma_f32_16x16x32_bf16 v[60:63], a[108:111], v[136:139], v[60:63]\n\t" \
    "s_waitcnt vmcnt(11)\n\t" \
    "v_mfma_f32_16x16x32_bf16 v[64:67], a[96:99], v[140:143], v[64:67]\n\t" \
    "buffer_load_dwordx4 v[188:191], v95, s[8:11], s29 offen sc1\n\t" \
    "v_mfma_f32_16x16x32_bf16 v[68:71], a[100:103], v[140:143], v[68:71]\n\t" \
    "v_mfma_f32_16x16x32_bf16 v[72:75], a[104:107], v[140:143], v[72:75]\n\t" \
    "v_mfma_f32_16x16x32_bf16 v[76:79], a[108:111], v[140:143], v[76:79]\n\t" \
    "buffer_load_dwordx4 v[80:83], v2, s[8:11], s80 offen offset:0 sc1\n\t" \
    "buffer_load_dwordx4 v[84:87], v2, s[8:11], s80 offen offset:1024 sc1\n\t" \
    "buffer_load_dwordx4 v[224:227], v90, s[8:11], s82 offen offset:0 sc1\n\t" \
    "buffer_load_dwordx4 v[228:231], v90, s[8:11], s82 offen offset:16 sc1\n\t" \
    "buffer_load_dwordx4 v[232:235], v90, s[8:11], s82 offen offset:128 sc1\n\t" \
    "buffer_load_dwordx4 v[236:239], v90, s[8:11], s82 offen offset:144 sc1\n\t" \
    "s_waitcnt vmcnt(17)\n\t" \
    "v_mfma_f32_16x16x32_bf16 v[16:19], a[144:147], v[144:147], v[16:19]\n\t" \
    "v_mfma_f32_16x16x32_bf16 v[20:23], a[148:151], v[144:147], v[20:23]\n\t" \
    "v_mfma_f32_16x16x32_bf16 v[24:27], a[152:155], v[144:147], v[24:27]\n\t" \
    "v_mfma_f32_16x16x32_bf16 v[28:31], a[156:159], v[144:147], v[28:31]\n\t" \
    "s_waitcnt vmcnt(16)\n\t" \
    "v_mfma_f32_16x16x32_bf16 v[32:35], a[144:147], v[148:151], v[32:35]\n\t" \
    "v_mfma_f32_16x16x32_bf16 v[36:39], a[148:151], v[148:151], v[36:39]\n\t" \
    "v_mfma_f32_16x16x32_bf16 v[40:43], a[152:155], v[148:151], v[40:43]\n\t" \
    "v_mfma_f32_16x16x32_bf16 v[44:47], a[156:159], v[148:151], v[44:47]\n\t" \
    "s_waitcnt vmcnt(15)\n\t" \
    "v_mfma_f32_16x16x32_bf16 v[48:51], a[144:147], v[152:155], v[48:51]\n\t" \
    "v_mfma_f32_16x16x32_bf16 v[52:55], a[148:151], v[152:155], v[52:55]\n\t" \
    "v_mfma_f32_16x16x32_bf16 v[56:59], a[152:155], v[152:155], v[56:59]\n\t" \
    "v_mfma_f32_16x16x32_bf16 v[60:63], a[156:159], v[152:155], v[60:63]\n\t" \
    "s_waitcnt vmcnt(14)\n\t" \
    "v_mfma_f32_16x16x32_bf16 v[64:67], a[144:147], v[156:159], v[64:67]\n\t" \
    "v_mfma_f32_16x16x32_bf16 v[68:71], a[148:151], v[156:159], v[68:71]\n\t" \
    "v_mfma_f32_16x16x32_bf16 v[72:75], a[152:155], v[156:159], v[72:75]\n\t" \
    "v_mfma_f32_16x16x32_bf16 v[76:79], a[156:159], v[156:159], v[76:79]\n\t" \
    "ds_read_b32 v12, v3 offset:4\n\t" \
    "s_waitcnt vmcnt(12)\n\t" \
    "s_mov_b64 exec, s[84:85]\n\t" \
    "v_swap_b32 v162, v164\n\t" \
    "v_swap_b32 v163, v165\n\t" \
    "s_not_b64 exec, exec\n\t" \
    "v_swap_b32 v160, v166\n\t" \
    "v_swap_b32 v161, v167\n\t" \
    "s_mov_b64 exec, -1\n\t" \
    "s_nop 1\n\t" \
    "v_mfma_f32_16x16x32_bf16 v[16:19], a[160:163], v[160:163], v[16:19]\n\t" \
    "v_mfma_f32_16x16x32_bf16 v[20:23], a[164:167], v[160:163], v[20:23]\n\t" \
    "v_mfma_f32_16x16x32_bf16 v[24:27], a[168:171], v[160:163], v[24:27]\n\t" \
    "v_mfma_f32_16x16x32_bf16 v[28:31], a[172:175], v[160:163], v[28:31]\n\t" \
    "v_mfma_f32_16x16x32_bf16 v[32:35], a[160:163], v[164:167], v[32:35]\n\t" \
    "v_mfma_f32_16x16x32_bf16 v[36:39], a[164:167], v[164:167], v[36:39]\n\t" \
    "v_mfma_f32_16x16x32_bf16 v[40:43], a[168:171], v[164:167], v[40:43]\n\t" \
    "v_mfma_f32_16x16x32_bf16 v[44:47], a[172:175], v[164:167], v[44:47]\n\t" \
    "s_waitcnt vmcnt(10)\n\t" \
    "s_mov_b64 exec, s[84:85]\n\t" \
    "v_swap_b32 v170, v172\n\t" \
    "v_swap_b32 v171, v173\n\t" \
    "s_not_b64 exec, exec\n\t" \
    "v_swap_b32 v168, v174\n\t" \
    "v_swap_b32 v169, v175\n\t" \
    "s_mov_b64 exec, -1\n\t" \
    "s_nop 1\n\t" \
    "v_mfma_f32_16x16x32_bf16 v[48:51], a[160:163], v[168:171], v[48:51]\n\t" \
    "v_mfma_f32_16x16x32_bf16 v[52:55], a[164:167], v[168:171], v[52:55]\n\t" \
    "v_mfma_f32_16x16x32_bf16 v[56:59], a[168:171], v[168:171], v[56:59]\n\t" \
    "v_mfma_f32_16x16x32_bf16 v[60:63], a[172:175], v[168:171], v[60:63]\n\t" \
    "v_mfma_f32_16x16x32_bf16 v[64:67], a[160:163], v[172:175], v[64:67]\n\t" \
    "v_mfma_f32_16x16x32_bf16 v[68:71], a[164:167], v[172:175], v[68:71]\n\t" \
    "v_mfma_f32_16x16x32_bf16 v[72:75], a[168:171], v[172:175], v[72:75]\n\t" \
    "v_mfma_f32_16x16x32_bf16 v[76:79], a[172:175], v[172:175], v[76:79]\n\t" \
    "s_waitcnt lgkmcnt(0)\n\t" \
    "v_cmp_ne_u32 vcc, 4, v12\n\t" \
    "s_nop 0\n\t" \
    "s_cbranch_vccnz .Lc4_%=\n\t" \
    ".Lc4r_%=:\n\t" \
    "v_xor_b32 v12, 0xc000, v89\n\t" \
    "v_xor_b32 v13, 0x8000, v89\n\t" \
    "v_xor_b32 v14, 0x4000, v89\n\t" \
    "s_waitcnt vmcnt(9)\n\t" \
    "v_mfma_f32_16x16x32_bf16 v[16:19], a[208:211], v[176:179], v[16:19]\n\t" \
    "v_mfma_f32_16x16x32_bf16 v[20:23], a[212:215], v[176:179], v[20:23]\n\t" \
    "v_mfma_f32_16x16x32_bf16 v[24:27], a[216:219], v[176:179], v[24:27]\n\t" \
    "v_mfma_f32_16x16x32_bf16 v[28:31], a[220:223], v[176:179], v[28:31]\n\t" \
    "s_waitcnt vmcnt(8)\n\t" \
    "v_mfma_f32_16x16x32_bf16 v[32:35], a[208:211], v[180:183], v[32:35]\n\t" \
    "v_mfma_f32_16x16x32_bf16 v[36:39], a[212:215], v[180:183], v[36:39]\n\t" \
    "v_mfma_f32_16x16x32_bf16 v[40:43], a[216:219], v[180:183], v[40:43]\n\t" \
    "v_mfma_f32_16x16x32_bf16 v[44:47], a[220:223], v[180:183], v[44:47]\n\t" \
    "s_waitcnt vmcnt(7)\n\t" \
    "v_mfma_f32_16x16x32_bf16 v[48:51], a[208:211], v[184:187], v[48:51]\n\t" \
    "ds_write_b128 v12, v[16:19] offset:0\n\t" \
    "v_mfma_f32_16x16x32_bf16 v[52:55], a[212:215], v[184:187], v[52:55]\n\t" \
    "ds_write_b128 v12, v[20:23] offset:1024\n\t" \
    "v_mfma_f32_16x16x32_bf16 v[56:59], a[216:219], v[184:187], v[56:59]\n\t" \
    "ds_write_b128 v12, v[24:27] offset:2048\n\t" \
    "v_mfma_f32_16x16x32_bf16 v[60:63], a[220:223], v[184:187], v[60:63]\n\t" \
    "ds_write_b128 v12, v[28:31] offset:3072\n\t" \
    "s_waitcnt vmcnt(6)\n\t" \
    "v_mfma_f32_16x16x32_bf16 v[64:67], a[208:211], v[188:191], v[64:67]\n\t" \
    "ds_write_b128 v13, v[32:35] offset:0\n\t" \
    "v_mfma_f32_16x16x32_bf16 v[68:71], a[212:215], v[188:191], v[68:71]\n\t" \
    "ds_write_b128 v13, v[36:39] offset:1024\n\t" \
    "v_mfma_f32_16x16x32_bf16 v[72:75], a[216:219], v[188:191], v[72:75]\n\t" \
    "ds_write_b128 v13, v[40:43] offset:2048\n\t" \
    "v_mfma_f32_16x16x32_bf16 v[76:79], a[220:223], v[188:191], v[76:79]\n\t" \
    "ds_write_b128 v13, v[44:47] offset:3072\n\t" \
    "s_waitcnt vmcnt(0)\n\t" \
    "v_lshlrev_b32 v240, 16, v80\n\t" \
    "v_and_b32 v241, 0xffff0000, v80\n\t" \
    "v_lshlrev_b32 v242, 16, v81\n\t" \
    "v_and_b32 v243, 0xffff0000, v81\n\t" \
    "v_lshlrev_b32 v244, 16, v82\n\t" \
    "v_and_b32 v245, 0xffff0000, v82\n\t" \
    "v_lshlrev_b32 v246, 16, v83\n\t" \
    "v_and_b32 v247, 0xffff0000, v83\n\t" \
    "ds_write_b128 v14, v[48:51] offset:0\n\t" \
    "ds_write_b128 v14, v[52:55] offset:1024\n\t" \
    "ds_write_b128 v14, v[56:59] offset:2048\n\t" \
    "ds_write_b128 v14, v[60:63] offset:3072\n\t" \
    "v_lshlrev_b32 v248, 16, v84\n\t" \
    "v_and_b32 v249, 0xffff0000, v84\n\t" \
    "v_lshlrev_b32 v250, 16, v85\n\t" \
    "v_and_b32 v251, 0xffff0000, v85\n\t" \
    "v_lshlrev_b32 v252, 16, v86\n\t" \
    "v_and_b32 v253, 0xffff0000, v86\n\t" \
    "v_lshlrev_b32 v254, 16, v87\n\t" \
    "v_and_b32 v255, 0xffff0000, v87\n\t" \
    "s_waitcnt lgkmcnt(0)\n\t" \
    "s_barrier\n\t" \
    "v_xor_b32 v12, 0x1000, v89\n\t" \
    "ds_read_b128 v[96:99], v12 offset:0\n\t" \
    "ds_read_b128 v[100:103], v12 offset:1024\n\t" \
    "ds_read_b128 v[104:107], v12 offset:2048\n\t" \
    "ds_read_b128 v[108:111], v12 offset:3072\n\t" \
    "v_xor_b32 v13, 0x2000, v89\n\t" \
    "ds_read_b128 v[112:115], v13 offset:0\n\t" \
    "ds_read_b128 v[116:119], v13 offset:1024\n\t" \
    "ds_read_b128 v[120:123], v13 offset:2048\n\t" \
    "ds_read_b128 v[124:127], v13 offset:3072\n\t" \
    "v_xor_b32 v14, 0x3000, v89\n\t" \
    "ds_read_b128 v[128:131], v14 offset:0\n\t" \
    "ds_read_b128 v[132:135], v14 offset:1024\n\t" \
    "ds_read_b128 v[136:139], v14 offset:2048\n\t" \
    "ds_read_b128 v[140:143], v14 offset:3072\n\t" \
    "s_waitcnt lgkmcnt(8)\n\t" \
    "v_pk_add_f32 v[64:65], v[64:65], v[96:97]\n\t" \
    "v_pk_add_f32 v[66:67], v[66:67], v[98:99]\n\t" \
    "v_pk_add_f32 v[68:69], v[68:69], v[100:101]\n\t" \
    "v_pk_add_f32 v[70:71], v[70:71], v[102:103]\n\t" \
    "v_pk_add_f32 v[72:73], v[72:73], v[104:105]\n\t" \
    "v_pk_add_f32 v[74:75], v[74:75], v[106:107]\n\t" \
    "v_pk_add_f32 v[76:77], v[76:77], v[108:109]\n\t" \
    "v_pk_add_f32 v[78:79], v[78:79], v[110:111]\n\t" \
    "s_waitcnt lgkmcnt(4)\n\t" \
    "v_pk_add_f32 v[64:65], v[64:65], v[112:113]\n\t" \
    "v_pk_add_f32 v[66:67], v[66:67], v[114:115]\n\t" \
    "v_pk_add_f32 v[68:69], v[68:69], v[116:117]\n\t" \
    "v_pk_add_f32 v[70:71], v[70:71], v[118:119]\n\t" \
    "v_pk_add_f32 v[72:73], v[72:73], v[120:121]\n\t" \
    "v_pk_add_f32 v[74:75], v[74:75], v[122:123]\n\t" \
    "v_pk_add_f32 v[76:77], v[76:77], v[124:125]\n\t" \
    "v_pk_add_f32 v[78:79], v[78:79], v[126:127]\n\t" \
    "s_waitcnt lgkmcnt(0)\n\t" \
    "v_pk_add_f32 v[64:65], v[64:65], v[128:129]\n\t" \
    "v_pk_add_f32 v[66:67], v[66:67], v[130:131]\n\t" \
    "v_pk_add_f32 v[68:69], v[68:69], v[132:133]\n\t" \
    "v_pk_add_f32 v[70:71], v[70:71], v[134:135]\n\t" \
    "v_pk_add_f32 v[72:73], v[72:73], v[136:137]\n\t" \
    "v_pk_add_f32 v[74:75], v[74:75], v[138:139]\n\t" \
    "v_pk_add_f32 v[76:77], v[76:77], v[140:141]\n\t" \
    "v_pk_add_f32 v[78:79], v[78:79], v[142:143]\n\t" \
    "v_pk_fma_f32 v[64:65], v[64:65], v[224:225], v[240:241]\n\t" \
    "v_pk_fma_f32 v[66:67], v[66:67], v[226:227], v[242:243]\n\t" \
    "v_pk_fma_f32 v[68:69], v[68:69], v[228:229], v[244:245]\n\t" \
    "v_pk_fma_f32 v[70:71], v[70:71], v[230:231], v[246:247]\n\t" \
    "v_pk_fma_f32 v[72:73], v[72:73], v[232:233], v[248:249]\n\t" \
    "v_pk_fma_f32 v[74:75], v[74:75], v[234:235], v[250:251]\n\t" \
    "v_pk_fma_f32 v[76:77], v[76:77], v[236:237], v[252:253]\n\t" \
    "v_pk_fma_f32 v[78:79], v[78:79], v[238:239], v[254:255]\n\t" \
    "v_cvt_pk_bf16_f32 v96, v64, v65\n\t" \
    "v_cvt_pk_bf16_f32 v97, v66, v67\n\t" \
    "v_cvt_pk_bf16_f32 v98, v68, v69\n\t" \
    "v_cvt_pk_bf16_f32 v99, v70, v71\n\t" \
    "v_cvt_pk_bf16_f32 v100, v72, v73\n\t" \
    "v_cvt_pk_bf16_f32 v101, v74, v75\n\t" \
    "v_cvt_pk_bf16_f32 v102, v76, v77\n\t" \
    "v_cvt_pk_bf16_f32 v103, v78, v79\n\t" \
    "v_mov_b32 v104, v100\n\t" \
    "v_mov_b32 v105, v101\n\t" \
    "v_mov_b32 v106, v102\n\t" \
    "v_mov_b32 v107, v103\n\t" \
    "v_mov_b32_dpp v100, v96 row_ror:8 row_mask:0xf bank_mask:0x3\n\t" \
    "v_mov_b32_dpp v101, v97 row_ror:8 row_mask:0xf bank_mask:0x3\n\t" \
    "v_mov_b32_dpp v102, v98 row_ror:8 row_mask:0xf bank_mask:0x3\n\t" \
    "v_mov_b32_dpp v103, v99 row_ror:8 row_mask:0xf bank_mask:0x3\n\t" \
    "v_mov_b32_dpp v96, v104 row_ror:8 row_mask:0xf bank_mask:0xc\n\t" \
    "v_mov_b32_dpp v97, v105 row_ror:8 row_mask:0xf bank_mask:0xc\n\t" \
    "v_mov_b32_dpp v98, v106 row_ror:8 row_mask:0xf bank_mask:0xc\n\t" \
    "v_mov_b32_dpp v99, v107 row_ror:8 row_mask:0xf bank_mask:0xc\n\t" \
    "buffer_store_dwordx4 v[96:99], v91, s[40:43], s94 offen sc1\n\t" \
    "s_add_u32 s94, s94, 0x2000\n\t" \
    "buffer_store_dwordx4 v[100:103], v91, s[40:43], s94 offen sc1\n\t" \
    "s_branch .Lend_%=\n\t" \
      \
    ".Lsl0_%=:\n\t" \
    "ds_read_b32 v82, v253\n\t" \
    "v_add_u32 v253, 128, v253\n\t" \
    "s_nop 7\n\t" \
    "s_waitcnt lgkmcnt(0)\n\t" \
    "v_mov_b32 v100, v82\n\t" \
    "v_mov_b32 v101, v82\n\t" \
    "v_mov_b32 v102, v82\n\t" \
    "v_mov_b32 v103, v82\n\t" \
    "v_mov_b32 v104, v82\n\t" \
    "v_mov_b32 v105, v82\n\t" \
    "v_mov_b32 v106, v82\n\t" \
    "v_mov_b32 v107, v82\n\t" \
    "v_mov_b32 v108, v82\n\t" \
    "v_mov_b32 v109, v82\n\t" \
    "v_mov_b32 v110, v82\n\t" \
    "v_mov_b32 v111, v82\n\t" \
    "v_mov_b32 v112, v82\n\t" \
    "v_mov_b32 v113, v82\n\t" \
    "v_mov_b32 v114, v82\n\t" \
    "v_mov_b32 v115, v82\n\t" \
    "s_nop 1\n\t" \
    "s_branch .Lslr0_%=\n\t" \
    ".Lsl1_%=:\n\t" \
    "ds_read_b32 v82, v253\n\t" \
    "v_add_u32 v253, 128, v253\n\t" \
    "s_nop 7\n\t" \
    "s_waitcnt lgkmcnt(0)\n\t" \
    "v_mov_b32 v100, v82\n\t" \
    "v_mov_b32 v101, v82\n\t" \
    "v_mov_b32 v102, v82\n\t" \
    "v_mov_b32 v103, v82\n\t" \
    "v_mov_b32 v104, v82\n\t" \
    "v_mov_b32 v105, v82\n\t" \
    "v_mov_b32 v106, v82\n\t" \
    "v_mov_b32 v107, v82\n\t" \
    "v_mov_b32 v108, v82\n\t" \
    "v_mov_b32 v109, v82\n\t" \
    "v_mov_b32 v110, v82\n\t" \
    "v_mov_b32 v111, v82\n\t" \
    "v_mov_b32 v112, v82\n\t" \
    "v_mov_b32 v113, v82\n\t" \
    "v_mov_b32 v114, v82\n\t" \
    "v_mov_b32 v115, v82\n\t" \
    "s_nop 1\n\t" \
    "s_branch .Lslr1_%=\n\t" \
    ".Lr11_%=:\n\t" \
    "s_sub_u32 s62, s62, 1\n\t" \
    "s_cmp_lt_i32 s62, 0\n\t" \
    "s_cbranch_scc1 .Lr1r1_%=\n\t" \
    "ds_read_b32 v80, v78\n\t" \
    "ds_read_b64 v[224:225], v244 offset:0\n\t" \
    "ds_read_b64 v[226:227], v244 offset:2048\n\t" \
    "ds_read_b64 v[228:229], v244 offset:4096\n\t" \
    "ds_read_b64 v[230:231], v244 offset:6144\n\t" \
    "ds_read_b128 v[232:235], v245\n\t" \
    "s_waitcnt lgkmcnt(0)\n\t" \
    "v_cmp_ne_u32 vcc, s71, v80\n\t" \
    "v_mul_f32 v232, v232, v236\n\t" \
    "v_mul_f32 v224, v224, v236\n\t" \
    "v_mul_f32 v225, v225, v236\n\t" \
    "v_fmac_f32 v232, v233, v237\n\t" \
    "v_fmac_f32 v224, v226, v237\n\t" \
    "v_fmac_f32 v225, v227, v237\n\t" \
    "v_fmac_f32 v232, v234, v238\n\t" \
    "v_fmac_f32 v224, v228, v238\n\t" \
    "v_fmac_f32 v225, v229, v238\n\t" \
    "v_fmac_f32 v232, v235, v239\n\t" \
    "v_fmac_f32 v224, v230, v239\n\t" \
    "v_rcp_f32 v232, v232\n\t" \
    "v_fmac_f32 v225, v231, v239\n\t" \
    "s_cbranch_vccnz .Lr11_%=\n\t" \
    "s_branch .Lr1r1_%=\n\t" \
    ".Lr21_%=:\n\t" \
    "s_sub_u32 s62, s62, 1\n\t" \
    "s_cmp_lt_i32 s62, 0\n\t" \
    "s_cbranch_scc1 .Lr2r1_%=\n\t" \
    "ds_read_b32 v80, v78 offset:16\n\t" \
    "ds_read_b128 v[208:211], v247 offset:0\n\t" \
    "ds_read_b128 v[212:215], v247 offset:64\n\t" \
    "ds_read_b128 v[216:219], v248 offset:0\n\t" \
    "ds_read_b128 v[220:223], v248 offset:64\n\t" \
    "s_waitcnt lgkmcnt(0)\n\t" \
    "v_cmp_ne_u32 vcc, s71, v80\n\t" \
    "s_nop 0\n\t" \
    "s_cbranch_vccnz .Lr21_%=\n\t" \
    "s_branch .Lr2r1_%=\n\t" \
    ".Lr13_%=:\n\t" \
    "s_sub_u32 s62, s62, 1\n\t" \
    "s_cmp_lt_i32 s62, 0\n\t" \
    "s_cbranch_scc1 .Lr1r3_%=\n\t" \
    "ds_read_b32 v80, v78\n\t" \
    "ds_read_b64 v[224:225], v244 offset:0\n\t" \
    "ds_read_b64 v[226:227], v244 offset:2048\n\t" \
    "ds_read_b64 v[228:229], v244 offset:4096\n\t" \
    "ds_read_b64 v[230:231], v244 offset:6144\n\t" \
    "ds_read_b128 v[232:235], v245\n\t" \
    "s_waitcnt lgkmcnt(0)\n\t" \
    "v_cmp_ne_u32 vcc, s71, v80\n\t" \
    "v_mul_f32 v232, v232, v236\n\t" \
    "v_mul_f32 v224, v224, v236\n\t" \
    "v_mul_f32 v225, v225, v236\n\t" \
    "v_fmac_f32 v232, v233, v237\n\t" \
    "v_fmac_f32 v224, v226, v237\n\t" \
    "v_fmac_f32 v225, v227, v237\n\t" \
    "v_fmac_f32 v232, v234, v238\n\t" \
    "v_fmac_f32 v224, v228, v238\n\t" \
    "v_fmac_f32 v225, v229, v238\n\t" \
    "v_fmac_f32 v232, v235, v239\n\t" \
    "v_fmac_f32 v224, v230, v239\n\t" \
    "v_rcp_f32 v232, v232\n\t" \
    "v_fmac_f32 v225, v231, v239\n\t" \
    "s_cbranch_vccnz .Lr13_%=\n\t" \
    "s_branch .Lr1r3_%=\n\t" \
    ".Lr23_%=:\n\t" \
    "s_sub_u32 s62, s62, 1\n\t" \
    "s_cmp_lt_i32 s62, 0\n\t" \
    "s_cbranch_scc1 .Lr2r3_%=\n\t" \
    "ds_read_b32 v80, v78 offset:16\n\t" \
    "ds_read_b128 v[208:211], v247 offset:0\n\t" \
    "ds_read_b128 v[212:215], v247 offset:64\n\t" \
    "ds_read_b128 v[216:219], v248 offset:0\n\t" \
    "ds_read_b128 v[220:223], v248 offset:64\n\t" \
    "s_waitcnt lgkmcnt(0)\n\t" \
    "v_cmp_ne_u32 vcc, s71, v80\n\t" \
    "s_nop 0\n\t" \
    "s_cbranch_vccnz .Lr23_%=\n\t" \
    "s_branch .Lr2r3_%=\n\t" \
    ".Lfa_%=:\n\t" \
    "s_sub_u32 s62, s62, 1\n\t" \
    "s_cmp_lt_i32 s62, 0\n\t" \
    "s_cbranch_scc1 .Lfar_%=\n\t" \
    "s_sleep 4\n\t" \
    "buffer_load_dwordx2 v[44:45], v37, s[8:11], 0 offen sc1\n\t" \
    "s_waitcnt vmcnt(0)\n\t" \
    "v_xor_b32 v44, s56, v44\n\t" \
    "v_xor_b32 v45, s57, v45\n\t" \
    "v_lshrrev_b32 v44, 16, v44\n\t" \
    "v_or_b32 v44, v44, v45\n\t" \
    "v_cmp_ne_u32 vcc, 0, v44\n\t" \
    "s_nop 0\n\t" \
    "s_cbranch_vccnz .Lfa_%=\n\t" \
    "s_branch .Lfar_%=\n\t" \
    ".Ls0_%=:\n\t" \
    "s_sub_u32 s62, s62, 1\n\t" \
    "s_cmp_lt_i32 s62, 0\n\t" \
    "s_cbranch_scc1 .Ls0r_%=\n\t" \
    "ds_read_b32 v80, v78 offset:48\n\t" \
    "ds_read_b128 v[236:239], v47\n\t" \
    "s_waitcnt lgkmcnt(0)\n\t" \
    "v_cmp_ne_u32 vcc, s95, v80\n\t" \
    "s_nop 0\n\t" \
    "s_cbranch_vccnz .Ls0_%=\n\t" \
    "s_branch .Ls0r_%=\n\t" \
    ".Lw_%=:\n\t" \
    "s_sub_u32 s62, s62, 1\n\t" \
    "s_cmp_lt_i32 s62, 0\n\t" \
    "s_cbranch_scc1 .Lwr_%=\n\t" \
    "s_sleep 4\n\t" \
    "buffer_load_dwordx2 v[84:85], v24, s[8:11], 0 offen sc1\n\t" \
    "s_waitcnt vmcnt(0)\n\t" \
    "v_cmp_ne_u64 vcc, s[54:55], v[84:85]\n\t" \
    "s_nop 0\n\t" \
    "s_cbranch_vccnz .Lw_%=\n\t" \
    "s_branch .Lwr_%=\n\t" \
    ".Lms_%=:\n\t" \
    "v_and_b32 v41, 31, v1\n\t" \
    "v_lshlrev_b32 v41, 2, v41\n\t" \
    "v_add_u32 v253, 157696, v41\n\t" \
    "ds_read_b32 v252, v253\n\t" \
    "v_add_u32 v253, 128, v253\n\t" \
    "v_mov_b32 v40, 0\n\t" \
    "s_waitcnt lgkmcnt(0)\n\t" \
    "s_branch .Lmr_%=\n\t" \
      \
    ".Lslow_%=:\n\t" \
    "s_waitcnt vmcnt(0) lgkmcnt(0)\n\t" \
    "s_nop 15\n\t" \
    "ds_read_b128 v[84:87], v254\n\t" \
    "ds_read_b128 v[88:91], v254 offset:1024\n\t" \
    "v_mov_b32 v48, 0xff800000\n\t" \
    "v_mov_b32 v49, 0xff800000\n\t" \
    "s_mov_b32 s88, 262144\n\t" \
    "s_mov_b32 s89, 0\n\t" \
    ".Lsm_%=:\n\t" \
    "buffer_load_dwordx4 v[92:95], v2, s[8:11], s88 offen sc1\n\t" \
    "buffer_load_dwordx4 v[96:99], v2, s[8:11], s88 offen offset:1024 sc1\n\t" \
    "s_waitcnt vmcnt(0) lgkmcnt(0)\n\t" \
    "v_mfma_f32_32x32x16_bf16 v[148:163], v[92:95], v[84:87], 0\n\t" \
    "v_mfma_f32_32x32x16_bf16 v[164:179], v[92:95], v[88:91], 0\n\t" \
    "s_nop 10\n\t" \
    "v_max3_f32 v48, v48, v148, v149\n\t" \
    "v_max3_f32 v48, v48, v150, v151\n\t" \
    "v_max3_f32 v48, v48, v152, v153\n\t" \
    "v_max3_f32 v48, v48, v154, v155\n\t" \
    "v_max3_f32 v48, v48, v156, v157\n\t" \
    "v_max3_f32 v48, v48, v158, v159\n\t" \
    "v_max3_f32 v48, v48, v160, v161\n\t" \
    "v_max3_f32 v48, v48, v162, v163\n\t" \
    "v_max3_f32 v49, v49, v164, v165\n\t" \
    "v_max3_f32 v49, v49, v166, v167\n\t" \
    "v_max3_f32 v49, v49, v168, v169\n\t" \
    "v_max3_f32 v49, v49, v170, v171\n\t" \
    "v_max3_f32 v49, v49, v172, v173\n\t" \
    "v_max3_f32 v49, v49, v174, v175\n\t" \
    "v_max3_f32 v49, v49, v176, v177\n\t" \
    "v_max3_f32 v49, v49, v178, v179\n\t" \
    "v_mfma_f32_32x32x16_bf16 v[148:163], v[96:99], v[84:87], 0\n\t" \
    "v_mfma_f32_32x32x16_bf16 v[164:179], v[96:99], v[88:91], 0\n\t" \
    "s_nop 10\n\t" \
    "v_max3_f32 v48, v48, v148, v149\n\t" \
    "v_max3_f32 v48, v48, v150, v151\n\t" \
    "v_max3_f32 v48, v48, v152, v153\n\t" \
    "v_max3_f32 v48, v48, v154, v155\n\t" \
    "v_max3_f32 v48, v48, v156, v157\n\t" \
    "v_max3_f32 v48, v48, v158, v159\n\t" \
    "v_max3_f32 v48, v48, v160, v161\n\t" \
    "v_max3_f32 v48, v48, v162, v163\n\t" \
    "v_max3_f32 v49, v49, v164, v165\n\t" \
    "v_max3_f32 v49, v49, v166, v167\n\t" \
    "v_max3_f32 v49, v49, v168, v169\n\t" \
    "v_max3_f32 v49, v49, v170, v171\n\t" \
    "v_max3_f32 v49, v49, v172, v173\n\t" \
    "v_max3_f32 v49, v49, v174, v175\n\t" \
    "v_max3_f32 v49, v49, v176, v177\n\t" \
    "v_max3_f32 v49, v49, v178, v179\n\t" \
    "s_add_u32 s88, s88, 0x1000\n\t" \
    "s_add_u32 s89, s89, 1\n\t" \
    "s_cmp_lt_u32 s89, 32\n\t" \
    "s_cbranch_scc1 .Lsm_%=\n\t" \
    "v_permlane32_swap_b32 v48, v49\n\t" \
    "v_max_f32 v48, v48, v49\n\t" \
    "v_xor_b32 v48, 0x80000000, v48\n\t" \
    "v_lshlrev_b32 v49, 2, v1\n\t" \
    "s_lshl_b32 s88, s50, 8\n\t" \
    "s_add_u32 s88, s88, 157696\n\t" \
    "v_add_u32 v49, s88, v49\n\t" \
    "ds_write_b32 v49, v48\n\t" \
    "s_mov_b32 s70, 1\n\t" \
    "s_waitcnt lgkmcnt(0)\n\t" \
    "s_barrier\n\t" \
    "s_branch .Lp2_%=\n\t" \
    ".Ld2_%=:\n\t" \
    "s_sub_u32 s62, s62, 1\n\t" \
    "s_cmp_lt_i32 s62, 0\n\t" \
    "s_cbranch_scc1 .Ld2r_%=\n\t" \
    "buffer_load_dwordx2 v[12:13], v88, s[8:11], s81 offen sc1\n\t" \
    "s_waitcnt vmcnt(0)\n\t" \
    "v_cmp_ne_u64 vcc, s[54:55], v[12:13]\n\t" \
    "s_nop 0\n\t" \
    "s_cbranch_vccnz .Ld2_%=\n\t" \
    "s_branch .Ld2r_%=\n\t" \
    ".Lc4_%=:\n\t" \
    "s_sub_u32 s62, s62, 1\n\t" \
    "s_cmp_lt_i32 s62, 0\n\t" \
    "s_cbranch_scc1 .Lc4r_%=\n\t" \
    "ds_read_b32 v12, v3 offset:4\n\t" \
    "s_waitcnt lgkmcnt(0)\n\t" \
    "v_cmp_ne_u32 vcc, 4, v12\n\t" \
    "s_nop 0\n\t" \
    "s_cbranch_vccnz .Lc4_%=\n\t" \
    "s_branch .Lc4r_%=\n\t" \
    ".Lend_%=:\n\t"
#define KCLOB "v1", "v2", "v3", "v4", "v5", "v6", "v7", "v8", "v9", "v10", "v11", "v12", "v13", "v14", "v15", "v16", "v17", "v18", "v19", "v20", "v21", "v22", "v23", "v24", "v25", "v26", "v27", "v28", "v29", "v30", "v31", "v32", "v33", "v34", "v35", "v36", "v37", "v38", "v39", "v40", "v41", "v42", "v43", "v44", "v45", "v46", "v47", "v48", "v49", "v50", "v51", "v52", "v53", "v54", "v55", "v56", "v57", "v58", "v59", "v60", "v61", "v62", "v63", "v64", "v65", "v66", "v67", "v68", "v69", "v70", "v71", "v72", "v73", "v74", "v75", "v76", "v77", "v78", "v79", "v80", "v81", "v82", "v83", "v84", "v85", "v86", "v87", "v88", "v89", "v90", "v91", "v92", "v93", "v94", "v95", "v96", "v97", "v98", "v99", "v100", "v101", "v102", "v103", "v104", "v105", "v106", "v107", "v108", "v109", "v110", "v111", "v112", "v113", "v114", "v115", "v116", "v117", "v118", "v119", "v120", "v121", "v122", "v123", "v124", "v125", "v126", "v127", "v128", "v129", "v130", "v131", "v132", "v133", "v134", "v135", "v136", "v137", "v138", "v139", "v140", "v141", "v142", "v143", "v144", "v145", "v146", "v147", "v148", "v149", "v150", "v151", "v152", "v153", "v154", "v155", "v156", "v157", "v158", "v159", "v160", "v161", "v162", "v163", "v164", "v165", "v166", "v167", "v168", "v169", "v170", "v171", "v172", "v173", "v174", "v175", "v176", "v177", "v178", "v179", "v180", "v181", "v182", "v183", "v184", "v185", "v186", "v187", "v188", "v189", "v190", "v191", "v192", "v193", "v194", "v195", "v196", "v197", "v198", "v199", "v200", "v201", "v202", "v203", "v204", "v205", "v206", "v207", "v208", "v209", "v210", "v211", "v212", "v213", "v214", "v215", "v216", "v217", "v218", "v219", "v220", "v221", "v222", "v223", "v224", "v225", "v226", "v227", "v228", "v229", "v230", "v231", "v232", "v233", "v234", "v235", "v236", "v237", "v238", "v239", "v240", "v241", "v242", "v243", "v244", "v245", "v246", "v247", "v248", "v249", "v250", "v251", "v252", "v253", "v254", "v255", "a0", "a1", "a2", "a3", "a4", "a5", "a6", "a7", "a8", "a9", "a10", "a11", "a12", "a13", "a14", "a15", "a16", "a17", "a18", "a19", "a20", "a21", "a22", "a23", "a24", "a25", "a26", "a27", "a28", "a29", "a30", "a31", "a32", "a33", "a34", "a35", "a36", "a37", "a38", "a39", "a40", "a41", "a42", "a43", "a44", "a45", "a46", "a47", "a48", "a49", "a50", "a51", "a52", "a53", "a54", "a55", "a56", "a57", "a58", "a59", "a60", "a61", "a62", "a63", "a64", "a65", "a66", "a67", "a68", "a69", "a70", "a71", "a72", "a73", "a74", "a75", "a76", "a77", "a78", "a79", "a80", "a81", "a82", "a83", "a84", "a85", "a86", "a87", "a88", "a89", "a90", "a91", "a92", "a93", "a94", "a95", "a96", "a97", "a98", "a99", "a100", "a101", "a102", "a103", "a104", "a105", "a106", "a107", "a108", "a109", "a110", "a111", "a112", "a113", "a114", "a115", "a116", "a117", "a118", "a119", "a120", "a121", "a122", "a123", "a124", "a125", "a126", "a127", "a128", "a129", "a130", "a131", "a132", "a133", "a134", "a135", "a136", "a137", "a138", "a139", "a140", "a141", "a142", "a143", "a144", "a145", "a146", "a147", "a148", "a149", "a150", "a151", "a152", "a153", "a154", "a155", "a156", "a157", "a158", "a159", "a160", "a161", "a162", "a163", "a164", "a165", "a166", "a167", "a168", "a169", "a170", "a171", "a172", "a173", "a174", "a175", "a176", "a177", "a178", "a179", "a180", "a181", "a182", "a183", "a184", "a185", "a186", "a187", "a188", "a189", "a190", "a191", "a192", "a193", "a194", "a195", "a196", "a197", "a198", "a199", "a200", "a201", "a202", "a203", "a204", "a205", "a206", "a207", "a208", "a209", "a210", "a211", "a212", "a213", "a214", "a215", "a216", "a217", "a218", "a219", "a220", "a221", "a222", "a223", "a224", "a225", "a226", "a227", "a228", "a229", "a230", "a231", "a232", "a233", "a234", "a235", "a236", "a237", "a238", "a239", "a240", "a241", "a242", "a243", "a244", "a245", "a246", "a247", "a248", "a249", "a250", "a251", "a252", "a253", "a254", "a255", "s7", "s8", "s9", "s10", "s11", "s12", "s13", "s14", "s15", "s16", "s17", "s18", "s19", "s20", "s21", "s22", "s23", "s24", "s25", "s26", "s27", "s28", "s29", "s30", "s31", "s36", "s37", "s38", "s39", "s40", "s41", "s42", "s43", "s44", "s45", "s46", "s47", "s48", "s49", "s50", "s51", "s52", "s53", "s54", "s55", "s56", "s57", "s58", "s59", "s60", "s61", "s62", "s63", "s64", "s65", "s66", "s67", "s68", "s69", "s70", "s71", "s72", "s73", "s74", "s75", "s76", "s77", "s78", "s79", "s80", "s81", "s82", "s83", "s84", "s85", "s86", "s87", "s88", "s89", "s90", "s91", "s92", "s93", "s94", "s95", "s96", "s97", "s98", "s99", "vcc", "scc", "memory"
constexpr int L_BYTES = 160256;


__global__ __launch_bounds__(256, 1) void mega(const void* x, const void* wq, const void* wk, const void* wv, const void* wo, const void* w1,
                                               const void* w2, const void* c, const void* wmod, void* ws, void* out) {
    __shared__ __attribute__((aligned(16))) char lds[L_BYTES];
    asm volatile(KBODY
                 :
                 : "{s[4:5]}"(__builtin_amdgcn_kernarg_segment_ptr()), "{s6}"(blockIdx.x), "{v0}"(threadIdx.x), "{s3}"((unsigned)(uintptr_t)lds)
                 : KCLOB);
}

extern "C" void kernel_launch(void* const* d_in, const int* in_sizes, int n_in, void* d_out, int out_size, void* d_ws,
                              size_t ws_size, hipStream_t stream) {
    mega<<<256, 256, 0, stream>>>(d_in[0], d_in[1], d_in[2], d_in[3], d_in[4], d_in[6], d_in[7], d_in[8], d_in[9], d_ws, d_out);
}
```
